```python
import torch

def _softmax(x, axis=-1):
    return torch.softmax(x, dim=axis)
def _gelu(x):
    return torch.nn.functional.gelu(x, approximate="tanh")
def _silu(x):
    return torch.nn.functional.silu(x)
def _sigmoid(x):
    return torch.sigmoid(x)
def _layernorm(x, axis=-1, eps=1e-5):
    m = x.mean(dim=axis, keepdim=True)
    v = x.var(dim=axis, keepdim=True, unbiased=False)
    return (x - m) / torch.sqrt(v + eps)
def _scatter_add(tgt, idx, src, axis=0):
    return tgt.clone().index_add_(axis, idx, src)
def _segment_sum(src, seg, n):
    out = torch.zeros((n,) + src.shape[1:], dtype=src.dtype)
    return out.index_add_(0, seg, src)
def _pad(x, pw):
    flat = tuple(v for pair in reversed(pw) for v in pair)
    return torch.nn.functional.pad(x, flat)

kernel_name = 'interleave_layers__mha_block__dense_activation_gelu__prompt'

def setup_inputs():

    _w = (lambda a: (lambda t: t.to(torch.bfloat16) if t.is_floating_point() else t)(torch.as_tensor(a)))
    def _A_setup():
        B, S, D, H = 1, 2048, 448, 16
        return {
            "x": _w((lambda *s: torch.randn(*s, dtype=torch.bfloat16) * 0.05)(B, S, D)),
            "wqkv": _w((lambda *s: torch.randn(*s, dtype=torch.bfloat16) * 0.05)(D, 3 * D)),
            "wo": _w((lambda *s: torch.randn(*s, dtype=torch.bfloat16) * 0.05)(D, D)),
        }
    _A_in = _A_setup()

    def _B_setup():
        B, S, D, F = 1, 2048, 448, 1792
        return {
            "x": _w((lambda *s: torch.randn(*s, dtype=torch.bfloat16) * 0.05)(B, S, D)),
            "w1": _w((lambda *s: torch.randn(*s, dtype=torch.bfloat16) * 0.8)(D, F) * (D ** -0.5)),
            "b1": _w((lambda *s: torch.randn(*s, dtype=torch.bfloat16) * 12.8)(F) * 0.01),
            "w2": _w((lambda *s: torch.randn(*s, dtype=torch.bfloat16) * 0.2)(F, D) * (F ** -0.5)),
            "b2": _w((lambda *s: torch.randn(*s, dtype=torch.bfloat16) * 1.6)(D) * 0.01),
        }
    _B_in = _B_setup()

    return {
        "A_x": _A_in["x"],
        "A_wqkv": _A_in["wqkv"],
        "A_wo": _A_in["wo"],
        "B_x": _B_in["x"],
        "B_w1": _B_in["w1"],
        "B_b1": _B_in["b1"],
        "B_w2": _B_in["w2"],
        "B_b2": _B_in["b2"],
    }

def reference(A_x, A_wqkv, A_wo, B_x, B_w1, B_b1, B_w2, B_b2):
    def _A_ref(x, wqkv, wo):
        B, S, D = x.shape
        H = 16
        Dh = D // H
        qkv = (lambda x, *d: x.permute(*d))((x @ wqkv).reshape(B, S, 3, H, Dh), 2, 0, 3, 1, 4)
        q, k, v = qkv[0], qkv[1], qkv[2]
        scores = torch.einsum("bhqd,bhkd->bhqk", q, k) * (Dh ** -0.5)
        p = _softmax(scores, axis=-1)
        o = torch.einsum("bhqk,bhkd->bhqd", p, v)
        return (lambda x, *d: x.permute(*d))(o, 0, 2, 1, 3).reshape(B, S, D) @ wo

    def _B_ref(x, w1, b1, w2, b2):
        h = torch.einsum("bsd,df->bsf", x, w1) + b1
        h = _gelu(h)
        return torch.einsum("bsf,fd->bsd", h, w2) + b2

    x = A_x
    x = x + _A_ref(_layernorm(x, axis=-1), A_wqkv, A_wo)
    x = x + _B_ref(_layernorm(x, axis=-1), B_w1, B_b1, B_w2, B_b2)
    x = x + _A_ref(_layernorm(x, axis=-1), A_wqkv, A_wo)
    x = x + _B_ref(_layernorm(x, axis=-1), B_w1, B_b1, B_w2, B_b2)
    return x
```

```cpp
#include <hip/hip_runtime.h>
#include <hip/hip_bf16.h>

#define DI __device__ __forceinline__
typedef short bf16x8 __attribute__((ext_vector_type(8)));
typedef float f32x16 __attribute__((ext_vector_type(16)));
typedef float f32x4 __attribute__((ext_vector_type(4)));
typedef float f32x2 __attribute__((ext_vector_type(2)));
typedef unsigned u32x4 __attribute__((ext_vector_type(4)));
typedef unsigned u32x2 __attribute__((ext_vector_type(2)));
typedef __bf16 bf16x2_t __attribute__((ext_vector_type(2)));
typedef unsigned short u16;
#define LAS __attribute__((address_space(3)))
#define GAS __attribute__((address_space(1)))
#define RLX_AGENT __ATOMIC_RELAXED, __HIP_MEMORY_SCOPE_AGENT
#define MFMA(a, b, c) __builtin_amdgcn_mfma_f32_32x32x16_bf16(__builtin_bit_cast(bf16x8, a), __builtin_bit_cast(bf16x8, b), c, 0, 0, 0)
#define SBAR() __builtin_amdgcn_sched_barrier(0)
#define PIN(x) asm volatile("" : "+v"(x))

constexpr int S = 2048, D = 448, F = 1792;
constexpr int KB = 1024;
constexpr int FLAG_BYTES = 128 * KB;
constexpr int F_LAYER = 40 * KB;
constexpr int F_KV = 0, F_O = 4 * KB, F_X1 = 12 * KB, F_H = 20 * KB, F_X2 = 28 * KB;
constexpr int WQ_J = 87 * KB, W1_J = 102 * KB, WO_J = 32 * KB, W2_J = 56 * KB;
constexpr int WS_WQ = FLAG_BYTES;
constexpr int WS_W1 = WS_WQ + 16 * WQ_J;
constexpr int WS_WO = WS_W1 + 16 * W1_J;
constexpr int WS_W2 = WS_WO + 14 * WO_J;
constexpr int XB = 64 * 28 * KB, STB = 64 * 14 * 256;
constexpr int WS_X = WS_W2 + 28 * W2_J;
constexpr int WS_X1 = WS_X + 2 * XB;
constexpr int WS_ST = WS_X1 + 2 * XB;
constexpr int WS_ST1 = WS_ST + 2 * STB;
constexpr int KVB = 16 * 64 * 4 * KB, OB = 64 * 32 * KB, HB = 64 * 112 * KB;
constexpr int WS_KV = WS_ST1 + 2 * STB;
constexpr int WS_O = WS_KV + 2 * KVB;
constexpr int WS_H = WS_O + 2 * OB;
constexpr int WS_END = WS_H + 2 * HB;
constexpr int WS_DBG = WS_END;
constexpr int L_W = 0;
constexpr int L_WO = W1_J;
constexpr int L_SC = L_WO + WO_J;
constexpr int L_Q = L_SC, L_KV = L_SC + 8 * KB, L_RS = L_SC + 24 * KB, L_MISC = L_RS + 512;
constexpr int L_RED = WQ_J;
constexpr int LDS_BYTES = 160 * KB;
static_assert(L_MISC + 1024 <= LDS_BYTES && L_RED + 48 * KB <= L_RS, "lds map");

DI unsigned cvtpk(float lo, float hi) { f32x2 v = {lo, hi}; return __builtin_bit_cast(unsigned, __builtin_convertvector(v, bf16x2_t)); }
DI float bflo(unsigned u) { return __uint_as_float(u << 16); }
DI float bfhi(unsigned u) { return __uint_as_float(u & 0xffff0000u); }
DI float xhalf(float v) {
    auto r = __builtin_amdgcn_permlane32_swap(__float_as_uint(v), __float_as_uint(v), false, false);
    return __uint_as_float((threadIdx.x & 32) ? r[0] : r[1]);
}
DI float xsum(float v) { auto r = __builtin_amdgcn_permlane32_swap(__float_as_uint(v), __float_as_uint(v), false, false); return __uint_as_float(r[0]) + __uint_as_float(r[1]); }
DI float xmax(float v) { auto r = __builtin_amdgcn_permlane32_swap(__float_as_uint(v), __float_as_uint(v), false, false); return __builtin_fmaxf(__uint_as_float(r[0]), __uint_as_float(r[1])); }

DI int kperm(int hh, int j) { return 8 * (j >> 2) + 4 * hh + (j & 3); }

__global__ __launch_bounds__(256) void prep_kernel(const u16* __restrict__ x, const u16* __restrict__ wqkv, const u16* __restrict__ wo,
                                                   const u16* __restrict__ w1, const u16* __restrict__ b1, const u16* __restrict__ w2,
                                                   unsigned char* __restrict__ ws) {
    __shared__ float red[4][64];
    const int u = blockIdx.x, tid = threadIdx.x, lane = tid & 63, w = tid >> 6, r = lane & 31, hh = lane >> 5;
    ((u32x2*)ws)[u * 64 + (tid & 63)] = (u32x2){0u, 0u};
    if (u < 182) {
        const u16* src; int ld, nblk = 7, dst, col; bool valid = true, fix = false, half = false, wo_map = false; float bias = 0.f; int krow0 = 0;
        if (u < 48) { const int h = u / 3, t = u % 3; src = wqkv; ld = 3 * D; col = t * D + 28 * h + r; valid = r < 28; dst = WS_WQ + h * WQ_J + t * 29 * KB; fix = true; }
        else if (u < 112) { const int j = (u - 48) >> 2, t = (u - 48) & 3; src = w1; ld = F; half = (t == 3); col = 112 * j + 32 * t + (half ? (r & 15) : r); dst = WS_W1 + j * W1_J + t * 29 * KB; fix = true;
                            bias = __uint_as_float((unsigned)b1[col] << 16); }
        else if (u < 126) { const int j = u - 112; src = wo; ld = D; col = 32 * j + r; dst = WS_WO + j * WO_J; nblk = 8; wo_map = true; }
        else { const int v = u - 126, fp = v >> 3, kq = (v >> 1) & 3, f = v & 1; src = w2; ld = D; col = 64 * fp + 32 * f + r; krow0 = 448 * kq; dst = WS_W2 + (fp * 4 + kq) * W2_J + f * 28 * KB; }
        if (!valid) col = 0;
        float cs = 0.f;
#pragma unroll
        for (int i = 0; i < 8; ++i) {
            if (i < nblk) {
                const int ks = w * nblk + i;
                unsigned v[8];
#pragma unroll
                for (int j = 0; j < 8; ++j) {
                    int k = krow0 + 16 * ks + kperm(hh, j); bool ok = valid;
                    if (wo_map) { const int head = ks >> 1, d = 16 * (ks & 1) + kperm(hh, j); ok = d < 28; k = ok ? 28 * head + d : 0; }
                    const unsigned t = src[(size_t)k * ld + col];
                    v[j] = ok ? t : 0u;
                    cs += __uint_as_float(v[j] << 16);
                }
                const u32x4 o = {v[0] | (v[1] << 16), v[2] | (v[3] << 16), v[4] | (v[5] << 16), v[6] | (v[7] << 16)};
                if (!half) *(u32x4*)(ws + dst + ks * KB + lane * 16) = o;
                else if (r < 16) *(u32x4*)(ws + dst + ks * 512 + (r + 16 * hh) * 16) = o;
            }
        }
        if (fix) {
            red[w][lane] = cs;
            __syncthreads();
            if (w == 0) {
                float c = 0.f;
#pragma unroll
                for (int i = 0; i < 4; ++i) c += red[i][r] + red[i][r + 32];
                const unsigned hi = cvtpk(c, 0.f) & 0xffffu; const unsigned lo = cvtpk(c - bflo(hi), 0.f) & 0xffffu; const unsigned bb = __float_as_uint(bias) >> 16;
                u32x4 o = {hi | (hi << 16), lo | (bb << 16), bb, 0u};
                if (hh) o = (u32x4){0u, 0u, 0u, 0u};
                if (!half) *(u32x4*)(ws + dst + 28 * KB + lane * 16) = o;
                else if (r < 16) *(u32x4*)(ws + dst + 28 * 512 + (r + 16 * hh) * 16) = o;
            }
        }
    } else if (u < 246) {
        const int tt = u - 182;
        const u16* row = x + (size_t)(32 * tt + r) * D;
        float s = 0.f, q = 0.f;
#pragma unroll
        for (int i = 0; i < 7; ++i) {
            const int ks = w * 7 + i;
            const u32x2 a = *(const u32x2*)(row + 16 * ks + 4 * hh), b = *(const u32x2*)(row + 16 * ks + 8 + 4 * hh);
            const u32x4 o = {a[0], a[1], b[0], b[1]};
#pragma unroll
            for (int j = 0; j < 4; ++j) { const float lo = bflo(o[j]), hi = bfhi(o[j]); s += lo + hi; q += lo * lo + hi * hi; }
            *(u32x4*)(ws + WS_X + (tt * 28 + ks) * KB + lane * 16) = o;
        }
        s = xsum(s); q = xsum(q);
        f32x2* st = (f32x2*)(ws + WS_ST + tt * 14 * 256);
        if (hh == 0) st[w * 32 + r] = (f32x2){s, q};
        for (int i = 128 + tid; i < 14 * 32; i += 256) st[i] = (f32x2){0.f, 0.f};
    }
}

typedef __amdgpu_buffer_rsrc_t rsrc_t;
constexpr int SC1 = 16;
template <int AUX> DI u32x4 ldb(rsrc_t rs, int voff, int soff, int blk) {
    return __builtin_amdgcn_raw_buffer_load_b128(rs, voff + (blk & 3) * KB, soff + (blk >> 2) * 4 * KB, AUX);
}
DI void stb(rsrc_t rs, u32x4 v, int voff, int soff) { __builtin_amdgcn_raw_buffer_store_b128(v, rs, voff, soff, SC1); }
DI u32x4 lds16(const LAS unsigned char* p) { return *(const LAS u32x4*)p; }
#define DRAIN() asm volatile("s_waitcnt vmcnt(0)" ::: "memory")
#define LDSBAR() asm volatile("s_waitcnt lgkmcnt(0)\n\ts_barrier" ::: "memory")
DI void dma1k(rsrc_t rs, int voff, int soff, int ldst) {
    unsigned keep;
    asm volatile("s_mov_b32 %0, m0\n\ts_mov_b32 m0, %4\n\ts_nop 0\n\tbuffer_load_dwordx4 %1, %2, %3 offen lds\n\ts_mov_b32 m0, %0"
                 : "=&s"(keep) : "v"(voff), "s"(rs), "s"(soff), "s"(ldst) : "memory");
}
DI void publish(unsigned char* ws, int off) {
    DRAIN();
    if ((threadIdx.x & 63) == 0) __hip_atomic_store((GAS unsigned*)(ws + off), 1u, RLX_AGENT);
}
DI void waitflags(unsigned char* ws, int off_lane, bool active) {
    GAS unsigned* f = (GAS unsigned*)(ws + off_lane);
    for (unsigned spin = 0; spin < (1u << 20); ++spin) {
        const unsigned v = active ? __hip_atomic_load(f, RLX_AGENT) : 1u;
        if (__all(v == 1u)) break;
    }
    __builtin_amdgcn_fence(__ATOMIC_ACQUIRE, "wavefront");
    asm volatile("" ::: "memory");
}
DI u32x4 pack8(const f32x16& a, int s) { return (u32x4){cvtpk(a[8 * s], a[8 * s + 1]), cvtpk(a[8 * s + 2], a[8 * s + 3]), cvtpk(a[8 * s + 4], a[8 * s + 5]), cvtpk(a[8 * s + 6], a[8 * s + 7])}; }

struct Stats { float rstd, sd; u32x4 fix; };
DI void load_stats(rsrc_t rs, int soff, int lane, u32x2 (&st)[7]) {
#pragma unroll
    for (int i = 0; i < 7; ++i) st[i] = __builtin_amdgcn_raw_buffer_load_b64(rs, ((lane >> 5) * 7 + i) * 256 + (lane & 31) * 8, soff, SC1);
}
DI Stats make_stats(const u32x2 (&st)[7], int hh) {
    float s = 0.f, q = 0.f;
#pragma unroll
    for (int i = 0; i < 7; ++i) { s += __uint_as_float(st[i][0]); q += __uint_as_float(st[i][1]); }
    s = xsum(s); q = xsum(q);
    const float mean = s * (1.f / D), var = __builtin_fmaxf(q * (1.f / D) - mean * mean, 0.f) + 1e-5f;
    Stats o; o.rstd = __builtin_amdgcn_rsqf(var); o.sd = var * o.rstd;
    const unsigned mh = cvtpk(-mean, 0.f) & 0xffffu, ml = cvtpk(-mean - bflo(mh), 0.f) & 0xffffu;
    const unsigned sh = cvtpk(o.sd, 0.f) & 0xffffu, sl = cvtpk(o.sd - bflo(sh), 0.f) & 0xffffu;
    o.fix = (u32x4){mh | (ml << 16), mh | (sh << 16), sl, 0u};
    if (hh) o.fix = (u32x4){0u, 0u, 0u, 0u};
    return o;
}
DI void store_x(rsrc_t rs, const f32x16& a, int lane, int xoff, int stoff) {
    float s = 0.f, q = 0.f;
#pragma unroll
    for (int i = 0; i < 16; ++i) { s += a[i]; q += a[i] * a[i]; }
    stb(rs, pack8(a, 0), lane * 16, xoff); stb(rs, pack8(a, 1), lane * 16 + KB, xoff);
    s = xsum(s); q = xsum(q);
    if (lane < 32) __builtin_amdgcn_raw_buffer_store_b64((u32x2){__float_as_uint(s), __float_as_uint(q)}, rs, lane * 8, stoff, SC1);
}

#define STAMP(i)

__global__ __launch_bounds__(256, 1) void main_kernel(const u16* __restrict__ b2, u16* __restrict__ out, unsigned char* __restrict__ ws) {
    extern __shared__ __attribute__((aligned(16))) unsigned char smem[];
    LAS unsigned char* L = (LAS unsigned char*)smem;
    const int lane = threadIdx.x & 63, w = __builtin_amdgcn_readfirstlane(threadIdx.x >> 6), r = lane & 31, hh = lane >> 5, l16 = lane * 16;
    const int p = 2 * (blockIdx.x & 7) + (blockIdx.x >> 7), j = (blockIdx.x >> 3) & 15;
    const int tt = 4 * p + w;
    const rsrc_t rs = __builtin_amdgcn_make_buffer_rsrc(ws, 0, WS_END, 0x00020000);
    const f32x16 zero = {};
    const bool act = j < 14;
    const int half2 = j >= 7 ? 1 : 0, fp = act ? j - 7 * half2 : 0;

    for (int b = w; b < 87; b += 4) dma1k(rs, l16, WS_WQ + j * WQ_J + b * KB, L_W + b * KB);
    u32x4 w2[56];

#pragma unroll 1
    for (int layer = 0; layer < 2; ++layer) {
        const int fl = layer * F_LAYER;
        STAMP(0);
        u32x4 qf[2][2], kv[4][4];
        {
            if (layer) waitflags(ws, fl - F_LAYER + F_X2 + tt * 128 + lane * 4, lane < 14);
            STAMP(1);
            u32x2 st[7]; load_stats(rs, WS_ST + layer * STB + tt * 14 * 256, lane, st);
            u32x4 xf[28];
#pragma unroll
            for (int i = 0; i < 28; ++i) xf[i] = ldb<SC1>(rs, l16, WS_X + layer * XB + tt * 28 * KB, i);
            if (layer == 0) {
                asm volatile("s_waitcnt vmcnt(35)\n\ts_barrier" ::: "memory");
#pragma unroll
                for (int i = 0; i < 56; ++i) w2[i] = ldb<0>(rs, l16, WS_W2 + (fp * 4 + w) * W2_J, i);
            }
            f32x16 aq = zero, ak = zero, av = zero;
            const LAS unsigned char* wp = L + L_W + l16;
#pragma unroll
            for (int i = 0; i < 28; ++i) {
                aq = MFMA(lds16(wp + i * KB), xf[i], aq);
                ak = MFMA(lds16(wp + (29 + i) * KB), xf[i], ak);
                av = MFMA(xf[i], lds16(wp + (58 + i) * KB), av);
            }
            const Stats sx = make_stats(st, hh);
            *(LAS float*)(L + L_RS + w * 128 + r * 4) = sx.rstd;
            aq = MFMA(lds16(wp + 28 * KB), sx.fix, aq);
            ak = MFMA(lds16(wp + 57 * KB), sx.fix, ak);
            av = MFMA(sx.fix, lds16(wp + 86 * KB), av);
            const float qs = sx.rstd * (0.18898223650461362f * 1.4426950408889634f);
#pragma unroll
            for (int g = 0; g < 4; ++g) {
                const f32x4 rt = *(const LAS f32x4*)(L + L_RS + w * 128 + (8 * g + 4 * hh) * 4);
#pragma unroll
                for (int i = 0; i < 4; ++i) { ak[4 * g + i] *= sx.rstd; av[4 * g + i] *= rt[i]; aq[4 * g + i] *= qs; }
            }
            u32x4 k0 = pack8(ak, 0), k1 = pack8(ak, 1), v0 = pack8(av, 0), v1 = pack8(av, 1);
            if (hh) k1[2] = 0x00003F80u;
            if (r == 28) { v0 = (u32x4){0x3F803F80u, 0x3F803F80u, 0x3F803F80u, 0x3F803F80u}; v1 = v0; }
            const int kvo = WS_KV + layer * KVB + (j * 64 + tt) * 4 * KB;
            stb(rs, k0, l16, kvo); stb(rs, k1, l16 + KB, kvo); stb(rs, v0, l16 + 2 * KB, kvo); stb(rs, v1, l16 + 3 * KB, kvo);
            LAS unsigned char* kq = L + L_KV + w * 4 * KB + l16;
            *(LAS u32x4*)(kq) = k0; *(LAS u32x4*)(kq + KB) = k1; *(LAS u32x4*)(kq + 2 * KB) = v0; *(LAS u32x4*)(kq + 3 * KB) = v1;
            *(LAS u32x4*)(L + L_Q + w * 2 * KB + l16) = pack8(aq, 0); *(LAS u32x4*)(L + L_Q + w * 2 * KB + KB + l16) = pack8(aq, 1);
            publish(ws, fl + F_KV + ((j * 2 + (w & 1)) * 32 + p * 2 + (w >> 1)) * 4);
        }
        STAMP(2);
        {
            const int qh = w >> 1, kh = w & 1;
            LDSBAR();
#pragma unroll
            for (int b = 0; b < 2; ++b)
#pragma unroll
                for (int s = 0; s < 2; ++s) qf[b][s] = lds16(L + L_Q + ((2 * qh + b) * 2 + s) * KB + l16);
#pragma unroll
            for (int t = 0; t < 2; ++t)
#pragma unroll
                for (int c = 0; c < 4; ++c) kv[t][c] = lds16(L + L_KV + ((kh + 2 * t) * 4 + c) * KB + l16);
            LDSBAR();
            float mref[2];
#pragma unroll
            for (int b = 0; b < 2; ++b) {
                f32x16 s = MFMA(kv[0][0], qf[b][0], zero); s = MFMA(kv[0][1], qf[b][1], s);
                float m = s[0];
#pragma unroll
                for (int i = 1; i < 16; ++i) m = __builtin_fmaxf(m, s[i]);
                m = xmax(m);
                const unsigned nm = cvtpk(-m, 0.f) & 0xffffu;
                mref[b] = -bflo(nm);
                if (hh) qf[b][1][2] = nm;
            }
            const int kvb = WS_KV + layer * KVB + (j * 64 + kh) * 4 * KB;
#define KVOFF(t) (kvb + ((((p + ((t) >> 1)) & 15) * 4 + 2 * ((t) & 1)) * 4 * KB))
            f32x16 oA = zero, oB = zero, sA, sB;
            u32x4 pa0, pa1;
            sA = MFMA(kv[0][0], qf[0][0], zero); sA = MFMA(kv[0][1], qf[0][1], sA);
#define EX4(X, i, P, o) do { X[i] = __builtin_amdgcn_exp2f(X[i]); X[i + 1] = __builtin_amdgcn_exp2f(X[i + 1]); X[i + 2] = __builtin_amdgcn_exp2f(X[i + 2]); X[i + 3] = __builtin_amdgcn_exp2f(X[i + 3]); \
                             P[o] = cvtpk(X[i], X[i + 1]); P[o + 1] = cvtpk(X[i + 2], X[i + 3]); PIN(P); SBAR(); } while (0)
#define STEP(u, un, t, LD, NEXT) do { \
            sB = MFMA(kv[u][0], qf[1][0], zero); EX4(sA, 0, pa0, 0); \
            sB = MFMA(kv[u][1], qf[1][1], sB); \
            if (LD) { const int so = KVOFF((t) + 4); kv[u][0] = ldb<SC1>(rs, l16, so, 0); kv[u][1] = ldb<SC1>(rs, l16, so, 1); } \
            EX4(sA, 4, pa0, 2); EX4(sA, 8, pa1, 0); \
            oA = MFMA(kv[u][2], pa0, oA); EX4(sA, 12, pa1, 2); EX4(sB, 0, pa0, 0); \
            oA = MFMA(kv[u][3], pa1, oA); EX4(sB, 4, pa0, 2); \
            if (NEXT) sA = MFMA(kv[un][0], qf[0][0], zero); \
            EX4(sB, 8, pa1, 0); \
            oB = MFMA(kv[u][2], pa0, oB); \
            if (NEXT) sA = MFMA(kv[un][1], qf[0][1], sA); \
            EX4(sB, 12, pa1, 2); \
            oB = MFMA(kv[u][3], pa1, oB); \
            if (LD) { const int so = KVOFF((t) + 4); kv[u][2] = ldb<SC1>(rs, l16, so, 2); kv[u][3] = ldb<SC1>(rs, l16, so, 3); } \
            SBAR(); } while (0)
#define WDMA(t) do { const int b_ = w + 4 * (t); if (b_ < 134) dma1k(rs, l16, b_ < 102 ? WS_W1 + j * W1_J + b_ * KB : WS_WO + (act ? j : 0) * WO_J + (b_ - 102) * KB, L_W + b_ * KB); } while (0)
            STEP(0, 1, 0, false, true);
            waitflags(ws, fl + F_KV + (j * 2 + kh) * 128 + r * 4, true);
            STAMP(3);
#pragma unroll
            for (int c = 0; c < 4; ++c) kv[2][c] = ldb<SC1>(rs, l16, KVOFF(2), c);
#pragma unroll
            for (int c = 0; c < 4; ++c) kv[3][c] = ldb<SC1>(rs, l16, KVOFF(3), c);
#pragma unroll
            for (int c = 0; c < 4; ++c) kv[0][c] = ldb<SC1>(rs, l16, KVOFF(4), c);
            SBAR();
#pragma unroll 1
            for (int t = 1; t < 25; t += 4) {
                STEP(1, 2, t, true, true); WDMA(t - 1);
                STEP(2, 3, t + 1, true, true); WDMA(t);
                STEP(3, 0, t + 2, true, true); WDMA(t + 1);
                STEP(0, 1, t + 3, true, true); WDMA(t + 2);
            }
            STEP(1, 2, 25, true, true); WDMA(24); WDMA(25);
            STEP(2, 3, 26, true, true); WDMA(26); WDMA(27);
            STEP(3, 0, 27, true, true); WDMA(28); WDMA(29);
            STEP(0, 1, 28, false, true); WDMA(30); WDMA(31);
            STEP(1, 2, 29, false, true); WDMA(32); WDMA(33);
            STEP(2, 3, 30, false, true);
            STEP(3, 0, 31, false, false);
            STAMP(4);
            LAS unsigned* bad = (LAS unsigned*)(L + L_MISC);
            const bool unsafe = hh && !(oA[12] < 1.0995e12f && oB[12] < 1.0995e12f);
            bad[w] = __any(unsafe) ? 1u : 0u;
            DRAIN(); LDSBAR();
            if (__builtin_expect(__builtin_amdgcn_readfirstlane(bad[0] | bad[1] | bad[2] | bad[3]) != 0, 0)) {
                if (hh) { qf[0][1][2] = 0u; qf[1][1][2] = 0u; }
                oA = zero; oB = zero; mref[0] = -INFINITY; mref[1] = -INFINITY;
#pragma unroll 1
                for (int t = 0; t < 32; ++t) {
                    const int so = KVOFF(t);
                    const u32x4 k0 = ldb<SC1>(rs, l16, so, 0), k1 = ldb<SC1>(rs, l16, so, 1), v0 = ldb<SC1>(rs, l16, so, 2), v1 = ldb<SC1>(rs, l16, so, 3);
#pragma unroll
                    for (int b = 0; b < 2; ++b) {
                        f32x16 s = MFMA(k0, qf[b][0], zero); s = MFMA(k1, qf[b][1], s);
                        float m = mref[b];
#pragma unroll
                        for (int i = 0; i < 16; ++i) m = __builtin_fmaxf(m, s[i]);
                        m = xmax(m);
                        const float al = __builtin_amdgcn_exp2f(mref[b] - m); mref[b] = m;
                        f32x16& o = b ? oB : oA;
#pragma unroll
                        for (int i = 0; i < 16; ++i) { s[i] = __builtin_amdgcn_exp2f(s[i] - m); o[i] *= al; }
                        o = MFMA(v0, pack8(s, 0), o); o = MFMA(v1, pack8(s, 1), o);
                    }
                }
            }
            {
                const f32x16 snd = kh ? oA : oB; f32x16 o = kh ? oB : oA; const float ms = kh ? mref[0] : mref[1], mo = kh ? mref[1] : mref[0];
                LAS unsigned char* sp = L + L_KV + w * 4 * KB + l16;
#pragma unroll
                for (int g = 0; g < 4; ++g) *(LAS f32x4*)(sp + g * KB) = (f32x4){snd[4 * g], snd[4 * g + 1], snd[4 * g + 2], snd[4 * g + 3]};
                *(LAS float*)(L + L_Q + w * 256 + lane * 4) = ms;
                LDSBAR();
                const LAS unsigned char* gp = L + L_KV + (w ^ 1) * 4 * KB + l16;
                const float mp = *(const LAS float*)(L + L_Q + (w ^ 1) * 256 + lane * 4);
                const float m = __builtin_fmaxf(mo, mp), fo = __builtin_amdgcn_exp2f(mo - m), fq = __builtin_amdgcn_exp2f(mp - m);
#pragma unroll
                for (int g = 0; g < 4; ++g) { const f32x4 t = *(const LAS f32x4*)(gp + g * KB);
#pragma unroll
                    for (int i = 0; i < 4; ++i) o[4 * g + i] = o[4 * g + i] * fo + t[i] * fq; }
                const float lo = xhalf(o[12]), rl = __builtin_amdgcn_rcpf(hh ? o[12] : lo);
#pragma unroll
                for (int i = 0; i < 16; ++i) o[i] *= rl;
                const int oo = WS_O + layer * OB + (tt * 32 + 2 * j) * KB;
                stb(rs, pack8(o, 0), l16, oo); stb(rs, pack8(o, 1), l16 + KB, oo);
                publish(ws, fl + F_O + tt * 128 + j * 4);
            }
        }
        STAMP(5);
        if (act) {
            const u32x4 x0 = ldb<SC1>(rs, l16, WS_X + layer * XB + (tt * 28 + 2 * j) * KB, 0), x1 = ldb<SC1>(rs, l16, WS_X + layer * XB + (tt * 28 + 2 * j) * KB, 1);
            waitflags(ws, fl + F_O + tt * 128 + (lane & 15) * 4, true);
            STAMP(6);
            u32x4 xf[32];
#pragma unroll
            for (int i = 0; i < 32; ++i) xf[i] = ldb<SC1>(rs, l16, WS_O + layer * OB + tt * 32 * KB, i);
            f32x16 a;
#pragma unroll
            for (int i = 0; i < 4; ++i) { a[2 * i] = bflo(x0[i]); a[2 * i + 1] = bfhi(x0[i]); a[8 + 2 * i] = bflo(x1[i]); a[9 + 2 * i] = bfhi(x1[i]); }
#pragma unroll
            for (int i = 0; i < 32; ++i) a = MFMA(lds16(L + L_WO + i * KB + l16), xf[i], a);
            store_x(rs, a, lane, WS_X1 + layer * XB + (tt * 28 + 2 * j) * KB, WS_ST1 + layer * STB + (tt * 14 + j) * 256);
            publish(ws, fl + F_X1 + tt * 128 + j * 4);
        }
        STAMP(7);
        {
            waitflags(ws, fl + F_X1 + tt * 128 + lane * 4, lane < 14);
            STAMP(8);
            u32x2 st[7]; load_stats(rs, WS_ST1 + layer * STB + tt * 14 * 256, lane, st);
            u32x4 xf[28];
#pragma unroll
            for (int i = 0; i < 28; ++i) xf[i] = ldb<SC1>(rs, l16, WS_X1 + layer * XB + tt * 28 * KB, i);
            const LAS unsigned char* wp = L + L_W + l16;
            const LAS unsigned char* wh = L + L_W + 87 * KB + ((r & 15) + 16 * hh) * 16;
            f32x16 a0 = zero, a3 = zero;
#pragma unroll
            for (int i = 0; i < 28; ++i) { a3 = MFMA(lds16(wh + i * 512), xf[i], a3); a0 = MFMA(lds16(wp + i * KB), xf[i], a0); }
            const Stats sx = make_stats(st, hh);
            a3 = MFMA(lds16(wh + 28 * 512), sx.fix, a3); a0 = MFMA(lds16(wp + 28 * KB), sx.fix, a0);
            const float k1 = -2.302208198f * sx.rstd, k2 = -0.1029432397f * sx.rstd * sx.rstd * sx.rstd;
#define GELU(a, i) a[i] = a[i] * __builtin_amdgcn_rcpf(__builtin_fmaf(__builtin_amdgcn_exp2f(a[i] * __builtin_fmaf(a[i] * a[i], k2, k1)), sx.sd, sx.sd))
            const int ho = WS_H + layer * HB + (tt * 112 + 7 * j) * KB;
            f32x16 a1 = zero, a2 = zero;
#pragma unroll
            for (int i = 0; i < 29; ++i) {
                a1 = MFMA(lds16(wp + (29 + i) * KB), i < 28 ? xf[i < 28 ? i : 0] : sx.fix, a1);
                if (i < 8) GELU(a3, i); else if (i < 24) GELU(a0, i - 8);
                if (i == 8) stb(rs, pack8(a3, 0), l16 + 2 * KB, ho + 4 * KB);
                if (i == 24) { stb(rs, pack8(a0, 0), l16, ho); stb(rs, pack8(a0, 1), l16 + KB, ho); }
            }
#pragma unroll
            for (int i = 0; i < 29; ++i) {
                a2 = MFMA(lds16(wp + (58 + i) * KB), i < 28 ? xf[i < 28 ? i : 0] : sx.fix, a2);
                if (i >= 2 && i < 18) GELU(a1, i - 2);
                if (i == 18) { stb(rs, pack8(a1, 0), l16 + 2 * KB, ho); stb(rs, pack8(a1, 1), l16 + 3 * KB, ho); }
            }
#pragma unroll
            for (int i = 0; i < 16; ++i) GELU(a2, i);
            stb(rs, pack8(a2, 0), l16, ho + 4 * KB); stb(rs, pack8(a2, 1), l16 + KB, ho + 4 * KB);
            publish(ws, fl + F_H + tt * 128 + j * 4);
            LDSBAR();
            if (layer == 0) for (int b = w; b < 87; b += 4) dma1k(rs, l16, WS_WQ + j * WQ_J + b * KB, L_W + b * KB);
        }
        STAMP(9);
        if (act) {
            const int t0 = 4 * p + 2 * half2, tf = t0 + (w >> 1), ft = 2 * fp + (w & 1);
            waitflags(ws, fl + F_H + (t0 + ((lane >> 2) & 1)) * 128 + (4 * w + (lane & 3)) * 4, true);
            STAMP(10);
            const int ho = WS_H + layer * HB + (t0 * 112 + 28 * w) * KB;
            constexpr int WIN = 12;
            u32x4 h0[WIN], h1[WIN];
#pragma unroll
            for (int i = 0; i < WIN; ++i) { h0[i] = ldb<SC1>(rs, l16, ho, i); h1[i] = ldb<SC1>(rs, l16, ho + 112 * KB, i); }
            SBAR();
            f32x16 a00 = zero, a01 = zero, a10 = zero, a11 = zero;
#pragma unroll
            for (int i = 0; i < 28; ++i) {
                const u32x4 c0 = h0[i % WIN], c1 = h1[i % WIN];
                a00 = MFMA(w2[i], c0, a00); a01 = MFMA(w2[28 + i], c0, a01); a10 = MFMA(w2[i], c1, a10); a11 = MFMA(w2[28 + i], c1, a11);
                if (i + WIN < 28) { h0[i % WIN] = ldb<SC1>(rs, l16, ho, i + WIN); h1[i % WIN] = ldb<SC1>(rs, l16, ho + 112 * KB, i + WIN); }
                SBAR();
            }
            const u32x4 x0 = ldb<SC1>(rs, l16, WS_X1 + layer * XB + (tf * 28 + 2 * ft) * KB, 0), x1 = ldb<SC1>(rs, l16, WS_X1 + layer * XB + (tf * 28 + 2 * ft) * KB, 1);
            u32x2 bb[4];
#pragma unroll
            for (int g = 0; g < 4; ++g) bb[g] = *(const u32x2*)(b2 + 32 * ft + 8 * g + 4 * hh);
#define RSLOT(d, s) (L_RED + ((d) * 3 + ((s) > (d) ? (s) - 1 : (s))) * 4 * KB)
#define RSEND(d, A) do { if (w != (d)) { LAS unsigned char* sp = L + RSLOT(d, w) + l16; _Pragma("unroll") for (int g = 0; g < 4; ++g) *(LAS f32x4*)(sp + g * KB) = (f32x4){A[4 * g], A[4 * g + 1], A[4 * g + 2], A[4 * g + 3]}; } else a = A; } while (0)
            f32x16 a = zero;
            RSEND(0, a00); RSEND(1, a01); RSEND(2, a10); RSEND(3, a11);
            DRAIN(); LDSBAR();
#pragma unroll
            for (int s = 0; s < 3; ++s)
#pragma unroll
                for (int g = 0; g < 4; ++g) { const f32x4 t = *(const LAS f32x4*)(L + L_RED + (w * 3 + s) * 4 * KB + g * KB + l16);
#pragma unroll
                    for (int i = 0; i < 4; ++i) a[4 * g + i] += t[i]; }
#pragma unroll
            for (int i = 0; i < 4; ++i) {
                a[2 * i] += bflo(x0[i]); a[2 * i + 1] += bfhi(x0[i]); a[8 + 2 * i] += bflo(x1[i]); a[9 + 2 * i] += bfhi(x1[i]);
                a[4 * i] += bflo(bb[i][0]); a[4 * i + 1] += bfhi(bb[i][0]); a[4 * i + 2] += bflo(bb[i][1]); a[4 * i + 3] += bfhi(bb[i][1]);
            }
            if (layer == 0) {
                store_x(rs, a, lane, WS_X + XB + (tf * 28 + 2 * ft) * KB, WS_ST + STB + (tf * 14 + ft) * 256);
                publish(ws, fl + F_X2 + tf * 128 + ft * 4);
            } else {
                u16* o = out + (size_t)(32 * tf + r) * D + 32 * ft + 4 * hh;
#pragma unroll
                for (int g = 0; g < 4; ++g) *(u32x2*)(o + 8 * g) = (u32x2){cvtpk(a[4 * g], a[4 * g + 1]), cvtpk(a[4 * g + 2], a[4 * g + 3])};
            }
        } else {
            DRAIN(); LDSBAR();
        }
        STAMP(11);
    }
}

extern "C" void kernel_launch(void* const* d_in, const int* in_sizes, int n_in, void* d_out, int out_size, void* d_ws, size_t ws_size, hipStream_t stream) {
    (void)hipFuncSetAttribute((const void*)main_kernel, hipFuncAttributeMaxDynamicSharedMemorySize, LDS_BYTES);
    prep_kernel<<<256, 256, 0, stream>>>((const u16*)d_in[0], (const u16*)d_in[1], (const u16*)d_in[2], (const u16*)d_in[4], (const u16*)d_in[5], (const u16*)d_in[6], (unsigned char*)d_ws);
    main_kernel<<<256, 256, LDS_BYTES, stream>>>((const u16*)d_in[7], (u16*)d_out, (unsigned char*)d_ws);
}
```

```cpp
#include <hip/hip_runtime.h>
#include <hip/hip_bf16.h>

typedef unsigned short u16;

constexpr int LDS_BYTES = 160 * 1024;


__global__ __launch_bounds__(256, 1) void main_kernel(const u16* x, const u16* wqkv, const u16* wo, const u16* w1, const u16* b1, const u16* w2, const u16* b2,
                                                      u16* out, unsigned char* ws) {
    unsigned tid = threadIdx.x;
    asm volatile(
        "s_load_dwordx16 s[64:79], %[ka], 0\n"
        "s_load_dwordx2 s[80:81], %[ka], 64\n"
        "s_mov_b32 s55, %[bid]\n"
        "s_and_b32 s25, %[bid], 6\n"
        "s_lshl_b32 s25, s25, 1\n"
        "s_lshr_b32 s30, %[bid], 6\n"
        "s_add_u32 s25, s25, s30\n"
        "s_bfe_u32 s26, %[bid], 0x30003\n"
        "s_and_b32 s30, %[bid], 1\n"
        "s_lshl_b32 s30, s30, 3\n"
        "s_add_u32 s26, s26, s30\n"
        "v_lshrrev_b32 v16, 6, v0\n"
        "v_and_b32 v0, 63, v0\n"
        "v_readfirstlane_b32 s24, v16\n"
        "v_lshlrev_b32 v1, 4, v0\n"
        "v_lshlrev_b32 v240, 13, v16\n"
        "v_lshl_add_u32 v240, v0, 7, v240\n"
        "v_lshrrev_b32 v20, 3, v0\n"
        "v_mul_u32_u24 v20, 896, v20\n"
        "v_lshrrev_b32 v21, 4, v0\n"
        "v_and_b32 v22, 7, v0\n"
        "v_xor_b32 v21, v21, v22\n"
        "v_lshl_add_u32 v241, v21, 4, v20\n"
        "v_xor_b32 v21, 4, v21\n"
        "v_lshl_add_u32 v242, v21, 4, v20\n"
        "s_lshl_b32 s27, s25, 2\n"
        "s_add_u32 s27, s27, s24\n"
        "s_mul_i32 s31, s27, 28672\n"
        "s_mul_i32 s32, s24, 28672\n"
        "s_mul_i32 s85, s24, 7168\n"
        "s_waitcnt lgkmcnt(0)\n"
        "s_load_dwordx2 s[96:97], s[80:81], 0\n"
        "s_mov_b32 s56, s64\n"
        "s_and_b32 s57, s65, 0xffff\n"
        "s_mov_b32 s58, 1835008\n"
        "s_mov_b32 s59, 0x20000\n"
        "s_mov_b64 s[20:21], s[72:73]\n"
        "s_mov_b64 s[72:73], s[66:67]\n"
        "s_mov_b32 s64, s20\n"
        "s_and_b32 s65, s21, 0xffff\n"
        "s_mov_b32 s66, 0\n"
        "s_mov_b32 s67, 0x20000\n"
        "s_and_b32 s87, s55, 0xe0\n"
        "s_bfe_u32 s35, s55, 0x20003\n"
        "s_or_b32 s87, s87, s35\n"
        "s_and_b32 s35, s55, 7\n"
        "s_lshl_b32 s35, s35, 2\n"
        "s_or_b32 s87, s87, s35\n"
        "s_mov_b32 s89, 1024\n"
        "s_mov_b32 s90, 0\n"
        "s_mov_b32 s93, 0\n"
        "s_mov_b32 s63, 0x20000\n"
        "s_mul_i32 s37, s24, 7\n"
        "s_cmp_lt_u32 s87, 84\n"
        "s_cbranch_scc0 .Lu1\n"
        "s_mul_i32 s35, s87, 0x925\n"
        "s_lshr_b32 s35, s35, 16\n"
        "s_mul_i32 s36, s35, 28\n"
        "s_sub_u32 s36, s87, s36\n"
        "s_add_u32 s35, s35, 1\n"
        "s_cmp_eq_u32 s35, 3\n"
        "s_cselect_b32 s35, 0, s35\n"
        "s_mov_b32 s60, s72\n"
        "s_mov_b32 s61, s73\n"
        "s_mov_b32 s62, 1204224\n"
        "s_mul_i32 s33, s35, 896\n"
        "s_mul_i32 s39, s36, 43008\n"
        "s_add_u32 s33, s33, s39\n"
        "s_mul_i32 s39, s24, 10752\n"
        "s_add_u32 s33, s33, s39\n"
        "s_mul_i32 s94, s35, 29696\n"
        "s_mul_i32 s39, s36, 1024\n"
        "s_add_u32 s94, s94, s39\n"
        "s_mul_i32 s39, s24, 356352\n"
        "s_add_u32 s94, s94, s39\n"
        "s_add_u32 s94, s94, 1574912\n"
        "s_mul_i32 s95, s87, 512\n"
        "s_mul_i32 s39, s24, 128\n"
        "s_add_u32 s95, s95, s39\n"
        "s_add_u32 s95, s95, 1443840\n"
        "s_mov_b32 s88, 1073741824\n"
        "s_mov_b32 s93, 2\n"
        "s_branch .Lu_done\n"
        ".Lu1:\n"
        "s_cmp_lt_u32 s87, 148\n"
        "s_cbranch_scc0 .Lu2\n"
        "s_sub_u32 s36, s87, 84\n"
        "s_lshr_b32 s35, s36, 2\n"
        "s_and_b32 s36, s36, 3\n"
        "s_mov_b32 s60, s70\n"
        "s_mov_b32 s61, s71\n"
        "s_mov_b32 s62, 1605632\n"
        "s_mov_b32 s34, 3584\n"
        "s_mul_i32 s33, s35, 224\n"
        "s_mul_i32 s39, s36, 64\n"
        "s_add_u32 s33, s33, s39\n"
        "s_mul_i32 s88, s35, 137216\n"
        "s_mul_i32 s39, s36, 29696\n"
        "s_add_u32 s88, s88, s39\n"
        "s_add_u32 s88, s88, 3000320\n"
        "s_mov_b32 s93, 1\n"
        "s_mov_b32 s66, 3584\n"
        "s_cmp_eq_u32 s36, 3\n"
        "s_cselect_b32 s89, 512, s89\n"
        "s_branch .Lu_std\n"
        ".Lu2:\n"
        "s_mov_b32 s60, s74\n"
        "s_mov_b32 s61, s75\n"
        "s_mov_b32 s62, 1605632\n"
        "s_mov_b32 s34, 896\n"
        "s_cmp_lt_u32 s87, 176\n"
        "s_cbranch_scc0 .Lu3\n"
        "s_sub_u32 s36, s87, 148\n"
        "s_lshr_b32 s35, s36, 1\n"
        "s_and_b32 s36, s36, 1\n"
        "s_mov_b32 s60, s68\n"
        "s_mov_b32 s61, s69\n"
        "s_mov_b32 s62, 401408\n"
        "s_mul_i32 s33, s35, 64\n"
        "s_mul_i32 s39, s36, 200704\n"
        "s_add_u32 s33, s33, s39\n"
        "s_mul_i32 s39, s24, 50176\n"
        "s_add_u32 s33, s33, s39\n"
        "s_mul_i32 s88, s35, 137216\n"
        "s_mul_i32 s39, s36, 16384\n"
        "s_add_u32 s88, s88, s39\n"
        "s_mul_i32 s39, s24, 4096\n"
        "s_add_u32 s88, s88, s39\n"
        "s_add_u32 s88, s88, 3104768\n"
        "s_mov_b32 s90, 1073741824\n"
        "s_mov_b32 s91, 14336\n"
        "s_mov_b32 s92, 10752\n"
        "s_branch .Lu_done\n"
        ".Lu3:\n"
        "s_sub_u32 s36, s87, 176\n"
        "s_lshr_b32 s35, s36, 1\n"
        "s_and_b32 s36, s36, 1\n"
        "s_lshr_b32 s38, s35, 2\n"
        "s_mul_i32 s33, s38, 128\n"
        "s_mul_i32 s39, s36, 64\n"
        "s_add_u32 s33, s33, s39\n"
        "s_and_b32 s38, s35, 3\n"
        "s_mul_i32 s38, s38, 401408\n"
        "s_add_u32 s33, s33, s38\n"
        "s_mul_i32 s88, s35, 57344\n"
        "s_mul_i32 s39, s36, 28672\n"
        "s_add_u32 s88, s88, s39\n"
        "s_add_u32 s88, s88, 5195776\n"
        "s_cmp_lt_u32 s87, 232\n"
        "s_cselect_b32 s88, s88, 1073741824\n"
        "s_cselect_b32 s62, s62, 0\n"
        ".Lu_std:\n"
        "s_lshl_b32 s91, s34, 4\n"
        "s_mov_b32 s92, s91\n"
        "s_mov_b32 s94, s33\n"
        "s_mul_i32 s38, s37, s91\n"
        "s_add_u32 s33, s33, s38\n"
        "s_mul_i32 s95, s89, 28\n"
        "s_add_u32 s95, s95, s88\n"
        "s_mul_i32 s38, s37, s89\n"
        "s_add_u32 s88, s88, s38\n"
        ".Lu_done:\n"
        "s_and_b32 s61, s61, 0xffff\n"
        "s_cmp_eq_u32 s93, 2\n"
        "s_cbranch_scc1 .Lwq_ld\n"
        "v_lshrrev_b32 v251, 6, v1\n"
        "v_mul_lo_u32 v251, s34, v251\n"
        "v_and_b32 v250, 48, v1\n"
        "v_add_u32 v251, v251, v250\n"
        "s_add_u32 m0, s85, 114688\n"
        "s_nop 0\n"
        "buffer_load_dwordx4 v251, s[60:63], s33 offen lds\n"
        "s_add_u32 s33, s33, s91\n"
        "s_add_u32 m0, s85, 115712\n"
        "s_nop 0\n"
        "buffer_load_dwordx4 v251, s[60:63], s33 offen lds\n"
        "s_add_u32 s33, s33, s92\n"
        "s_add_u32 m0, s85, 116736\n"
        "s_nop 0\n"
        "buffer_load_dwordx4 v251, s[60:63], s33 offen lds\n"
        "s_add_u32 s33, s33, s91\n"
        "s_add_u32 m0, s85, 117760\n"
        "s_nop 0\n"
        "buffer_load_dwordx4 v251, s[60:63], s33 offen lds\n"
        "s_add_u32 s33, s33, s92\n"
        "s_add_u32 m0, s85, 118784\n"
        "s_nop 0\n"
        "buffer_load_dwordx4 v251, s[60:63], s33 offen lds\n"
        "s_add_u32 s33, s33, s91\n"
        "s_add_u32 m0, s85, 119808\n"
        "s_nop 0\n"
        "buffer_load_dwordx4 v251, s[60:63], s33 offen lds\n"
        "s_add_u32 s33, s33, s92\n"
        "s_add_u32 m0, s85, 120832\n"
        "s_nop 0\n"
        "buffer_load_dwordx4 v251, s[60:63], s33 offen lds\n"
        "v_and_b32 v19, 31, v0\n"
        "v_lshlrev_b32 v19, 1, v19\n"
        "buffer_load_ushort v159, v19, s[64:67], s94 offen\n"
        ".Lwq_ld_done:\n"
        "s_add_u32 m0, s32, 0\n"
        "s_add_u32 s52, s31, 0\n"
        "buffer_load_dwordx4 v241, s[56:59], s52 offen lds\n"
        "s_add_u32 m0, s32, 1024\n"
        "s_add_u32 s52, s31, 7168\n"
        "buffer_load_dwordx4 v242, s[56:59], s52 offen lds\n"
        "s_mov_b32 s8, s80\n"
        "s_and_b32 s9, s81, 0xffff\n"
        "s_mov_b32 s10, 42715136\n"
        "s_mov_b32 s11, 0x20000\n"
        "v_and_b32 v16, 31, v0\n"
        "v_lshrrev_b32 v17, 5, v0\n"
        "v_bfe_u32 v20, v0, 4, 1\n"
        "s_add_u32 m0, s32, 2048\n"
        "s_add_u32 s52, s31, 14336\n"
        "buffer_load_dwordx4 v241, s[56:59], s52 offen lds\n"
        "v_lshlrev_b32 v20, 5, v20\n"
        "v_and_b32 v21, 3, v0\n"
        "v_lshl_add_u32 v20, v21, 3, v20\n"
        "v_bfe_u32 v21, v0, 2, 2\n"
        "v_lshl_add_u32 v21, v17, 2, v21\n"
        "v_lshl_add_u32 v243, v21, 10, v20\n"
        "s_mul_i32 s37, s24, 224\n"
        "s_add_u32 m0, s32, 3072\n"
        "s_add_u32 s52, s31, 21504\n"
        "buffer_load_dwordx4 v242, s[56:59], s52 offen lds\n"
        "s_add_u32 s37, s37, 114688\n"
        "v_add_u32 v243, s37, v243\n"
        "v_lshl_add_u32 v245, v21, 6, v20\n"
        "s_add_u32 s37, s85, 114688\n"
        "v_add_u32 v245, s37, v245\n"
        "v_bfe_u32 v20, v0, 1, 3\n"
        "v_xor_b32 v20, v20, v17\n"
        "s_add_u32 m0, s32, 4096\n"
        "s_add_u32 s52, s31, 128\n"
        "buffer_load_dwordx4 v241, s[56:59], s52 offen lds\n"
        "v_lshlrev_b32 v20, 4, v20\n"
        "v_lshl_add_u32 v246, v16, 7, v20\n"
        "v_add_u32 v246, s32, v246\n"
        "v_xor_b32 v247, 32, v246\n"
        "v_xor_b32 v248, 64, v246\n"
        "v_xor_b32 v249, 96, v246\n"
        "v_mov_b32 v252, 0x3f803f80\n"
        "s_add_u32 m0, s32, 5120\n"
        "s_add_u32 s52, s31, 7296\n"
        "buffer_load_dwordx4 v242, s[56:59], s52 offen lds\n"
        "v_mov_b32 v144, 0\n"
        "v_mov_b32 v253, 0x3f803f80\n"
        "v_mov_b32 v145, 0\n"
        "v_mov_b32 v254, 0x3f803f80\n"
        "v_mov_b32 v146, 0\n"
        "v_mov_b32 v255, 0x3f803f80\n"
        "v_mov_b32 v147, 0\n"
        "s_waitcnt lgkmcnt(0)\n"
        "s_add_u32 s96, s96, 1\n"
        "s_addc_u32 s97, s97, 0\n"
        "s_cmp_eq_u32 s93, 2\n"
        "s_cbranch_scc1 .Lwq_tr\n"
        ".Lwq_tr_done:\n"
        "s_add_u32 m0, s32, 6144\n"
        "s_add_u32 s52, s31, 14464\n"
        "buffer_load_dwordx4 v241, s[56:59], s52 offen lds\n"
        "s_mov_b32 s12, s78\n"
        "s_and_b32 s13, s79, 0xffff\n"
        "s_mov_b32 s14, 1835008\n"
        "s_mov_b32 s15, 0x20000\n"
        "s_mov_b32 s28, 0\n"
        "s_mov_b32 s29, 0\n"
        "s_add_u32 s47, s80, 7063551\n"
        "s_and_b32 s47, s47, 0xfffc0000\n"
        "s_sub_u32 s47, s47, s80\n"
        "s_add_u32 m0, s32, 7168\n"
        "s_add_u32 s52, s31, 21632\n"
        "buffer_load_dwordx4 v242, s[56:59], s52 offen lds\n"
        "s_mov_b32 s40, 0\n"
        "s_mov_b32 s41, -1\n"
        "s_mov_b32 s42, 0x10000000\n"
        "s_mov_b32 s43, 0x10000000\n"
        "s_mov_b32 s16, s76\n"
        "s_and_b32 s17, s77, 0xffff\n"
        "s_mov_b32 s18, 896\n"
        "s_mov_b32 s19, 0x20000\n"
        "s_mov_b32 s76, 0xf0000000\n"
        "s_add_u32 m0, s32, 8192\n"
        "s_add_u32 s52, s31, 256\n"
        "buffer_load_dwordx4 v241, s[56:59], s52 offen lds\n"
        "s_mov_b32 s77, s76\n"
        "v_add_u32 v2, 0x10000, v1\n"
        "v_add_u32 v3, 0x20000, v1\n"
        "v_mul_u32_u24 v7, 1792, v17\n"
        "v_lshlrev_b32 v8, 3, v16\n"
        "v_add_u32 v7, v7, v8\n"
        "v_add_u32 v7, 28672, v7\n"
        "v_min_u32 v4, 13, v0\n"
        "v_mul_u32_u24 v4, 128, v4\n"
        "v_and_b32 v5, 15, v0\n"
        "s_add_u32 m0, s32, 9216\n"
        "s_add_u32 s52, s31, 7424\n"
        "buffer_load_dwordx4 v242, s[56:59], s52 offen lds\n"
        "v_mul_u32_u24 v5, 128, v5\n"
        "v_and_b32 v6, 3, v0\n"
        "v_mul_u32_u24 v6, 128, v6\n"
        "v_bfe_u32 v18, v0, 2, 1\n"
        "v_mul_u32_u24 v18, 2048, v18\n"
        "v_add_u32 v6, v6, v18\n"
        "v_and_b32 v9, 15, v0\n"
        "v_lshlrev_b32 v9, 4, v9\n"
        "v_lshl_add_u32 v9, v17, 8, v9\n"
        "s_add_u32 m0, s32, 10240\n"
        "s_add_u32 s52, s31, 14592\n"
        "buffer_load_dwordx4 v241, s[56:59], s52 offen lds\n"
        "s_lshl_b32 s37, s24, 7\n"
        "s_add_u32 s37, s37, 155136\n"
        "v_lshl_add_u32 v10, v16, 2, s37\n"
        "v_lshl_add_u32 v11, v17, 4, s37\n"
        "v_mul_u32_u24 v12, 896, v16\n"
        "v_lshl_add_u32 v12, v17, 4, v12\n"
        "s_and_b32 s37, s26, 1\n"
        "s_lshl_b32 s37, s37, 1\n"
        "s_lshr_b32 s46, s26, 1\n"
        "s_min_u32 s46, s46, 6\n"
        "s_add_u32 m0, s32, 11264\n"
        "s_add_u32 s52, s31, 21760\n"
        "buffer_load_dwordx4 v242, s[56:59], s52 offen lds\n"
        "s_lshl_b32 s45, s25, 2\n"
        "s_add_u32 s45, s45, s37\n"
        "s_lshr_b32 s48, s24, 1\n"
        "s_add_u32 s48, s48, s45\n"
        "s_and_b32 s49, s24, 1\n"
        "s_lshl_b32 s37, s46, 1\n"
        "s_add_u32 s49, s49, s37\n"
        "s_mul_i32 s53, s27, 212992\n"
        "s_mul_i32 s39, s28, 13631488\n"
        "s_add_u32 m0, s32, 12288\n"
        "s_add_u32 s52, s31, 384\n"
        "buffer_load_dwordx4 v241, s[56:59], s52 offen lds\n"
        "s_add_u32 s53, s53, s39\n"
        "s_add_u32 s53, s53, 15452160\n"
        "s_mul_i32 s54, s48, 212992\n"
        "s_mul_i32 s39, s28, 13631488\n"
        "s_add_u32 s54, s54, s39\n"
        "s_add_u32 s54, s54, 15452160\n"
        "v_lshrrev_b32 v18, 2, v0\n"
        "s_lshl_b32 s37, s46, 3\n"
        "s_lshl_b32 s38, s24, 1\n"
        "s_add_u32 s37, s37, s38\n"
        "s_cmp_eq_u32 s93, 2\n"
        "s_cbranch_scc1 .Lwq_pub\n"
        ".Lwq_pub_done:\n"
        "s_add_u32 m0, s32, 13312\n"
        "s_add_u32 s52, s31, 7552\n"
        "buffer_load_dwordx4 v242, s[56:59], s52 offen lds\n"
        "s_add_u32 s37, s37, 170\n"
        "v_min_u32 v19, 7, v18\n"
        "v_add_u32 v19, s37, v19\n"
        "s_min_u32 s37, s26, 13\n"
        "s_lshl_b32 s37, s37, 1\n"
        "s_add_u32 s37, s37, 144\n"
        "v_add_u32 v20, s37, v18\n"
        "v_cmp_gt_u32 vcc, 6, v18\n"
        "s_nop 1\n"
        "v_cndmask_b32 v19, v19, v20, vcc\n"
        "s_add_u32 m0, s32, 14336\n"
        "s_add_u32 s52, s31, 14720\n"
        "buffer_load_dwordx4 v241, s[56:59], s52 offen lds\n"
        "s_lshl_b32 s37, s26, 2\n"
        "s_add_u32 s37, s37, 84\n"
        "v_add_u32 v20, s37, v18\n"
        "v_cmp_gt_u32 vcc, 4, v18\n"
        "s_nop 1\n"
        "v_cndmask_b32 v19, v19, v20, vcc\n"
        "v_bfe_u32 v13, v19, 2, 1\n"
        "v_bfe_u32 v20, v19, 5, 1\n"
        "v_lshl_add_u32 v13, v13, 1, v20\n"
        "v_and_b32 v20, 3, v19\n"
        "s_waitcnt vmcnt(11)\n"
        "ds_read_b128 v[32:35], v246 offset:0\n"
        "ds_read_b128 v[36:39], v247 offset:0\n"
        "ds_read_b128 v[40:43], v248 offset:0\n"
        "ds_read_b128 v[44:47], v249 offset:0\n"
        "s_add_u32 m0, s32, 15360\n"
        "s_add_u32 s52, s31, 21888\n"
        "buffer_load_dwordx4 v242, s[56:59], s52 offen lds\n"
        "v_lshl_add_u32 v13, v13, 2, v20\n"
        "v_and_b32 v20, 3, v0\n"
        "v_lshl_add_u32 v13, v13, 2, v20\n"
        "v_lshlrev_b32 v13, 4, v13\n"
        "v_bfe_u32 v20, v19, 3, 2\n"
        "v_lshl_add_u32 v13, v20, 2, v13\n"
        "v_lshrrev_b32 v20, 6, v19\n"
        "v_add_u32 v13, v13, v20\n"
        "v_mul_u32_u24 v13, 128, v13\n"
        "v_add_u32 v13, 2048, v13\n"
        "s_add_u32 m0, s32, 16384\n"
        "s_add_u32 s52, s31, 512\n"
        "buffer_load_dwordx4 v241, s[56:59], s52 offen lds\n"
        "v_cmp_gt_u32 vcc, 128, v8\n"
        "v_mov_b32 v250, 1073741824\n"
        "s_nop 0\n"
        "v_cndmask_b32 v250, v250, v9, vcc\n"
        "s_cmp_eq_u32 s89, 512\n"
        "s_cselect_b64 vcc, -1, 0\n"
        "v_cndmask_b32 v250, v1, v250, vcc\n"
        "v_add_u32 v9, 89088, v9\n"
        "s_lshr_b32 s37, s26, 2\n"
        "s_mul_i32 s37, s37, 128\n"
        "s_add_u32 s37, s37, 1443840\n"
        "s_add_u32 m0, s32, 17408\n"
        "s_add_u32 s52, s31, 7680\n"
        "buffer_load_dwordx4 v242, s[56:59], s52 offen lds\n"
        "v_mul_u32_u24 v238, 512, v0\n"
        "v_add_u32 v238, s37, v238\n"
        "v_min_u32 v239, 19, v0\n"
        "v_mul_u32_u24 v239, 512, v239\n"
        "s_add_u32 s37, s37, 32768\n"
        "v_add_u32 v239, s37, v239\n"
        "s_lshr_b32 s37, s24, 1\n"
        "s_and_b32 s38, s24, 1\n"
        "s_mul_i32 s83, s37, 29696\n"
        "s_mul_i32 s39, s38, 7168\n"
        "s_add_u32 m0, s32, 18432\n"
        "s_add_u32 s52, s31, 14848\n"
        "buffer_load_dwordx4 v241, s[56:59], s52 offen lds\n"
        "s_add_u32 s83, s83, s39\n"
        "s_add_u32 s83, s83, 29696\n"
        "s_mul_i32 s84, s26, 89088\n"
        "s_add_u32 s84, s84, 1574912\n"
        "v_add_u32 v26, s83, v1\n"
        "v_add_u32 v27, s85, v1\n"
        "s_mul_i32 s34, s26, 262144\n"
        "s_mul_i32 s39, s27, 4096\n"
        "s_add_u32 s34, s34, s39\n"
        "s_waitcnt vmcnt(11)\n"
        "ds_read_b128 v[48:51], v246 offset:4096\n"
        "ds_read_b128 v[52:55], v247 offset:4096\n"
        "ds_read_b128 v[56:59], v248 offset:4096\n"
        "ds_read_b128 v[60:63], v249 offset:4096\n"
        "s_waitcnt lgkmcnt(4)\n"
        "v_dot2_f32_bf16 v144, v32, v252, v144\n"
        "v_dot2_f32_bf16 v145, v32, v32, v145\n"
        "v_dot2_f32_bf16 v146, v33, v252, v146\n"
        "v_dot2_f32_bf16 v147, v33, v33, v147\n"
        "v_dot2_f32_bf16 v144, v34, v252, v144\n"
        "v_dot2_f32_bf16 v145, v34, v34, v145\n"
        "v_dot2_f32_bf16 v146, v35, v252, v146\n"
        "v_dot2_f32_bf16 v147, v35, v35, v147\n"
        "v_permlane32_swap_b32 v32, v34\n"
        "v_permlane32_swap_b32 v33, v35\n"
        "s_add_u32 m0, s32, 19456\n"
        "s_add_u32 s52, s31, 22016\n"
        "buffer_load_dwordx4 v242, s[56:59], s52 offen lds\n"
        "s_mul_i32 s39, s28, 4194304\n"
        "s_add_u32 s34, s34, s39\n"
        "s_add_u32 s34, s34, s47\n"
        "s_mul_i32 s35, s26, 8192\n"
        "s_mul_i32 s39, s24, 2048\n"
        "s_add_u32 s35, s35, s39\n"
        "s_mul_i32 s39, s25, 128\n"
        "s_add_u32 s35, s35, s39\n"
        "s_add_u32 s35, s35, s29\n"
        "s_add_u32 s35, s35, 2048\n"
        "v_dot2_f32_bf16 v144, v36, v252, v144\n"
        "v_dot2_f32_bf16 v145, v36, v36, v145\n"
        "v_dot2_f32_bf16 v146, v37, v252, v146\n"
        "v_dot2_f32_bf16 v147, v37, v37, v147\n"
        "v_dot2_f32_bf16 v144, v38, v252, v144\n"
        "v_dot2_f32_bf16 v145, v38, v38, v145\n"
        "v_dot2_f32_bf16 v146, v39, v252, v146\n"
        "v_dot2_f32_bf16 v147, v39, v39, v147\n"
        "v_permlane32_swap_b32 v36, v38\n"
        "v_permlane32_swap_b32 v37, v39\n"
        "s_add_u32 m0, s32, 20480\n"
        "s_add_u32 s52, s31, 640\n"
        "buffer_load_dwordx4 v241, s[56:59], s52 offen lds\n"
        "v_dot2_f32_bf16 v144, v40, v252, v144\n"
        "v_dot2_f32_bf16 v145, v40, v40, v145\n"
        "v_dot2_f32_bf16 v146, v41, v252, v146\n"
        "v_dot2_f32_bf16 v147, v41, v41, v147\n"
        "v_dot2_f32_bf16 v144, v42, v252, v144\n"
        "v_dot2_f32_bf16 v145, v42, v42, v145\n"
        "v_dot2_f32_bf16 v146, v43, v252, v146\n"
        "v_dot2_f32_bf16 v147, v43, v43, v147\n"
        "v_permlane32_swap_b32 v40, v42\n"
        "v_permlane32_swap_b32 v41, v43\n"
        "s_add_u32 m0, s32, 21504\n"
        "s_add_u32 s52, s31, 7808\n"
        "buffer_load_dwordx4 v242, s[56:59], s52 offen lds\n"
        "v_dot2_f32_bf16 v144, v44, v252, v144\n"
        "v_dot2_f32_bf16 v145, v44, v44, v145\n"
        "v_dot2_f32_bf16 v146, v45, v252, v146\n"
        "v_dot2_f32_bf16 v147, v45, v45, v147\n"
        "v_dot2_f32_bf16 v144, v46, v252, v144\n"
        "v_dot2_f32_bf16 v145, v46, v46, v145\n"
        "v_dot2_f32_bf16 v146, v47, v252, v146\n"
        "v_dot2_f32_bf16 v147, v47, v47, v147\n"
        "v_permlane32_swap_b32 v44, v46\n"
        "v_permlane32_swap_b32 v45, v47\n"
        "s_add_u32 m0, s32, 22528\n"
        "s_add_u32 s52, s31, 14976\n"
        "buffer_load_dwordx4 v241, s[56:59], s52 offen lds\n"
        "s_waitcnt vmcnt(11)\n"
        "ds_read_b128 v[64:67], v246 offset:8192\n"
        "ds_read_b128 v[68:71], v247 offset:8192\n"
        "ds_read_b128 v[72:75], v248 offset:8192\n"
        "ds_read_b128 v[76:79], v249 offset:8192\n"
        "s_waitcnt lgkmcnt(4)\n"
        "v_dot2_f32_bf16 v144, v48, v252, v144\n"
        "v_dot2_f32_bf16 v145, v48, v48, v145\n"
        "v_dot2_f32_bf16 v146, v49, v252, v146\n"
        "v_dot2_f32_bf16 v147, v49, v49, v147\n"
        "v_dot2_f32_bf16 v144, v50, v252, v144\n"
        "v_dot2_f32_bf16 v145, v50, v50, v145\n"
        "v_dot2_f32_bf16 v146, v51, v252, v146\n"
        "v_dot2_f32_bf16 v147, v51, v51, v147\n"
        "v_permlane32_swap_b32 v48, v50\n"
        "v_permlane32_swap_b32 v49, v51\n"
        "s_add_u32 m0, s32, 23552\n"
        "s_add_u32 s52, s31, 22144\n"
        "buffer_load_dwordx4 v242, s[56:59], s52 offen lds\n"
        "v_dot2_f32_bf16 v144, v52, v252, v144\n"
        "v_dot2_f32_bf16 v145, v52, v52, v145\n"
        "v_dot2_f32_bf16 v146, v53, v252, v146\n"
        "v_dot2_f32_bf16 v147, v53, v53, v147\n"
        "v_dot2_f32_bf16 v144, v54, v252, v144\n"
        "v_dot2_f32_bf16 v145, v54, v54, v145\n"
        "v_dot2_f32_bf16 v146, v55, v252, v146\n"
        "v_dot2_f32_bf16 v147, v55, v55, v147\n"
        "v_permlane32_swap_b32 v52, v54\n"
        "v_permlane32_swap_b32 v53, v55\n"
        "s_add_u32 m0, s32, 24576\n"
        "s_add_u32 s52, s31, 768\n"
        "buffer_load_dwordx4 v241, s[56:59], s52 offen lds\n"
        "v_dot2_f32_bf16 v144, v56, v252, v144\n"
        "v_dot2_f32_bf16 v145, v56, v56, v145\n"
        "v_dot2_f32_bf16 v146, v57, v252, v146\n"
        "v_dot2_f32_bf16 v147, v57, v57, v147\n"
        "v_dot2_f32_bf16 v144, v58, v252, v144\n"
        "v_dot2_f32_bf16 v145, v58, v58, v145\n"
        "v_dot2_f32_bf16 v146, v59, v252, v146\n"
        "v_dot2_f32_bf16 v147, v59, v59, v147\n"
        "v_permlane32_swap_b32 v56, v58\n"
        "v_permlane32_swap_b32 v57, v59\n"
        "s_add_u32 m0, s32, 25600\n"
        "s_add_u32 s52, s31, 7936\n"
        "buffer_load_dwordx4 v242, s[56:59], s52 offen lds\n"
        "v_dot2_f32_bf16 v144, v60, v252, v144\n"
        "v_dot2_f32_bf16 v145, v60, v60, v145\n"
        "v_dot2_f32_bf16 v146, v61, v252, v146\n"
        "v_dot2_f32_bf16 v147, v61, v61, v147\n"
        "v_dot2_f32_bf16 v144, v62, v252, v144\n"
        "v_dot2_f32_bf16 v145, v62, v62, v145\n"
        "v_dot2_f32_bf16 v146, v63, v252, v146\n"
        "v_dot2_f32_bf16 v147, v63, v63, v147\n"
        "v_permlane32_swap_b32 v60, v62\n"
        "v_permlane32_swap_b32 v61, v63\n"
        "s_add_u32 m0, s32, 26624\n"
        "s_add_u32 s52, s31, 15104\n"
        "buffer_load_dwordx4 v241, s[56:59], s52 offen lds\n"
        "s_waitcnt vmcnt(11)\n"
        "ds_read_b128 v[80:83], v246 offset:12288\n"
        "ds_read_b128 v[84:87], v247 offset:12288\n"
        "ds_read_b128 v[88:91], v248 offset:12288\n"
        "ds_read_b128 v[92:95], v249 offset:12288\n"
        "s_waitcnt lgkmcnt(4)\n"
        "v_dot2_f32_bf16 v144, v64, v252, v144\n"
        "v_dot2_f32_bf16 v145, v64, v64, v145\n"
        "v_dot2_f32_bf16 v146, v65, v252, v146\n"
        "v_dot2_f32_bf16 v147, v65, v65, v147\n"
        "v_dot2_f32_bf16 v144, v66, v252, v144\n"
        "v_dot2_f32_bf16 v145, v66, v66, v145\n"
        "v_dot2_f32_bf16 v146, v67, v252, v146\n"
        "v_dot2_f32_bf16 v147, v67, v67, v147\n"
        "v_permlane32_swap_b32 v64, v66\n"
        "v_permlane32_swap_b32 v65, v67\n"
        "s_add_u32 m0, s32, 27648\n"
        "s_add_u32 s52, s31, 22272\n"
        "buffer_load_dwordx4 v242, s[56:59], s52 offen lds\n"
        "v_dot2_f32_bf16 v144, v68, v252, v144\n"
        "v_dot2_f32_bf16 v145, v68, v68, v145\n"
        "v_dot2_f32_bf16 v146, v69, v252, v146\n"
        "v_dot2_f32_bf16 v147, v69, v69, v147\n"
        "v_dot2_f32_bf16 v144, v70, v252, v144\n"
        "v_dot2_f32_bf16 v145, v70, v70, v145\n"
        "v_dot2_f32_bf16 v146, v71, v252, v146\n"
        "v_dot2_f32_bf16 v147, v71, v71, v147\n"
        "v_permlane32_swap_b32 v68, v70\n"
        "v_permlane32_swap_b32 v69, v71\n"
        "buffer_load_dwordx2 v[176:177], v238, s[8:11], 0 offen sc1\n"
        "buffer_load_dwordx2 v[178:179], v239, s[8:11], 0 offen sc1\n"
        "v_dot2_f32_bf16 v144, v72, v252, v144\n"
        "v_dot2_f32_bf16 v145, v72, v72, v145\n"
        "v_dot2_f32_bf16 v146, v73, v252, v146\n"
        "v_dot2_f32_bf16 v147, v73, v73, v147\n"
        "v_dot2_f32_bf16 v144, v74, v252, v144\n"
        "v_dot2_f32_bf16 v145, v74, v74, v145\n"
        "v_dot2_f32_bf16 v146, v75, v252, v146\n"
        "v_dot2_f32_bf16 v147, v75, v75, v147\n"
        "v_permlane32_swap_b32 v72, v74\n"
        "v_permlane32_swap_b32 v73, v75\n"
        "v_dot2_f32_bf16 v144, v76, v252, v144\n"
        "v_dot2_f32_bf16 v145, v76, v76, v145\n"
        "v_dot2_f32_bf16 v146, v77, v252, v146\n"
        "v_dot2_f32_bf16 v147, v77, v77, v147\n"
        "v_dot2_f32_bf16 v144, v78, v252, v144\n"
        "v_dot2_f32_bf16 v145, v78, v78, v145\n"
        "v_dot2_f32_bf16 v146, v79, v252, v146\n"
        "v_dot2_f32_bf16 v147, v79, v79, v147\n"
        "v_permlane32_swap_b32 v76, v78\n"
        "v_permlane32_swap_b32 v77, v79\n"
        "s_waitcnt lgkmcnt(0)\n"
        "v_dot2_f32_bf16 v144, v80, v252, v144\n"
        "v_dot2_f32_bf16 v145, v80, v80, v145\n"
        "v_dot2_f32_bf16 v146, v81, v252, v146\n"
        "v_dot2_f32_bf16 v147, v81, v81, v147\n"
        "v_dot2_f32_bf16 v144, v82, v252, v144\n"
        "v_dot2_f32_bf16 v145, v82, v82, v145\n"
        "v_dot2_f32_bf16 v146, v83, v252, v146\n"
        "v_dot2_f32_bf16 v147, v83, v83, v147\n"
        "v_permlane32_swap_b32 v80, v82\n"
        "v_permlane32_swap_b32 v81, v83\n"
        "s_waitcnt vmcnt(10)\n"
        "ds_read_b128 v[96:99], v246 offset:16384\n"
        "ds_read_b128 v[100:103], v247 offset:16384\n"
        "ds_read_b128 v[104:107], v248 offset:16384\n"
        "ds_read_b128 v[108:111], v249 offset:16384\n"
        "v_dot2_f32_bf16 v144, v84, v252, v144\n"
        "v_dot2_f32_bf16 v145, v84, v84, v145\n"
        "v_dot2_f32_bf16 v146, v85, v252, v146\n"
        "v_dot2_f32_bf16 v147, v85, v85, v147\n"
        "v_dot2_f32_bf16 v144, v86, v252, v144\n"
        "v_dot2_f32_bf16 v145, v86, v86, v145\n"
        "v_dot2_f32_bf16 v146, v87, v252, v146\n"
        "v_dot2_f32_bf16 v147, v87, v87, v147\n"
        "v_permlane32_swap_b32 v84, v86\n"
        "v_permlane32_swap_b32 v85, v87\n"
        "v_dot2_f32_bf16 v144, v88, v252, v144\n"
        "v_dot2_f32_bf16 v145, v88, v88, v145\n"
        "v_dot2_f32_bf16 v146, v89, v252, v146\n"
        "v_dot2_f32_bf16 v147, v89, v89, v147\n"
        "v_dot2_f32_bf16 v144, v90, v252, v144\n"
        "v_dot2_f32_bf16 v145, v90, v90, v145\n"
        "v_dot2_f32_bf16 v146, v91, v252, v146\n"
        "v_dot2_f32_bf16 v147, v91, v91, v147\n"
        "v_permlane32_swap_b32 v88, v90\n"
        "v_permlane32_swap_b32 v89, v91\n"
        "v_dot2_f32_bf16 v144, v92, v252, v144\n"
        "v_dot2_f32_bf16 v145, v92, v92, v145\n"
        "v_dot2_f32_bf16 v146, v93, v252, v146\n"
        "v_dot2_f32_bf16 v147, v93, v93, v147\n"
        "v_dot2_f32_bf16 v144, v94, v252, v144\n"
        "v_dot2_f32_bf16 v145, v94, v94, v145\n"
        "v_dot2_f32_bf16 v146, v95, v252, v146\n"
        "v_dot2_f32_bf16 v147, v95, v95, v147\n"
        "v_permlane32_swap_b32 v92, v94\n"
        "v_permlane32_swap_b32 v93, v95\n"
        "s_waitcnt vmcnt(6)\n"
        "ds_read_b128 v[112:115], v246 offset:20480\n"
        "ds_read_b128 v[116:119], v247 offset:20480\n"
        "ds_read_b128 v[120:123], v248 offset:20480\n"
        "ds_read_b128 v[124:127], v249 offset:20480\n"
        "s_waitcnt lgkmcnt(4)\n"
        "v_dot2_f32_bf16 v144, v96, v252, v144\n"
        "v_dot2_f32_bf16 v145, v96, v96, v145\n"
        "v_dot2_f32_bf16 v146, v97, v252, v146\n"
        "v_dot2_f32_bf16 v147, v97, v97, v147\n"
        "v_dot2_f32_bf16 v144, v98, v252, v144\n"
        "v_dot2_f32_bf16 v145, v98, v98, v145\n"
        "v_dot2_f32_bf16 v146, v99, v252, v146\n"
        "v_dot2_f32_bf16 v147, v99, v99, v147\n"
        "v_permlane32_swap_b32 v96, v98\n"
        "v_permlane32_swap_b32 v97, v99\n"
        "v_dot2_f32_bf16 v144, v100, v252, v144\n"
        "v_dot2_f32_bf16 v145, v100, v100, v145\n"
        "v_dot2_f32_bf16 v146, v101, v252, v146\n"
        "v_dot2_f32_bf16 v147, v101, v101, v147\n"
        "v_dot2_f32_bf16 v144, v102, v252, v144\n"
        "v_dot2_f32_bf16 v145, v102, v102, v145\n"
        "v_dot2_f32_bf16 v146, v103, v252, v146\n"
        "v_dot2_f32_bf16 v147, v103, v103, v147\n"
        "v_permlane32_swap_b32 v100, v102\n"
        "v_permlane32_swap_b32 v101, v103\n"
        "v_dot2_f32_bf16 v144, v104, v252, v144\n"
        "v_dot2_f32_bf16 v145, v104, v104, v145\n"
        "v_dot2_f32_bf16 v146, v105, v252, v146\n"
        "v_dot2_f32_bf16 v147, v105, v105, v147\n"
        "v_dot2_f32_bf16 v144, v106, v252, v144\n"
        "v_dot2_f32_bf16 v145, v106, v106, v145\n"
        "v_dot2_f32_bf16 v146, v107, v252, v146\n"
        "v_dot2_f32_bf16 v147, v107, v107, v147\n"
        "v_permlane32_swap_b32 v104, v106\n"
        "v_permlane32_swap_b32 v105, v107\n"
        "s_waitcnt vmcnt(2)\n"
        "ds_read_b128 v[128:131], v246 offset:24576\n"
        "ds_read_b128 v[132:135], v247 offset:24576\n"
        "ds_read_b128 v[136:139], v248 offset:24576\n"
        "ds_read_b128 v[140:143], v249 offset:24576\n"
        "v_dot2_f32_bf16 v144, v108, v252, v144\n"
        "v_dot2_f32_bf16 v145, v108, v108, v145\n"
        "v_dot2_f32_bf16 v146, v109, v252, v146\n"
        "v_dot2_f32_bf16 v147, v109, v109, v147\n"
        "v_dot2_f32_bf16 v144, v110, v252, v144\n"
        "v_dot2_f32_bf16 v145, v110, v110, v145\n"
        "v_dot2_f32_bf16 v146, v111, v252, v146\n"
        "v_dot2_f32_bf16 v147, v111, v111, v147\n"
        "v_permlane32_swap_b32 v108, v110\n"
        "v_permlane32_swap_b32 v109, v111\n"
        "s_waitcnt lgkmcnt(4)\n"
        "v_dot2_f32_bf16 v144, v112, v252, v144\n"
        "v_dot2_f32_bf16 v145, v112, v112, v145\n"
        "v_dot2_f32_bf16 v146, v113, v252, v146\n"
        "v_dot2_f32_bf16 v147, v113, v113, v147\n"
        "v_dot2_f32_bf16 v144, v114, v252, v144\n"
        "v_dot2_f32_bf16 v145, v114, v114, v145\n"
        "v_dot2_f32_bf16 v146, v115, v252, v146\n"
        "v_dot2_f32_bf16 v147, v115, v115, v147\n"
        "v_permlane32_swap_b32 v112, v114\n"
        "v_permlane32_swap_b32 v113, v115\n"
        "s_waitcnt vmcnt(0)\n"
        "s_mov_b32 s44, 0x20000\n"
        ".Lwqpoll1:\n"
        "v_cmp_ne_u64 vcc, s[96:97], v[176:177]\n"
        "v_cmp_ne_u64 s[32:33], s[96:97], v[178:179]\n"
        "s_nop 0\n"
        "s_or_b64 vcc, vcc, s[32:33]\n"
        "s_cbranch_vccz .Lwqpolled2\n"
        "buffer_load_dwordx2 v[176:177], v238, s[8:11], 0 offen sc1\n"
        "buffer_load_dwordx2 v[178:179], v239, s[8:11], 0 offen sc1\n"
        "s_waitcnt vmcnt(0)\n"
        "s_sub_u32 s44, s44, 1\n"
        "s_cbranch_scc0 .Lwqpoll1\n"
        ".Lwqpolled2:\n"
        "s_waitcnt lgkmcnt(0)\n"
        "s_barrier\n"
        "s_add_u32 m0, s83, 0\n"
        "s_add_u32 s52, m0, s84\n"
        "buffer_load_dwordx4 v1, s[8:11], s52 offen offset:0 sc1 lds\n"
        "buffer_load_dwordx4 v1, s[8:11], s52 offen offset:1024 sc1 lds\n"
        "s_waitcnt lgkmcnt(4)\n"
        "v_dot2_f32_bf16 v144, v116, v252, v144\n"
        "v_dot2_f32_bf16 v145, v116, v116, v145\n"
        "v_dot2_f32_bf16 v146, v117, v252, v146\n"
        "v_dot2_f32_bf16 v147, v117, v117, v147\n"
        "v_dot2_f32_bf16 v144, v118, v252, v144\n"
        "v_dot2_f32_bf16 v145, v118, v118, v145\n"
        "v_dot2_f32_bf16 v146, v119, v252, v146\n"
        "v_dot2_f32_bf16 v147, v119, v119, v147\n"
        "v_permlane32_swap_b32 v116, v118\n"
        "v_permlane32_swap_b32 v117, v119\n"
        "buffer_load_dwordx4 v1, s[8:11], s52 offen offset:2048 sc1 lds\n"
        "buffer_load_dwordx4 v1, s[8:11], s52 offen offset:3072 sc1 lds\n"
        "v_dot2_f32_bf16 v144, v120, v252, v144\n"
        "v_dot2_f32_bf16 v145, v120, v120, v145\n"
        "v_dot2_f32_bf16 v146, v121, v252, v146\n"
        "v_dot2_f32_bf16 v147, v121, v121, v147\n"
        "v_dot2_f32_bf16 v144, v122, v252, v144\n"
        "v_dot2_f32_bf16 v145, v122, v122, v145\n"
        "v_dot2_f32_bf16 v146, v123, v252, v146\n"
        "v_dot2_f32_bf16 v147, v123, v123, v147\n"
        "v_permlane32_swap_b32 v120, v122\n"
        "v_permlane32_swap_b32 v121, v123\n"
        "s_add_u32 m0, s83, 4096\n"
        "s_add_u32 s52, m0, s84\n"
        "buffer_load_dwordx4 v1, s[8:11], s52 offen offset:0 sc1 lds\n"
        "buffer_load_dwordx4 v1, s[8:11], s52 offen offset:1024 sc1 lds\n"
        "v_dot2_f32_bf16 v144, v124, v252, v144\n"
        "v_dot2_f32_bf16 v145, v124, v124, v145\n"
        "v_dot2_f32_bf16 v146, v125, v252, v146\n"
        "v_dot2_f32_bf16 v147, v125, v125, v147\n"
        "v_dot2_f32_bf16 v144, v126, v252, v144\n"
        "v_dot2_f32_bf16 v145, v126, v126, v145\n"
        "v_dot2_f32_bf16 v146, v127, v252, v146\n"
        "v_dot2_f32_bf16 v147, v127, v127, v147\n"
        "v_permlane32_swap_b32 v124, v126\n"
        "v_permlane32_swap_b32 v125, v127\n"
        "buffer_load_dwordx4 v1, s[8:11], s52 offen offset:2048 sc1 lds\n"
        "s_add_u32 m0, s83, 14336\n"
        "s_add_u32 s52, m0, s84\n"
        "buffer_load_dwordx4 v1, s[8:11], s52 offen offset:0 sc1 lds\n"
        "s_waitcnt lgkmcnt(0)\n"
        "v_dot2_f32_bf16 v144, v128, v252, v144\n"
        "v_dot2_f32_bf16 v145, v128, v128, v145\n"
        "v_dot2_f32_bf16 v146, v129, v252, v146\n"
        "v_dot2_f32_bf16 v147, v129, v129, v147\n"
        "v_dot2_f32_bf16 v144, v130, v252, v144\n"
        "v_dot2_f32_bf16 v145, v130, v130, v145\n"
        "v_dot2_f32_bf16 v146, v131, v252, v146\n"
        "v_dot2_f32_bf16 v147, v131, v131, v147\n"
        "v_permlane32_swap_b32 v128, v130\n"
        "v_permlane32_swap_b32 v129, v131\n"
        "buffer_load_dwordx4 v1, s[8:11], s52 offen offset:1024 sc1 lds\n"
        "buffer_load_dwordx4 v1, s[8:11], s52 offen offset:2048 sc1 lds\n"
        "v_dot2_f32_bf16 v144, v132, v252, v144\n"
        "v_dot2_f32_bf16 v145, v132, v132, v145\n"
        "v_dot2_f32_bf16 v146, v133, v252, v146\n"
        "v_dot2_f32_bf16 v147, v133, v133, v147\n"
        "v_dot2_f32_bf16 v144, v134, v252, v144\n"
        "v_dot2_f32_bf16 v145, v134, v134, v145\n"
        "v_dot2_f32_bf16 v146, v135, v252, v146\n"
        "v_dot2_f32_bf16 v147, v135, v135, v147\n"
        "v_permlane32_swap_b32 v132, v134\n"
        "v_permlane32_swap_b32 v133, v135\n"
        "buffer_load_dwordx4 v1, s[8:11], s52 offen offset:3072 sc1 lds\n"
        "s_add_u32 m0, s83, 18432\n"
        "s_add_u32 s52, m0, s84\n"
        "buffer_load_dwordx4 v1, s[8:11], s52 offen offset:0 sc1 lds\n"
        "v_dot2_f32_bf16 v144, v136, v252, v144\n"
        "v_dot2_f32_bf16 v145, v136, v136, v145\n"
        "v_dot2_f32_bf16 v146, v137, v252, v146\n"
        "v_dot2_f32_bf16 v147, v137, v137, v147\n"
        "v_dot2_f32_bf16 v144, v138, v252, v144\n"
        "v_dot2_f32_bf16 v145, v138, v138, v145\n"
        "v_dot2_f32_bf16 v146, v139, v252, v146\n"
        "v_dot2_f32_bf16 v147, v139, v139, v147\n"
        "v_permlane32_swap_b32 v136, v138\n"
        "v_permlane32_swap_b32 v137, v139\n"
        "buffer_load_dwordx4 v1, s[8:11], s52 offen offset:1024 sc1 lds\n"
        "buffer_load_dwordx4 v1, s[8:11], s52 offen offset:2048 sc1 lds\n"
        "v_dot2_f32_bf16 v144, v140, v252, v144\n"
        "v_dot2_f32_bf16 v145, v140, v140, v145\n"
        "v_dot2_f32_bf16 v146, v141, v252, v146\n"
        "v_dot2_f32_bf16 v147, v141, v141, v147\n"
        "v_dot2_f32_bf16 v144, v142, v252, v144\n"
        "v_dot2_f32_bf16 v145, v142, v142, v145\n"
        "v_dot2_f32_bf16 v146, v143, v252, v146\n"
        "v_dot2_f32_bf16 v147, v143, v143, v147\n"
        "v_permlane32_swap_b32 v140, v142\n"
        "v_permlane32_swap_b32 v141, v143\n"
        "s_cmp_eq_u32 s24, 0\n"
        "s_cselect_b32 s37, s93, 0\n"
        "s_cmp_eq_u32 s37, 1\n"
        "s_cselect_b32 s95, s95, 1073741824\n"
        "v_lshrrev_b32 v242, 1, v8\n"
        "v_add_u32 v242, 143360, v242\n"
        "s_lshl_b32 s30, s24, 7\n"
        "v_add_u32 v241, s30, v242\n"
        "s_waitcnt vmcnt(7)\n"
        "s_barrier\n"
        "ds_read_b128 v[208:211], v1 offset:29696\n"
        "ds_read_b128 v[212:215], v1 offset:59392\n"
        "ds_read_b128 v[216:219], v1 offset:30720\n"
        "ds_read_b128 v[220:223], v1 offset:60416\n"
        "ds_read_b128 v[224:227], v1 offset:31744\n"
        "ds_read_b128 v[228:231], v1 offset:61440\n"
        "s_waitcnt lgkmcnt(5)\n"
        "v_mfma_f32_32x32x16_bf16 v[160:175], v[208:211], v[32:35], 0\n"
        "ds_read_b128 v[232:235], v1 offset:32768\n"
        "s_add_u32 m0, s85, 0\n"
        "s_add_u32 s52, m0, s84\n"
        "buffer_load_dwordx4 v1, s[8:11], s52 offen offset:0 sc1 lds\n"
        "v_add_f32 v144, v144, v146\n"
        "s_waitcnt lgkmcnt(5)\n"
        "v_mfma_f32_32x32x16_bf16 v[176:191], v[32:35], v[212:215], 0\n"
        "ds_read_b128 v[208:211], v1 offset:62464\n"
        "ds_read_b64_tr_b16 v[192:193], v245 offset:0\n"
        "ds_read_b64_tr_b16 v[194:195], v245 offset:512\n"
        "v_add_f32 v145, v145, v147\n"
        "s_waitcnt lgkmcnt(7)\n"
        "v_mfma_f32_32x32x16_bf16 v[160:175], v[216:219], v[36:39], v[160:175]\n"
        "ds_read_b128 v[212:215], v1 offset:33792\n"
        "v_mov_b32 v16, v144\n"
        "s_waitcnt lgkmcnt(7)\n"
        "v_mfma_f32_32x32x16_bf16 v[176:191], v[36:39], v[220:223], v[176:191]\n"
        "ds_read_b128 v[216:219], v1 offset:63488\n"
        "s_waitcnt lgkmcnt(2)\n"
        "v_mfma_f32_32x32x16_bf16 a[128:143], v[252:255], v[192:195], 0\n"
        "buffer_store_dwordx4 v[192:195], v250, s[8:11], s88 offen sc1\n"
        "s_add_u32 s88, s88, s89\n"
        "v_mov_b32 v17, v145\n"
        "v_mfma_f32_32x32x16_bf16 v[160:175], v[224:227], v[40:43], v[160:175]\n"
        "ds_read_b128 v[220:223], v1 offset:34816\n"
        "buffer_load_dwordx4 v1, s[8:11], s52 offen offset:1024 sc1 lds\n"
        "s_nop 0\n"
        "v_mfma_f32_32x32x16_bf16 v[176:191], v[40:43], v[228:231], v[176:191]\n"
        "ds_read_b128 v[224:227], v1 offset:64512\n"
        "ds_read_b64_tr_b16 v[196:197], v245 offset:1024\n"
        "ds_read_b64_tr_b16 v[198:199], v245 offset:1536\n"
        "v_permlane32_swap_b32 v16, v144\n"
        "v_mfma_f32_32x32x16_bf16 v[160:175], v[232:235], v[44:47], v[160:175]\n"
        "ds_read_b128 v[228:231], v1 offset:35840\n"
        "v_permlane32_swap_b32 v17, v145\n"
        "v_mfma_f32_32x32x16_bf16 v[176:191], v[44:47], v[208:211], v[176:191]\n"
        "ds_read_b128 v[232:235], v2 offset:0\n"
        "s_waitcnt lgkmcnt(2)\n"
        "v_mfma_f32_32x32x16_bf16 a[128:143], v[252:255], v[196:199], a[128:143]\n"
        "buffer_store_dwordx4 v[196:199], v250, s[8:11], s88 offen sc1\n"
        "s_add_u32 s88, s88, s89\n"
        "v_add_f32 v144, v144, v16\n"
        "v_mfma_f32_32x32x16_bf16 v[160:175], v[212:215], v[48:51], v[160:175]\n"
        "ds_read_b128 v[208:211], v1 offset:36864\n"
        "buffer_load_dwordx4 v1, s[8:11], s52 offen offset:2048 sc1 lds\n"
        "v_add_f32 v145, v145, v17\n"
        "v_mfma_f32_32x32x16_bf16 v[176:191], v[48:51], v[216:219], v[176:191]\n"
        "ds_read_b128 v[212:215], v2 offset:1024\n"
        "ds_read_b64_tr_b16 v[200:201], v245 offset:2048\n"
        "ds_read_b64_tr_b16 v[202:203], v245 offset:2560\n"
        "v_mul_f32 v144, 0xbb124925, v144\n"
        "v_mfma_f32_32x32x16_bf16 v[160:175], v[220:223], v[52:55], v[160:175]\n"
        "ds_read_b128 v[216:219], v1 offset:37888\n"
        "v_mul_f32 v145, 0x3b124925, v145\n"
        "v_mfma_f32_32x32x16_bf16 v[176:191], v[52:55], v[224:227], v[176:191]\n"
        "ds_read_b128 v[220:223], v2 offset:2048\n"
        "s_waitcnt lgkmcnt(2)\n"
        "v_mfma_f32_32x32x16_bf16 a[128:143], v[252:255], v[200:203], a[128:143]\n"
        "buffer_store_dwordx4 v[200:203], v250, s[8:11], s88 offen sc1\n"
        "s_add_u32 s88, s88, s89\n"
        "v_fma_f32 v145, -v144, v144, v145\n"
        "v_mfma_f32_32x32x16_bf16 v[160:175], v[228:231], v[56:59], v[160:175]\n"
        "ds_read_b128 v[224:227], v1 offset:38912\n"
        "buffer_load_dwordx4 v1, s[8:11], s52 offen offset:3072 sc1 lds\n"
        "v_max_f32 v145, 0, v145\n"
        "v_mfma_f32_32x32x16_bf16 v[176:191], v[56:59], v[232:235], v[176:191]\n"
        "ds_read_b128 v[228:231], v2 offset:3072\n"
        "ds_read_b64_tr_b16 v[204:205], v245 offset:3072\n"
        "ds_read_b64_tr_b16 v[206:207], v245 offset:3584\n"
        "v_add_f32 v145, 0x3727c5ac, v145\n"
        "v_mfma_f32_32x32x16_bf16 v[160:175], v[208:211], v[60:63], v[160:175]\n"
        "ds_read_b128 v[232:235], v1 offset:39936\n"
        "v_rsq_f32 v152, v145\n"
        "v_mfma_f32_32x32x16_bf16 v[176:191], v[60:63], v[212:215], v[176:191]\n"
        "ds_read_b128 v[208:211], v2 offset:4096\n"
        "s_waitcnt lgkmcnt(2)\n"
        "v_mfma_f32_32x32x16_bf16 a[128:143], v[252:255], v[204:207], a[128:143]\n"
        "buffer_store_dwordx4 v[204:207], v250, s[8:11], s88 offen sc1\n"
        "s_add_u32 s88, s88, s89\n"
        "s_nop 0\n"
        "v_mfma_f32_32x32x16_bf16 v[160:175], v[216:219], v[64:67], v[160:175]\n"
        "ds_read_b128 v[212:215], v1 offset:40960\n"
        "s_add_u32 m0, s85, 4096\n"
        "s_add_u32 s52, m0, s84\n"
        "buffer_load_dwordx4 v1, s[8:11], s52 offen offset:0 sc1 lds\n"
        "v_mul_f32 v154, v145, v152\n"
        "v_mfma_f32_32x32x16_bf16 v[176:191], v[64:67], v[220:223], v[176:191]\n"
        "ds_read_b128 v[216:219], v2 offset:5120\n"
        "ds_read_b64_tr_b16 v[192:193], v245 offset:4096\n"
        "ds_read_b64_tr_b16 v[194:195], v245 offset:4608\n"
        "v_cvt_pk_bf16_f32 v149, v144, v154\n"
        "v_mfma_f32_32x32x16_bf16 v[160:175], v[224:227], v[68:71], v[160:175]\n"
        "ds_read_b128 v[220:223], v1 offset:41984\n"
        "v_lshlrev_b32 v16, 16, v149\n"
        "v_mfma_f32_32x32x16_bf16 v[176:191], v[68:71], v[228:231], v[176:191]\n"
        "ds_read_b128 v[224:227], v2 offset:6144\n"
        "s_waitcnt lgkmcnt(2)\n"
        "v_mfma_f32_32x32x16_bf16 a[128:143], v[252:255], v[192:195], a[128:143]\n"
        "s_add_u32 s88, s88, s90\n"
        "buffer_store_dwordx4 v[192:195], v250, s[8:11], s88 offen sc1\n"
        "s_add_u32 s88, s88, s89\n"
        "v_and_b32 v17, 0xffff0000, v149\n"
        "v_mfma_f32_32x32x16_bf16 v[160:175], v[232:235], v[72:75], v[160:175]\n"
        "ds_read_b128 v[228:231], v1 offset:43008\n"
        "buffer_load_dwordx4 v1, s[8:11], s52 offen offset:1024 sc1 lds\n"
        "v_sub_f32 v16, v144, v16\n"
        "v_mfma_f32_32x32x16_bf16 v[176:191], v[72:75], v[208:211], v[176:191]\n"
        "ds_read_b128 v[232:235], v2 offset:7168\n"
        "ds_read_b64_tr_b16 v[196:197], v245 offset:5120\n"
        "ds_read_b64_tr_b16 v[198:199], v245 offset:5632\n"
        "v_sub_f32 v17, v154, v17\n"
        "v_mfma_f32_32x32x16_bf16 v[160:175], v[212:215], v[76:79], v[160:175]\n"
        "v_cvt_pk_bf16_f32 v16, v16, v17\n"
        "v_mfma_f32_32x32x16_bf16 v[176:191], v[76:79], v[216:219], v[176:191]\n"
        "s_waitcnt lgkmcnt(0)\n"
        "v_mfma_f32_32x32x16_bf16 a[128:143], v[252:255], v[196:199], a[128:143]\n"
        "buffer_store_dwordx4 v[196:199], v250, s[8:11], s88 offen sc1\n"
        "s_add_u32 s88, s88, s89\n"
        "v_and_b32 v17, 0xffff, v149\n"
        "v_mfma_f32_32x32x16_bf16 v[160:175], v[220:223], v[80:83], v[160:175]\n"
        "buffer_load_dwordx4 v1, s[8:11], s52 offen offset:2048 sc1 lds\n"
        "v_lshl_or_b32 v148, v16, 16, v17\n"
        "v_mfma_f32_32x32x16_bf16 v[176:191], v[80:83], v[224:227], v[176:191]\n"
        "ds_read_b64_tr_b16 v[200:201], v245 offset:6144\n"
        "ds_read_b64_tr_b16 v[202:203], v245 offset:6656\n"
        "v_lshrrev_b32 v150, 16, v16\n"
        "v_mfma_f32_32x32x16_bf16 v[160:175], v[228:231], v[84:87], v[160:175]\n"
        "v_mov_b32 v151, 0\n"
        "v_mfma_f32_32x32x16_bf16 v[176:191], v[84:87], v[232:235], v[176:191]\n"
        "s_waitcnt lgkmcnt(0)\n"
        "v_mfma_f32_32x32x16_bf16 a[128:143], v[252:255], v[200:203], a[128:143]\n"
        "buffer_store_dwordx4 v[200:203], v250, s[8:11], s88 offen sc1\n"
        "s_add_u32 s88, s88, s89\n"
        "ds_write_b32 v10, v152\n"
        "v_mul_f32 v156, 0x3e8b97f6, v152\n"
        "s_nop 7\n"
        "ds_write_b32 v241, a128 offset:1024\n"
        "s_waitcnt vmcnt(14) lgkmcnt(0)\n"
        "s_barrier\n"
        "s_getpc_b64 s[86:87]\n"
        "global_load_dword a255, v240, s[86:87]\n"
        "v_add_u32 v240, 32768, v240\n"
        "v_min_u32 v240, 36864, v240\n"
        "global_load_dword a254, v240, s[86:87]\n"
        "ds_read_b128 v[208:211], v1 offset:44032\n"
        "ds_read_b128 v[212:215], v2 offset:8192\n"
        "ds_read_b128 v[216:219], v26 offset:0\n"
        "ds_read_b128 v[220:223], v1 offset:45056\n"
        "ds_read_b128 v[224:227], v2 offset:9216\n"
        "ds_read_b128 v[228:231], v26 offset:1024\n"
        "v_add_u32 v200, 1024, v242\n"
        "ds_read2_b32 v[192:193], v200 offset0:0 offset1:32\n"
        "ds_read2_b32 v[194:195], v200 offset0:64 offset1:96\n"
        "s_waitcnt lgkmcnt(7)\n"
        "v_mfma_f32_32x32x16_bf16 v[160:175], v[208:211], v[88:91], v[160:175]\n"
        "ds_read_b128 v[232:235], v1 offset:46080\n"
        "s_waitcnt lgkmcnt(7)\n"
        "v_mfma_f32_32x32x16_bf16 v[176:191], v[88:91], v[212:215], v[176:191]\n"
        "ds_read_b128 v[208:211], v2 offset:10240\n"
        "s_waitcnt lgkmcnt(7)\n"
        "v_mfma_f32_32x32x16_bf16 a[96:111], v[252:255], v[216:219], 0\n"
        "ds_read_b128 v[212:215], v26 offset:2048\n"
        "s_waitcnt lgkmcnt(7)\n"
        "v_mfma_f32_32x32x16_bf16 v[160:175], v[220:223], v[92:95], v[160:175]\n"
        "ds_read_b128 v[216:219], v1 offset:47104\n"
        "s_waitcnt lgkmcnt(7)\n"
        "v_mfma_f32_32x32x16_bf16 v[176:191], v[92:95], v[224:227], v[176:191]\n"
        "ds_read_b128 v[220:223], v2 offset:11264\n"
        "s_waitcnt lgkmcnt(5)\n"
        "v_add_f32 v192, v192, v193\n"
        "v_mfma_f32_32x32x16_bf16 a[96:111], v[252:255], v[228:231], a[96:111]\n"
        "ds_read_b128 v[224:227], v26 offset:3072\n"
        "v_add_f32 v194, v194, v195\n"
        "v_add_f32 v192, v192, v194\n"
        "s_waitcnt lgkmcnt(5)\n"
        "v_mfma_f32_32x32x16_bf16 v[160:175], v[232:235], v[96:99], v[160:175]\n"
        "ds_read_b128 v[228:231], v1 offset:48128\n"
        "v_cvt_pk_bf16_f32 v196, v192, v192\n"
        "v_and_b32 v18, 0xffff0000, v196\n"
        "s_waitcnt lgkmcnt(5)\n"
        "v_mfma_f32_32x32x16_bf16 v[176:191], v[96:99], v[208:211], v[176:191]\n"
        "ds_read_b128 v[232:235], v2 offset:12288\n"
        "v_sub_f32 v18, v192, v18\n"
        "v_cvt_pk_bf16_f32 v197, v18, 0\n"
        "s_waitcnt lgkmcnt(5)\n"
        "v_mfma_f32_32x32x16_bf16 a[96:111], v[252:255], v[212:215], a[96:111]\n"
        "ds_read_b128 v[208:211], v26 offset:4096\n"
        "v_lshl_or_b32 v197, v159, 16, v197\n"
        "v_cndmask_b32 v198, v159, 0, s[40:41]\n"
        "s_waitcnt lgkmcnt(5)\n"
        "v_mfma_f32_32x32x16_bf16 v[160:175], v[216:219], v[100:103], v[160:175]\n"
        "ds_read_b128 v[212:215], v1 offset:49152\n"
        "v_cndmask_b32 v196, v196, 0, s[40:41]\n"
        "v_cndmask_b32 v197, v197, 0, s[40:41]\n"
        "s_waitcnt lgkmcnt(5)\n"
        "v_mfma_f32_32x32x16_bf16 v[176:191], v[100:103], v[220:223], v[176:191]\n"
        "ds_read_b128 v[216:219], v2 offset:13312\n"
        "v_mov_b32 v199, 0\n"
        "buffer_store_dwordx4 v[196:199], v250, s[8:11], s95 offen sc1\n"
        "s_waitcnt lgkmcnt(5)\n"
        "v_mfma_f32_32x32x16_bf16 a[96:111], v[252:255], v[224:227], a[96:111]\n"
        "ds_read_b128 v[220:223], v26 offset:5120\n"
        "s_waitcnt lgkmcnt(5)\n"
        "v_mfma_f32_32x32x16_bf16 v[160:175], v[228:231], v[104:107], v[160:175]\n"
        "ds_read_b128 v[224:227], v1 offset:50176\n"
        "s_waitcnt lgkmcnt(5)\n"
        "v_mfma_f32_32x32x16_bf16 v[176:191], v[104:107], v[232:235], v[176:191]\n"
        "ds_read_b128 v[228:231], v2 offset:14336\n"
        "s_waitcnt lgkmcnt(5)\n"
        "v_mfma_f32_32x32x16_bf16 a[96:111], v[252:255], v[208:211], a[96:111]\n"
        "ds_read_b128 v[232:235], v26 offset:6144\n"
        "s_waitcnt lgkmcnt(5)\n"
        "v_mfma_f32_32x32x16_bf16 v[160:175], v[212:215], v[108:111], v[160:175]\n"
        "ds_read_b128 v[208:211], v1 offset:51200\n"
        "s_waitcnt lgkmcnt(5)\n"
        "v_mfma_f32_32x32x16_bf16 v[176:191], v[108:111], v[216:219], v[176:191]\n"
        "ds_read_b128 v[212:215], v2 offset:15360\n"
        "s_waitcnt lgkmcnt(5)\n"
        "v_mfma_f32_32x32x16_bf16 a[96:111], v[252:255], v[220:223], a[96:111]\n"
        "ds_read_b128 v[216:219], v26 offset:14336\n"
        "s_waitcnt lgkmcnt(5)\n"
        "v_mfma_f32_32x32x16_bf16 v[160:175], v[224:227], v[112:115], v[160:175]\n"
        "ds_read_b128 v[220:223], v1 offset:52224\n"
        "s_waitcnt lgkmcnt(5)\n"
        "v_mfma_f32_32x32x16_bf16 v[176:191], v[112:115], v[228:231], v[176:191]\n"
        "ds_read_b128 v[224:227], v2 offset:16384\n"
        "s_waitcnt lgkmcnt(5)\n"
        "v_mfma_f32_32x32x16_bf16 a[96:111], v[252:255], v[232:235], a[96:111]\n"
        "ds_read_b128 v[228:231], v26 offset:15360\n"
        "s_waitcnt lgkmcnt(5)\n"
        "v_mfma_f32_32x32x16_bf16 v[160:175], v[208:211], v[116:119], v[160:175]\n"
        "ds_read_b128 v[232:235], v1 offset:53248\n"
        "s_waitcnt lgkmcnt(5)\n"
        "v_mfma_f32_32x32x16_bf16 v[176:191], v[116:119], v[212:215], v[176:191]\n"
        "ds_read_b128 v[208:211], v2 offset:17408\n"
        "s_waitcnt lgkmcnt(5)\n"
        "v_mfma_f32_32x32x16_bf16 a[96:111], v[252:255], v[216:219], a[96:111]\n"
        "ds_read_b128 v[212:215], v26 offset:16384\n"
        "s_waitcnt lgkmcnt(5)\n"
        "v_mfma_f32_32x32x16_bf16 v[160:175], v[220:223], v[120:123], v[160:175]\n"
        "ds_read_b128 v[216:219], v1 offset:54272\n"
        "s_waitcnt lgkmcnt(5)\n"
        "v_mfma_f32_32x32x16_bf16 v[176:191], v[120:123], v[224:227], v[176:191]\n"
        "ds_read_b128 v[220:223], v2 offset:18432\n"
        "s_waitcnt lgkmcnt(5)\n"
        "v_mfma_f32_32x32x16_bf16 a[96:111], v[252:255], v[228:231], a[96:111]\n"
        "ds_read_b128 v[224:227], v26 offset:17408\n"
        "s_waitcnt lgkmcnt(5)\n"
        "v_mfma_f32_32x32x16_bf16 v[160:175], v[232:235], v[124:127], v[160:175]\n"
        "ds_read_b128 v[228:231], v1 offset:55296\n"
        "s_waitcnt lgkmcnt(5)\n"
        "v_mfma_f32_32x32x16_bf16 v[176:191], v[124:127], v[208:211], v[176:191]\n"
        "ds_read_b128 v[232:235], v2 offset:19456\n"
        "s_waitcnt lgkmcnt(5)\n"
        "v_mfma_f32_32x32x16_bf16 a[96:111], v[252:255], v[212:215], a[96:111]\n"
        "ds_read_b128 v[208:211], v26 offset:18432\n"
        "s_waitcnt lgkmcnt(5)\n"
        "v_mfma_f32_32x32x16_bf16 v[160:175], v[216:219], v[128:131], v[160:175]\n"
        "ds_read_b128 v[212:215], v1 offset:56320\n"
        "s_waitcnt lgkmcnt(5)\n"
        "v_mfma_f32_32x32x16_bf16 v[176:191], v[128:131], v[220:223], v[176:191]\n"
        "ds_read_b128 v[216:219], v2 offset:20480\n"
        "s_waitcnt lgkmcnt(5)\n"
        "v_mfma_f32_32x32x16_bf16 a[96:111], v[252:255], v[224:227], a[96:111]\n"
        "ds_read_b128 v[220:223], v26 offset:19456\n"
        "s_waitcnt lgkmcnt(5)\n"
        "v_mfma_f32_32x32x16_bf16 v[160:175], v[228:231], v[132:135], v[160:175]\n"
        "ds_read_b128 v[224:227], v1 offset:57344\n"
        "s_waitcnt vmcnt(4)\n"
        "s_waitcnt lgkmcnt(5)\n"
        "v_mfma_f32_32x32x16_bf16 v[176:191], v[132:135], v[232:235], v[176:191]\n"
        "ds_read_b128 v[228:231], v2 offset:21504\n"
        "s_waitcnt lgkmcnt(5)\n"
        "v_mfma_f32_32x32x16_bf16 a[96:111], v[252:255], v[208:211], a[96:111]\n"
        "ds_read_b128 v[232:235], v26 offset:20480\n"
        "s_waitcnt lgkmcnt(5)\n"
        "v_mfma_f32_32x32x16_bf16 v[160:175], v[212:215], v[136:139], v[160:175]\n"
        "ds_read_b128 v[208:211], v27 offset:0\n"
        "s_waitcnt lgkmcnt(5)\n"
        "v_mfma_f32_32x32x16_bf16 v[176:191], v[136:139], v[216:219], v[176:191]\n"
        "ds_read_b128 v[212:215], v27 offset:1024\n"
        "s_waitcnt lgkmcnt(5)\n"
        "v_mfma_f32_32x32x16_bf16 a[96:111], v[252:255], v[220:223], a[96:111]\n"
        "ds_read_b128 v[216:219], v27 offset:2048\n"
        "s_waitcnt lgkmcnt(5)\n"
        "v_mfma_f32_32x32x16_bf16 v[160:175], v[224:227], v[140:143], v[160:175]\n"
        "ds_read_b128 v[220:223], v27 offset:3072\n"
        "s_waitcnt lgkmcnt(5)\n"
        "v_mfma_f32_32x32x16_bf16 v[176:191], v[140:143], v[228:231], v[176:191]\n"
        "ds_read_b128 v[224:227], v27 offset:4096\n"
        "s_waitcnt lgkmcnt(5)\n"
        "v_mfma_f32_32x32x16_bf16 a[96:111], v[252:255], v[232:235], a[96:111]\n"
        "ds_read_b128 v[228:231], v27 offset:5120\n"
        "s_waitcnt lgkmcnt(5)\n"
        "v_mfma_f32_32x32x16_bf16 a[112:127], v[252:255], v[208:211], 0\n"
        "ds_read_b128 v[232:235], v27 offset:6144\n"
        "s_waitcnt lgkmcnt(5)\n"
        "v_mfma_f32_32x32x16_bf16 a[112:127], v[252:255], v[212:215], a[112:127]\n"
        "s_waitcnt lgkmcnt(4)\n"
        "v_mfma_f32_32x32x16_bf16 a[112:127], v[252:255], v[216:219], a[112:127]\n"
        "s_waitcnt lgkmcnt(3)\n"
        "v_mfma_f32_32x32x16_bf16 a[112:127], v[252:255], v[220:223], a[112:127]\n"
        "s_waitcnt lgkmcnt(2)\n"
        "v_mfma_f32_32x32x16_bf16 a[112:127], v[252:255], v[224:227], a[112:127]\n"
        "s_waitcnt lgkmcnt(1)\n"
        "v_mfma_f32_32x32x16_bf16 a[112:127], v[252:255], v[228:231], a[112:127]\n"
        "s_waitcnt lgkmcnt(0)\n"
        "v_mfma_f32_32x32x16_bf16 a[112:127], v[252:255], v[232:235], a[112:127]\n"
        "ds_write_b32 v241, a96 offset:0\n"
        "s_nop 7\n"
        "s_nop 2\n"
        "ds_write_b32 v241, a112 offset:512\n"
        "s_waitcnt lgkmcnt(0)\n"
        "s_barrier\n"
        "ds_read2_b32 v[144:145], v242 offset0:0 offset1:32\n"
        "ds_read2_b32 v[146:147], v242 offset0:64 offset1:96\n"
        "ds_read2_b32 v[252:253], v242 offset0:128 offset1:160\n"
        "ds_read2_b32 v[254:255], v242 offset0:192 offset1:224\n"
        "ds_read_b128 v[208:211], v1 offset:0\n"
        "ds_read_b128 v[212:215], v1 offset:1024\n"
        "ds_read_b128 v[216:219], v1 offset:2048\n"
        "ds_read_b128 v[220:223], v1 offset:3072\n"
        "ds_read_b128 v[224:227], v1 offset:4096\n"
        "ds_read_b128 v[228:231], v1 offset:5120\n"
        "s_waitcnt lgkmcnt(8)\n"
        "v_add_f32 v144, v144, v145\n"
        "v_add_f32 v146, v146, v147\n"
        "v_cvt_pk_bf16_f32 v240, v144, v144\n"
        "v_and_b32 v18, 0xffff0000, v240\n"
        "v_sub_f32 v18, v144, v18\n"
        "v_cvt_pk_bf16_f32 v241, v18, 0\n"
        "v_mov_b32 v242, 0\n"
        "v_cndmask_b32 v240, v240, 0, s[40:41]\n"
        "v_cndmask_b32 v241, v241, 0, s[40:41]\n"
        "v_mov_b32 v243, 0\n"
        "s_nop 1\n"
        "v_mfma_f32_32x32x16_bf16 v[160:175], v[240:243], v[148:151], v[160:175]\n"
        "v_cvt_pk_bf16_f32 v244, v146, v146\n"
        "v_and_b32 v18, 0xffff0000, v244\n"
        "v_sub_f32 v18, v146, v18\n"
        "v_cvt_pk_bf16_f32 v245, v18, 0\n"
        "v_mov_b32 v246, 0\n"
        "v_cndmask_b32 v244, v244, 0, s[40:41]\n"
        "v_cndmask_b32 v245, v245, 0, s[40:41]\n"
        "v_mov_b32 v247, 0\n"
        "s_nop 1\n"
        "v_mfma_f32_32x32x16_bf16 v[176:191], v[148:151], v[244:247], v[176:191]\n"
        "ds_read_b128 v[16:19], v11 offset:0\n"
        "ds_read_b128 v[20:23], v11 offset:32\n"
        "ds_read_b128 v[24:27], v11 offset:64\n"
        "ds_read_b128 v[28:31], v11 offset:96\n"
        "v_pk_mul_f32 v[160:161], v[160:161], v[152:153] op_sel_hi:[1,0]\n"
        "v_pk_mul_f32 v[162:163], v[162:163], v[152:153] op_sel_hi:[1,0]\n"
        "v_pk_mul_f32 v[164:165], v[164:165], v[152:153] op_sel_hi:[1,0]\n"
        "v_pk_mul_f32 v[166:167], v[166:167], v[152:153] op_sel_hi:[1,0]\n"
        "v_cvt_pk_bf16_f32 v160, v160, v161\n"
        "v_cvt_pk_bf16_f32 v161, v162, v163\n"
        "v_cvt_pk_bf16_f32 v162, v164, v165\n"
        "v_cvt_pk_bf16_f32 v163, v166, v167\n"
        "buffer_store_dwordx4 v[160:163], v1, s[8:11], s34 offen offset:0 sc1\n"
        "v_pk_mul_f32 v[168:169], v[168:169], v[152:153] op_sel_hi:[1,0]\n"
        "v_pk_mul_f32 v[170:171], v[170:171], v[152:153] op_sel_hi:[1,0]\n"
        "v_pk_mul_f32 v[172:173], v[172:173], v[152:153] op_sel_hi:[1,0]\n"
        "v_pk_mul_f32 v[174:175], v[174:175], v[152:153] op_sel_hi:[1,0]\n"
        "v_cvt_pk_bf16_f32 v164, v168, v169\n"
        "v_cvt_pk_bf16_f32 v165, v170, v171\n"
        "v_cvt_pk_bf16_f32 v166, v172, v173\n"
        "v_cvt_pk_bf16_f32 v167, v174, v175\n"
        "v_mov_b32 v154, 0x3f80\n"
        "v_cndmask_b32 v166, v166, v154, s[40:41]\n"
        "v_cndmask_b32 v167, v167, 0, s[40:41]\n"
        "buffer_store_dwordx4 v[164:167], v1, s[8:11], s34 offen offset:1024 sc1\n"
        "v_mov_b32 v154, 0x3f803f80\n"
        "v_cndmask_b32 v154, 0, v154, s[42:43]\n"
        "s_waitcnt lgkmcnt(0)\n"
        "v_pk_mul_f32 v[176:177], v[176:177], v[16:17]\n"
        "v_pk_mul_f32 v[178:179], v[178:179], v[18:19]\n"
        "v_pk_mul_f32 v[180:181], v[180:181], v[20:21]\n"
        "v_pk_mul_f32 v[182:183], v[182:183], v[22:23]\n"
        "v_cvt_pk_bf16_f32 v168, v176, v177\n"
        "v_cvt_pk_bf16_f32 v169, v178, v179\n"
        "v_cvt_pk_bf16_f32 v170, v180, v181\n"
        "v_cvt_pk_bf16_f32 v171, v182, v183\n"
        "v_cndmask_b32 v168, v168, v154, s[76:77]\n"
        "v_cndmask_b32 v169, v169, v154, s[76:77]\n"
        "v_cndmask_b32 v170, v170, v154, s[76:77]\n"
        "v_cndmask_b32 v171, v171, v154, s[76:77]\n"
        "buffer_store_dwordx4 v[168:171], v1, s[8:11], s34 offen offset:2048 sc1\n"
        "v_pk_mul_f32 v[184:185], v[184:185], v[24:25]\n"
        "v_pk_mul_f32 v[186:187], v[186:187], v[26:27]\n"
        "v_pk_mul_f32 v[188:189], v[188:189], v[28:29]\n"
        "v_pk_mul_f32 v[190:191], v[190:191], v[30:31]\n"
        "v_cvt_pk_bf16_f32 v172, v184, v185\n"
        "v_cvt_pk_bf16_f32 v173, v186, v187\n"
        "v_cvt_pk_bf16_f32 v174, v188, v189\n"
        "v_cvt_pk_bf16_f32 v175, v190, v191\n"
        "v_cndmask_b32 v172, v172, v154, s[76:77]\n"
        "v_cndmask_b32 v173, v173, v154, s[76:77]\n"
        "v_cndmask_b32 v174, v174, v154, s[76:77]\n"
        "v_cndmask_b32 v175, v175, v154, s[76:77]\n"
        "buffer_store_dwordx4 v[172:175], v1, s[8:11], s34 offen offset:3072 sc1\n"
        "v_mfma_f32_32x32x16_bf16 v[192:207], v[208:211], v[32:35], 0\n"
        "ds_read_b128 v[232:235], v1 offset:6144\n"
        "v_accvgpr_write_b32 a240, v240\n"
        "v_accvgpr_write_b32 a241, v241\n"
        "v_accvgpr_write_b32 a242, v242\n"
        "v_mfma_f32_32x32x16_bf16 v[192:207], v[212:215], v[36:39], v[192:207]\n"
        "ds_read_b128 v[208:211], v1 offset:7168\n"
        "v_accvgpr_write_b32 a243, v243\n"
        "v_accvgpr_write_b32 a244, v244\n"
        "v_accvgpr_write_b32 a245, v245\n"
        "v_mfma_f32_32x32x16_bf16 v[192:207], v[216:219], v[40:43], v[192:207]\n"
        "ds_read_b128 v[212:215], v1 offset:8192\n"
        "v_accvgpr_write_b32 a246, v246\n"
        "v_accvgpr_write_b32 a247, v247\n"
        "v_mfma_f32_32x32x16_bf16 v[192:207], v[220:223], v[44:47], v[192:207]\n"
        "ds_read_b128 v[216:219], v1 offset:9216\n"
        "v_add_f32 v252, v252, v253\n"
        "v_add_f32 v254, v254, v255\n"
        "v_add_f32 v252, v252, v254\n"
        "v_mfma_f32_32x32x16_bf16 v[192:207], v[224:227], v[48:51], v[192:207]\n"
        "ds_read_b128 v[220:223], v1 offset:10240\n"
        "v_cvt_pk_bf16_f32 v236, v252, v252\n"
        "v_and_b32 v18, 0xffff0000, v236\n"
        "v_sub_f32 v18, v252, v18\n"
        "v_mfma_f32_32x32x16_bf16 v[192:207], v[228:231], v[52:55], v[192:207]\n"
        "ds_read_b128 v[224:227], v1 offset:11264\n"
        "v_cvt_pk_bf16_f32 v237, v18, 0\n"
        "v_mov_b32 v238, 0\n"
        "v_cndmask_b32 v236, v236, 0, s[40:41]\n"
        "s_waitcnt lgkmcnt(5)\n"
        "v_mfma_f32_32x32x16_bf16 v[192:207], v[232:235], v[56:59], v[192:207]\n"
        "ds_read_b128 v[228:231], v1 offset:12288\n"
        "v_cndmask_b32 v237, v237, 0, s[40:41]\n"
        "v_mov_b32 v239, 0\n"
        "v_accvgpr_write_b32 a248, v236\n"
        "s_waitcnt lgkmcnt(5)\n"
        "v_mfma_f32_32x32x16_bf16 v[192:207], v[208:211], v[60:63], v[192:207]\n"
        "ds_read_b128 v[232:235], v1 offset:13312\n"
        "v_accvgpr_write_b32 a249, v237\n"
        "v_accvgpr_write_b32 a250, v238\n"
        "v_accvgpr_write_b32 a251, v239\n"
        "s_waitcnt lgkmcnt(5)\n"
        "v_mfma_f32_32x32x16_bf16 v[192:207], v[212:215], v[64:67], v[192:207]\n"
        "ds_read_b128 v[208:211], v1 offset:14336\n"
        "s_waitcnt lgkmcnt(5)\n"
        "v_mfma_f32_32x32x16_bf16 v[192:207], v[216:219], v[68:71], v[192:207]\n"
        "ds_read_b128 v[212:215], v1 offset:15360\n"
        "s_waitcnt lgkmcnt(5)\n"
        "v_mfma_f32_32x32x16_bf16 v[192:207], v[220:223], v[72:75], v[192:207]\n"
        "ds_read_b128 v[216:219], v1 offset:16384\n"
        "s_waitcnt lgkmcnt(5)\n"
        "v_mfma_f32_32x32x16_bf16 v[192:207], v[224:227], v[76:79], v[192:207]\n"
        "ds_read_b128 v[220:223], v1 offset:17408\n"
        "s_waitcnt lgkmcnt(5)\n"
        "v_mfma_f32_32x32x16_bf16 v[192:207], v[228:231], v[80:83], v[192:207]\n"
        "ds_read_b128 v[224:227], v1 offset:18432\n"
        "s_waitcnt lgkmcnt(5)\n"
        "v_mfma_f32_32x32x16_bf16 v[192:207], v[232:235], v[84:87], v[192:207]\n"
        "ds_read_b128 v[228:231], v1 offset:19456\n"
        "s_waitcnt lgkmcnt(5)\n"
        "v_mfma_f32_32x32x16_bf16 v[192:207], v[208:211], v[88:91], v[192:207]\n"
        "ds_read_b128 v[232:235], v1 offset:20480\n"
        "s_waitcnt lgkmcnt(5)\n"
        "v_mfma_f32_32x32x16_bf16 v[192:207], v[212:215], v[92:95], v[192:207]\n"
        "ds_read_b128 v[208:211], v1 offset:21504\n"
        "s_waitcnt lgkmcnt(5)\n"
        "v_mfma_f32_32x32x16_bf16 v[192:207], v[216:219], v[96:99], v[192:207]\n"
        "ds_read_b128 v[212:215], v1 offset:22528\n"
        "s_waitcnt lgkmcnt(5)\n"
        "v_mfma_f32_32x32x16_bf16 v[192:207], v[220:223], v[100:103], v[192:207]\n"
        "ds_read_b128 v[216:219], v1 offset:23552\n"
        "s_waitcnt lgkmcnt(5)\n"
        "v_mfma_f32_32x32x16_bf16 v[192:207], v[224:227], v[104:107], v[192:207]\n"
        "ds_read_b128 v[220:223], v1 offset:24576\n"
        "s_waitcnt lgkmcnt(5)\n"
        "v_mfma_f32_32x32x16_bf16 v[192:207], v[228:231], v[108:111], v[192:207]\n"
        "ds_read_b128 v[224:227], v1 offset:25600\n"
        "s_waitcnt lgkmcnt(5)\n"
        "v_mfma_f32_32x32x16_bf16 v[192:207], v[232:235], v[112:115], v[192:207]\n"
        "ds_read_b128 v[228:231], v1 offset:26624\n"
        "s_waitcnt lgkmcnt(5)\n"
        "v_mfma_f32_32x32x16_bf16 v[192:207], v[208:211], v[116:119], v[192:207]\n"
        "ds_read_b128 v[232:235], v1 offset:27648\n"
        "s_waitcnt lgkmcnt(5)\n"
        "v_mfma_f32_32x32x16_bf16 v[192:207], v[212:215], v[120:123], v[192:207]\n"
        "s_waitcnt lgkmcnt(4)\n"
        "v_mfma_f32_32x32x16_bf16 v[192:207], v[216:219], v[124:127], v[192:207]\n"
        "s_waitcnt vmcnt(0)\n"
        "v_mov_b32 v16, s96\n"
        "v_mov_b32 v17, s97\n"
        "v_mov_b32 v18, s96\n"
        "v_mov_b32 v19, s97\n"
        "s_mov_b64 exec, 0xff\n"
        "buffer_store_dwordx4 v[16:19], v1, s[8:11], s35 offen sc1\n"
        "s_mov_b64 exec, -1\n"
        "s_waitcnt lgkmcnt(3)\n"
        "v_mfma_f32_32x32x16_bf16 v[192:207], v[220:223], v[128:131], v[192:207]\n"
        "s_waitcnt lgkmcnt(2)\n"
        "v_mfma_f32_32x32x16_bf16 v[192:207], v[224:227], v[132:135], v[192:207]\n"
        "s_waitcnt lgkmcnt(1)\n"
        "v_mfma_f32_32x32x16_bf16 v[192:207], v[228:231], v[136:139], v[192:207]\n"
        "s_waitcnt lgkmcnt(0)\n"
        "v_mfma_f32_32x32x16_bf16 v[192:207], v[232:235], v[140:143], v[192:207]\n"
        "v_mfma_f32_32x32x16_bf16 v[192:207], v[236:239], v[148:151], v[192:207]\n"
        "s_nop 7\n"
        "s_nop 3\n"
        "v_pk_mul_f32 v[192:193], v[192:193], v[156:157] op_sel_hi:[1,0]\n"
        "v_pk_mul_f32 v[194:195], v[194:195], v[156:157] op_sel_hi:[1,0]\n"
        "v_pk_mul_f32 v[196:197], v[196:197], v[156:157] op_sel_hi:[1,0]\n"
        "v_pk_mul_f32 v[198:199], v[198:199], v[156:157] op_sel_hi:[1,0]\n"
        "v_pk_mul_f32 v[200:201], v[200:201], v[156:157] op_sel_hi:[1,0]\n"
        "v_pk_mul_f32 v[202:203], v[202:203], v[156:157] op_sel_hi:[1,0]\n"
        "v_pk_mul_f32 v[204:205], v[204:205], v[156:157] op_sel_hi:[1,0]\n"
        "v_pk_mul_f32 v[206:207], v[206:207], v[156:157] op_sel_hi:[1,0]\n"
        "v_cvt_pk_bf16_f32 v192, v192, v193\n"
        "v_cvt_pk_bf16_f32 v193, v194, v195\n"
        "v_cvt_pk_bf16_f32 v194, v196, v197\n"
        "v_cvt_pk_bf16_f32 v195, v198, v199\n"
        "v_cvt_pk_bf16_f32 v196, v200, v201\n"
        "v_cvt_pk_bf16_f32 v197, v202, v203\n"
        "v_cvt_pk_bf16_f32 v198, v204, v205\n"
        "v_cvt_pk_bf16_f32 v199, v206, v207\n"
        "v_cndmask_b32 v198, v198, 0, s[40:41]\n"
        "v_cndmask_b32 v199, v199, 0, s[40:41]\n"
        "s_lshl_b32 s36, s24, 11\n"
        "v_add_u32 v0, s36, v3\n"
        "ds_write_b128 v0, v[192:195] offset:24576\n"
        "ds_write_b128 v0, v[196:199] offset:25600\n"
        ".Llayer:\n"
        "s_xor_b32 s33, s24, 1\n"
        "s_lshl_b32 s34, s33, 11\n"
        "v_add_u32 v17, s34, v3\n"
        "s_mul_i32 s34, s33, 12288\n"
        "v_add_u32 v21, s34, v2\n"
        "s_xor_b32 s33, s24, 2\n"
        "s_lshl_b32 s34, s33, 11\n"
        "v_add_u32 v18, s34, v3\n"
        "s_mul_i32 s34, s33, 12288\n"
        "v_add_u32 v22, s34, v2\n"
        "s_xor_b32 s33, s24, 3\n"
        "s_lshl_b32 s34, s33, 11\n"
        "v_add_u32 v19, s34, v3\n"
        "s_mul_i32 s34, s33, 12288\n"
        "v_add_u32 v23, s34, v2\n"
        "s_waitcnt lgkmcnt(0)\n"
        "s_barrier\n"
        "ds_read_b128 v[40:43], v17 offset:24576\n"
        "ds_read_b128 v[44:47], v17 offset:25600\n"
        "ds_read_b128 v[48:51], v18 offset:24576\n"
        "ds_read_b128 v[52:55], v18 offset:25600\n"
        "ds_read_b128 v[56:59], v19 offset:24576\n"
        "ds_read_b128 v[60:63], v19 offset:25600\n"
        "ds_read_b128 v[32:35], v0 offset:24576\n"
        "ds_read_b128 v[36:39], v0 offset:25600\n"
        "s_mul_i32 s34, s24, 12288\n"
        "v_add_u32 v20, s34, v2\n"
        "s_mul_i32 s33, s26, 262144\n"
        "s_mul_i32 s39, s28, 4194304\n"
        "s_add_u32 s33, s33, s39\n"
        "s_add_u32 s33, s33, s47\n"
        "s_add_u32 s60, s80, s33\n"
        "s_addc_u32 s21, s81, 0\n"
        "s_lshl_b32 s51, s27, 12\n"
        "s_mul_i32 s34, s24, 26624\n"
        "s_min_u32 s34, s34, 77824\n"
        "s_mov_b32 m0, s34\n"
        "s_mul_i32 s52, s26, 137216\n"
        "s_add_u32 s52, s52, s34\n"
        "s_add_u32 s52, s52, 3000320\n"
        "s_add_u32 s22, s80, s52\n"
        "s_addc_u32 s23, s81, 0\n"
        "s_mul_i32 s35, s26, 8192\n"
        "s_mul_i32 s39, s24, 2048\n"
        "s_add_u32 s35, s35, s39\n"
        "s_add_u32 s35, s35, s29\n"
        "s_add_u32 s35, s35, 2048\n"
        "s_and_b32 s31, s24, 1\n"
        "s_mul_i32 s36, s46, 229376\n"
        "s_mul_i32 s39, s24, 57344\n"
        "s_add_u32 s36, s36, s39\n"
        "s_mul_i32 s39, s31, 28672\n"
        "s_add_u32 s36, s36, s39\n"
        "s_add_u32 s36, s36, 5195776\n"
        "s_add_u32 s62, s80, s36\n"
        "s_addc_u32 s63, s81, 0\n"
        "s_cmp_eq_u32 s31, 0\n"
        "s_cselect_b32 s75, 0, -1\n"
        "s_cselect_b32 s61, 0, -57344\n"
        "s_add_u32 s61, s61, 4096\n"
        "s_waitcnt lgkmcnt(0)\n"
        "v_mfma_f32_32x32x16_bf16 v[64:79], v[160:163], v[40:43], 0\n"
        "v_mov_b32 v96, 0\n"
        "v_mov_b32 v97, 0\n"
        "v_mov_b32 v98, 0\n"
        "v_mov_b32 v99, 0\n"
        "v_mov_b32 v100, 0\n"
        "v_mov_b32 v101, 0\n"
        "v_mfma_f32_32x32x16_bf16 v[64:79], v[164:167], v[44:47], v[64:79]\n"
        "v_mov_b32 v102, 0\n"
        "v_mov_b32 v103, 0\n"
        "v_mov_b32 v104, 0\n"
        "v_mov_b32 v105, 0\n"
        "v_mov_b32 v106, 0\n"
        "v_mov_b32 v107, 0\n"
        "v_mfma_f32_32x32x16_bf16 v[80:95], v[160:163], v[48:51], 0\n"
        "v_mov_b32 v108, 0\n"
        "v_mov_b32 v109, 0\n"
        "v_mov_b32 v110, 0\n"
        "v_mov_b32 v111, 0\n"
        "v_mov_b32 v112, 0\n"
        "v_mov_b32 v113, 0\n"
        "v_mfma_f32_32x32x16_bf16 v[80:95], v[164:167], v[52:55], v[80:95]\n"
        "v_mov_b32 v114, 0\n"
        "v_mov_b32 v115, 0\n"
        "v_mov_b32 v116, 0\n"
        "v_mov_b32 v117, 0\n"
        "v_mov_b32 v118, 0\n"
        "v_mov_b32 v119, 0\n"
        "v_mov_b32 v120, 0\n"
        "v_mov_b32 v121, 0\n"
        "v_mov_b32 v122, 0\n"
        "v_mov_b32 v123, 0\n"
        "v_mov_b32 v124, 0\n"
        "v_mov_b32 v125, 0\n"
        "v_mov_b32 v126, 0\n"
        "v_mov_b32 v127, 0\n"
        "v_mov_b32 v128, 0\n"
        "v_mov_b32 v129, 0\n"
        "v_mov_b32 v130, 0\n"
        "v_mov_b32 v131, 0\n"
        "v_max3_f32 v18, v64, v65, v66\n"
        "v_max3_f32 v19, v67, v68, v69\n"
        "v_max3_f32 v18, v18, v70, v71\n"
        "v_max3_f32 v19, v19, v72, v73\n"
        "v_max3_f32 v18, v18, v74, v75\n"
        "v_max3_f32 v19, v19, v76, v77\n"
        "v_max3_f32 v18, v18, v78, v79\n"
        "v_max_f32 v18, v18, v19\n"
        "v_mov_b32 v19, v18\n"
        "s_nop 1\n"
        "v_permlane32_swap_b32 v18, v19\n"
        "v_max_f32 v18, v18, v19\n"
        "v_cvt_pk_bf16_f32 v18, -v18, 0\n"
        "v_cndmask_b32 v46, v46, v18, s[40:41]\n"
        "v_lshlrev_b32 v243, 16, v18\n"
        "v_max3_f32 v18, v80, v81, v82\n"
        "v_max3_f32 v19, v83, v84, v85\n"
        "v_max3_f32 v18, v18, v86, v87\n"
        "v_max3_f32 v19, v19, v88, v89\n"
        "v_max3_f32 v18, v18, v90, v91\n"
        "v_max3_f32 v19, v19, v92, v93\n"
        "v_max3_f32 v18, v18, v94, v95\n"
        "v_max_f32 v18, v18, v19\n"
        "v_mov_b32 v19, v18\n"
        "s_nop 1\n"
        "v_permlane32_swap_b32 v18, v19\n"
        "v_max_f32 v18, v18, v19\n"
        "v_cvt_pk_bf16_f32 v18, -v18, 0\n"
        "v_cndmask_b32 v54, v54, v18, s[40:41]\n"
        "v_lshlrev_b32 v244, 16, v18\n"
        "v_mfma_f32_32x32x16_bf16 v[64:79], v[160:163], v[56:59], 0\n"
        "v_mov_b32 v132, 0\n"
        "v_mov_b32 v133, 0\n"
        "v_mov_b32 v134, 0\n"
        "v_mov_b32 v135, 0\n"
        "v_mov_b32 v136, 0\n"
        "v_mov_b32 v137, 0\n"
        "v_mfma_f32_32x32x16_bf16 v[64:79], v[164:167], v[60:63], v[64:79]\n"
        "v_mov_b32 v138, 0\n"
        "v_mov_b32 v139, 0\n"
        "v_mov_b32 v140, 0\n"
        "v_mov_b32 v141, 0\n"
        "v_mov_b32 v142, 0\n"
        "v_mov_b32 v143, 0\n"
        "v_mfma_f32_32x32x16_bf16 v[80:95], v[160:163], v[32:35], 0\n"
        "v_mov_b32 v144, 0\n"
        "v_mov_b32 v145, 0\n"
        "v_mov_b32 v146, 0\n"
        "v_mov_b32 v147, 0\n"
        "v_mov_b32 v148, 0\n"
        "v_mov_b32 v149, 0\n"
        "v_mfma_f32_32x32x16_bf16 v[80:95], v[164:167], v[36:39], v[80:95]\n"
        "v_mov_b32 v150, 0\n"
        "v_mov_b32 v151, 0\n"
        "v_mov_b32 v152, 0\n"
        "v_mov_b32 v153, 0\n"
        "v_mov_b32 v154, 0\n"
        "v_mov_b32 v155, 0\n"
        "v_mov_b32 v156, 0\n"
        "v_mov_b32 v157, 0\n"
        "v_mov_b32 v158, 0\n"
        "v_mov_b32 v159, 0\n"
        "v_mov_b32 v236, 0\n"
        "v_mov_b32 v237, 0\n"
        "v_mov_b32 v238, 0\n"
        "v_mov_b32 v239, 0\n"
        "v_mov_b32 v220, 0\n"
        "v_mov_b32 v221, 0\n"
        "v_mov_b32 v222, 0\n"
        "v_mov_b32 v223, 0\n"
        "v_max3_f32 v18, v64, v65, v66\n"
        "v_max3_f32 v19, v67, v68, v69\n"
        "v_max3_f32 v18, v18, v70, v71\n"
        "v_max3_f32 v19, v19, v72, v73\n"
        "v_max3_f32 v18, v18, v74, v75\n"
        "v_max3_f32 v19, v19, v76, v77\n"
        "v_max3_f32 v18, v18, v78, v79\n"
        "v_max_f32 v18, v18, v19\n"
        "v_mov_b32 v19, v18\n"
        "s_nop 1\n"
        "v_permlane32_swap_b32 v18, v19\n"
        "v_max_f32 v18, v18, v19\n"
        "v_cvt_pk_bf16_f32 v18, -v18, 0\n"
        "v_cndmask_b32 v62, v62, v18, s[40:41]\n"
        "v_lshlrev_b32 v245, 16, v18\n"
        "v_max3_f32 v18, v80, v81, v82\n"
        "v_max3_f32 v19, v83, v84, v85\n"
        "v_max3_f32 v18, v18, v86, v87\n"
        "v_max3_f32 v19, v19, v88, v89\n"
        "v_max3_f32 v18, v18, v90, v91\n"
        "v_max3_f32 v19, v19, v92, v93\n"
        "v_max3_f32 v18, v18, v94, v95\n"
        "v_max_f32 v18, v18, v19\n"
        "v_mov_b32 v19, v18\n"
        "s_nop 1\n"
        "v_permlane32_swap_b32 v18, v19\n"
        "v_max_f32 v18, v18, v19\n"
        "v_cvt_pk_bf16_f32 v18, -v18, 0\n"
        "v_cndmask_b32 v38, v38, v18, s[40:41]\n"
        "v_lshlrev_b32 v242, 16, v18\n"
        "v_mfma_f32_32x32x16_bf16 v[64:79], v[160:163], v[40:43], 0\n"
        "v_mfma_f32_32x32x16_bf16 v[64:79], v[164:167], v[44:47], v[64:79]\n"
        "s_mov_b32 s44, 0x20000\n"
        "s_sleep 1\n"
        "buffer_load_dwordx2 v[16:17], v5, s[8:11], s35 offen sc1\n"
        "v_lshrrev_b32 v18, 1, v8\n"
        "s_xor_b32 s30, s24, 1\n"
        "s_mul_i32 s30, s30, 384\n"
        "s_add_u32 s30, s30, 153600\n"
        "v_add_u32 v25, s30, v18\n"
        "ds_write_b32 v25, v243\n"
        "s_xor_b32 s30, s24, 2\n"
        "s_mul_i32 s30, s30, 384\n"
        "s_add_u32 s30, s30, 153728\n"
        "v_add_u32 v26, s30, v18\n"
        "ds_write_b32 v26, v244\n"
        "s_xor_b32 s30, s24, 3\n"
        "s_mul_i32 s30, s30, 384\n"
        "s_add_u32 s30, s30, 153856\n"
        "v_add_u32 v27, s30, v18\n"
        "ds_write_b32 v27, v245\n"
        "s_mul_i32 s30, s24, 384\n"
        "s_add_u32 s30, s30, 153600\n"
        "v_add_u32 v24, s30, v18\n"
        "v_mov_b32 v18, 155136\n"
        "v_mov_b32 v19, 0\n"
        "ds_write_b32 v18, v19\n"
        "s_mul_i32 s36, s26, 2048\n"
        "s_add_u32 s36, s36, s53\n"
        "s_mul_i32 s37, s27, 2048\n"
        "s_mul_i32 s39, s26, 128\n"
        "s_add_u32 s37, s37, s39\n"
        "s_add_u32 s37, s37, s29\n"
        "s_add_u32 s37, s37, 133120\n"
        "s_mov_b32 s30, 0x53800000\n"
        "s_mov_b32 s31, 0x2b800000\n"
        "s_add_u32 s51, s51, 0x4000\n"
        "s_and_b32 s51, s51, 0x3ffff\n"
        "s_or_b32 s20, s51, s60\n"
        "v_lshrrev_b32 v28, 7, v1\n"
        "v_min_u32 v29, 3, v28\n"
        "v_lshlrev_b32 v29, 17, v29\n"
        "v_add_u32 v29, s37, v29\n"
        "s_mul_i32 s32, s48, 2048\n"
        "s_mul_i32 s39, s49, 128\n"
        "s_add_u32 s32, s32, s39\n"
        "s_add_u32 s32, s32, 657408\n"
        "s_cmp_lt_u32 s26, 14\n"
        "s_cselect_b32 s32, s32, 1073741824\n"
        "s_mul_i32 s33, s26, 8192\n"
        "s_mul_i32 s39, s24, 2048\n"
        "s_add_u32 s33, s33, s39\n"
        "s_mul_i32 s39, s25, 128\n"
        "s_add_u32 s33, s33, s39\n"
        "s_add_u32 s33, s33, 788480\n"
        "s_cmp_eq_u32 s28, 0\n"
        "s_cselect_b32 s32, s32, 1073741824\n"
        "s_cselect_b32 s33, s33, 1073741824\n"
        "v_mov_b32 v30, s32\n"
        "v_cmp_eq_u32 vcc, 4, v28\n"
        "s_nop 1\n"
        "v_cndmask_b32 v29, v29, v30, vcc\n"
        "v_mov_b32 v30, s33\n"
        "v_cmp_eq_u32 vcc, 5, v28\n"
        "s_nop 1\n"
        "v_cndmask_b32 v29, v29, v30, vcc\n"
        "v_and_b32 v28, 0x70, v1\n"
        "v_add_u32 v29, v29, v28\n"
        "s_sub_u32 s32, s96, 1\n"
        "v_mov_b32 v224, s32\n"
        "v_mov_b32 v225, s32\n"
        "v_mov_b32 v226, s32\n"
        "v_mov_b32 v227, s32\n"
        "buffer_store_dwordx4 v[224:227], v29, s[8:11], 0 offen sc1\n"
        "s_waitcnt lgkmcnt(0)\n"
        "s_barrier\n"
        "ds_read2_b32 v[30:31], v24 offset0:32 offset1:64\n"
        "ds_read_b32 v29, v24\n"
        "s_waitcnt lgkmcnt(0)\n"
        "v_min3_f32 v28, v242, v29, v30\n"
        "v_min_f32 v28, v28, v31\n"
        "v_sub_f32 v248, v28, v242\n"
        "v_sub_f32 v250, v28, v29\n"
        "v_sub_f32 v252, v28, v30\n"
        "v_sub_f32 v254, v28, v31\n"
        "v_exp_f32 v248, v248\n"
        "v_exp_f32 v250, v250\n"
        "v_exp_f32 v252, v252\n"
        "v_exp_f32 v254, v254\n"
        "s_waitcnt vmcnt(1)\n"
        "v_cmp_ne_u64 vcc, s[96:97], v[16:17]\n"
        "s_cbranch_vccz .Lpolled4\n"
        ".Lpoll3:\n"
        "buffer_load_dwordx2 v[16:17], v5, s[8:11], s35 offen sc1\n"
        "s_waitcnt vmcnt(0)\n"
        "v_cmp_ne_u64 vcc, s[96:97], v[16:17]\n"
        "s_cbranch_vccz .Lpolled4\n"
        "s_sub_u32 s44, s44, 1\n"
        "s_cbranch_scc0 .Lpoll3\n"
        ".Lpolled4:\n"
        "global_load_dwordx4 v[176:179], v1, s[20:21] offset:0 sc1\n"
        "global_load_dwordx4 v[180:183], v1, s[20:21] offset:1024 sc1\n"
        "global_load_dwordx4 v[184:187], v1, s[20:21] offset:2048 sc1\n"
        "global_load_dwordx4 v[188:191], v1, s[20:21] offset:3072 sc1\n"
        "s_add_u32 s51, s51, 0x4000\n"
        "s_and_b32 s51, s51, 0x3ffff\n"
        "s_or_b32 s20, s51, s60\n"
        "v_mfma_f32_32x32x16_bf16 v[80:95], v[160:163], v[48:51], 0\n"
        "global_load_dwordx4 v[192:195], v1, s[20:21] offset:0 sc1\n"
        "v_exp_f32 v64, v64\n"
        "v_exp_f32 v65, v65\n"
        "v_exp_f32 v66, v66\n"
        "v_exp_f32 v67, v67\n"
        "v_cvt_pk_bf16_f32 v224, v64, v65\n"
        "v_cvt_pk_bf16_f32 v225, v66, v67\n"
        "v_mfma_f32_32x32x16_bf16 v[80:95], v[164:167], v[52:55], v[80:95]\n"
        "s_waitcnt vmcnt(63)\n"
        "v_exp_f32 v68, v68\n"
        "v_exp_f32 v69, v69\n"
        "v_exp_f32 v70, v70\n"
        "v_exp_f32 v71, v71\n"
        "v_cvt_pk_bf16_f32 v226, v68, v69\n"
        "v_cvt_pk_bf16_f32 v227, v70, v71\n"
        "v_mfma_f32_32x32x16_bf16 v[96:111], v[220:223], v[236:239], v[96:111]\n"
        "global_load_dwordx4 v[196:199], v1, s[20:21] offset:1024 sc1\n"
        "v_exp_f32 v72, v72\n"
        "v_exp_f32 v73, v73\n"
        "v_exp_f32 v74, v74\n"
        "v_exp_f32 v75, v75\n"
        "v_cvt_pk_bf16_f32 v228, v72, v73\n"
        "v_cvt_pk_bf16_f32 v229, v74, v75\n"
        "v_mfma_f32_32x32x16_bf16 v[112:127], v[168:171], v[224:227], v[112:127]\n"
        "v_exp_f32 v76, v76\n"
        "v_exp_f32 v77, v77\n"
        "v_exp_f32 v78, v78\n"
        "v_exp_f32 v79, v79\n"
        "v_cvt_pk_bf16_f32 v230, v76, v77\n"
        "v_cvt_pk_bf16_f32 v231, v78, v79\n"
        "v_mfma_f32_32x32x16_bf16 v[64:79], v[160:163], v[56:59], 0\n"
        "global_load_dwordx4 v[200:203], v1, s[20:21] offset:2048 sc1\n"
        "v_exp_f32 v80, v80\n"
        "v_exp_f32 v81, v81\n"
        "v_exp_f32 v82, v82\n"
        "v_exp_f32 v83, v83\n"
        "v_cvt_pk_bf16_f32 v232, v80, v81\n"
        "v_cvt_pk_bf16_f32 v233, v82, v83\n"
        "v_mfma_f32_32x32x16_bf16 v[64:79], v[164:167], v[60:63], v[64:79]\n"
        "v_exp_f32 v84, v84\n"
        "v_exp_f32 v85, v85\n"
        "v_exp_f32 v86, v86\n"
        "v_exp_f32 v87, v87\n"
        "v_cvt_pk_bf16_f32 v234, v84, v85\n"
        "v_cvt_pk_bf16_f32 v235, v86, v87\n"
        "v_mfma_f32_32x32x16_bf16 v[112:127], v[172:175], v[228:231], v[112:127]\n"
        "global_load_dwordx4 v[204:207], v1, s[20:21] offset:3072 sc1\n"
        "s_add_u32 s51, s51, 0x4000\n"
        "s_and_b32 s51, s51, 0x3ffff\n"
        "s_or_b32 s20, s51, s60\n"
        "v_exp_f32 v88, v88\n"
        "v_exp_f32 v89, v89\n"
        "v_exp_f32 v90, v90\n"
        "v_exp_f32 v91, v91\n"
        "v_cvt_pk_bf16_f32 v236, v88, v89\n"
        "v_cvt_pk_bf16_f32 v237, v90, v91\n"
        "v_mfma_f32_32x32x16_bf16 v[128:143], v[168:171], v[232:235], v[128:143]\n"
        "v_exp_f32 v92, v92\n"
        "v_exp_f32 v93, v93\n"
        "v_exp_f32 v94, v94\n"
        "v_exp_f32 v95, v95\n"
        "v_cvt_pk_bf16_f32 v238, v92, v93\n"
        "v_cvt_pk_bf16_f32 v239, v94, v95\n"
        "v_mfma_f32_32x32x16_bf16 v[80:95], v[160:163], v[32:35], 0\n"
        "global_load_dwordx4 v[208:211], v1, s[20:21] offset:0 sc1\n"
        "v_exp_f32 v64, v64\n"
        "v_exp_f32 v65, v65\n"
        "v_exp_f32 v66, v66\n"
        "v_exp_f32 v67, v67\n"
        "v_cvt_pk_bf16_f32 v224, v64, v65\n"
        "v_cvt_pk_bf16_f32 v225, v66, v67\n"
        "v_mfma_f32_32x32x16_bf16 v[80:95], v[164:167], v[36:39], v[80:95]\n"
        "s_waitcnt vmcnt(7)\n"
        "v_exp_f32 v68, v68\n"
        "v_exp_f32 v69, v69\n"
        "v_exp_f32 v70, v70\n"
        "v_exp_f32 v71, v71\n"
        "v_cvt_pk_bf16_f32 v226, v68, v69\n"
        "v_cvt_pk_bf16_f32 v227, v70, v71\n"
        "v_mfma_f32_32x32x16_bf16 v[128:143], v[172:175], v[236:239], v[128:143]\n"
        "global_load_dwordx4 v[212:215], v1, s[20:21] offset:1024 sc1\n"
        "v_exp_f32 v72, v72\n"
        "v_exp_f32 v73, v73\n"
        "v_exp_f32 v74, v74\n"
        "v_exp_f32 v75, v75\n"
        "v_cvt_pk_bf16_f32 v228, v72, v73\n"
        "v_cvt_pk_bf16_f32 v229, v74, v75\n"
        "v_mfma_f32_32x32x16_bf16 v[144:159], v[168:171], v[224:227], v[144:159]\n"
        "v_exp_f32 v76, v76\n"
        "v_exp_f32 v77, v77\n"
        "v_exp_f32 v78, v78\n"
        "v_exp_f32 v79, v79\n"
        "v_cvt_pk_bf16_f32 v230, v76, v77\n"
        "v_cvt_pk_bf16_f32 v231, v78, v79\n"
        "v_mfma_f32_32x32x16_bf16 v[64:79], v[176:179], v[40:43], 0\n"
        "global_load_dwordx4 v[216:219], v1, s[20:21] offset:2048 sc1\n"
        "v_exp_f32 v80, v80\n"
        "v_exp_f32 v81, v81\n"
        "v_exp_f32 v82, v82\n"
        "v_exp_f32 v83, v83\n"
        "v_cvt_pk_bf16_f32 v232, v80, v81\n"
        "v_cvt_pk_bf16_f32 v233, v82, v83\n"
        "v_mfma_f32_32x32x16_bf16 v[64:79], v[180:183], v[44:47], v[64:79]\n"
        "v_exp_f32 v84, v84\n"
        "v_exp_f32 v85, v85\n"
        "v_exp_f32 v86, v86\n"
        "v_exp_f32 v87, v87\n"
        "v_cvt_pk_bf16_f32 v234, v84, v85\n"
        "v_cvt_pk_bf16_f32 v235, v86, v87\n"
        "v_mfma_f32_32x32x16_bf16 v[144:159], v[172:175], v[228:231], v[144:159]\n"
        "global_load_dwordx4 v[220:223], v1, s[20:21] offset:3072 sc1\n"
        "s_add_u32 s51, s51, 0x4000\n"
        "s_and_b32 s51, s51, 0x3ffff\n"
        "s_or_b32 s20, s51, s60\n"
        "v_exp_f32 v88, v88\n"
        "v_exp_f32 v89, v89\n"
        "v_exp_f32 v90, v90\n"
        "v_exp_f32 v91, v91\n"
        "v_cvt_pk_bf16_f32 v236, v88, v89\n"
        "v_cvt_pk_bf16_f32 v237, v90, v91\n"
        "v_mfma_f32_32x32x16_bf16 v[96:111], v[168:171], v[232:235], v[96:111]\n"
        "v_exp_f32 v92, v92\n"
        "v_exp_f32 v93, v93\n"
        "v_exp_f32 v94, v94\n"
        "v_exp_f32 v95, v95\n"
        "v_cvt_pk_bf16_f32 v238, v92, v93\n"
        "v_cvt_pk_bf16_f32 v239, v94, v95\n"
        "v_mfma_f32_32x32x16_bf16 v[80:95], v[176:179], v[48:51], 0\n"
        "global_load_dwordx4 v[160:163], v1, s[20:21] offset:0 sc1\n"
        "v_exp_f32 v64, v64\n"
        "v_exp_f32 v65, v65\n"
        "v_exp_f32 v66, v66\n"
        "v_exp_f32 v67, v67\n"
        "v_cvt_pk_bf16_f32 v224, v64, v65\n"
        "v_cvt_pk_bf16_f32 v225, v66, v67\n"
        "v_mfma_f32_32x32x16_bf16 v[80:95], v[180:183], v[52:55], v[80:95]\n"
        "s_waitcnt vmcnt(9)\n"
        "v_exp_f32 v68, v68\n"
        "v_exp_f32 v69, v69\n"
        "v_exp_f32 v70, v70\n"
        "v_exp_f32 v71, v71\n"
        "v_cvt_pk_bf16_f32 v226, v68, v69\n"
        "v_cvt_pk_bf16_f32 v227, v70, v71\n"
        "v_mfma_f32_32x32x16_bf16 v[96:111], v[172:175], v[236:239], v[96:111]\n"
        "global_load_dwordx4 v[172:175], v1, s[20:21] offset:3072 sc1\n"
        "v_exp_f32 v72, v72\n"
        "v_exp_f32 v73, v73\n"
        "v_exp_f32 v74, v74\n"
        "v_exp_f32 v75, v75\n"
        "v_cvt_pk_bf16_f32 v228, v72, v73\n"
        "v_cvt_pk_bf16_f32 v229, v74, v75\n"
        "v_mfma_f32_32x32x16_bf16 v[112:127], v[184:187], v[224:227], v[112:127]\n"
        "v_exp_f32 v76, v76\n"
        "v_exp_f32 v77, v77\n"
        "v_exp_f32 v78, v78\n"
        "v_exp_f32 v79, v79\n"
        "v_cvt_pk_bf16_f32 v230, v76, v77\n"
        "v_cvt_pk_bf16_f32 v231, v78, v79\n"
        "v_mfma_f32_32x32x16_bf16 v[64:79], v[176:179], v[56:59], 0\n"
        "global_load_dwordx4 v[164:167], v1, s[20:21] offset:1024 sc1\n"
        "v_exp_f32 v80, v80\n"
        "v_exp_f32 v81, v81\n"
        "v_exp_f32 v82, v82\n"
        "v_exp_f32 v83, v83\n"
        "v_cvt_pk_bf16_f32 v232, v80, v81\n"
        "v_cvt_pk_bf16_f32 v233, v82, v83\n"
        "v_mfma_f32_32x32x16_bf16 v[64:79], v[180:183], v[60:63], v[64:79]\n"
        "v_exp_f32 v84, v84\n"
        "v_exp_f32 v85, v85\n"
        "v_exp_f32 v86, v86\n"
        "v_exp_f32 v87, v87\n"
        "v_cvt_pk_bf16_f32 v234, v84, v85\n"
        "v_cvt_pk_bf16_f32 v235, v86, v87\n"
        "v_mfma_f32_32x32x16_bf16 v[112:127], v[188:191], v[228:231], v[112:127]\n"
        "global_load_dwordx4 v[168:171], v1, s[20:21] offset:2048 sc1\n"
        "s_add_u32 s51, s51, 0x4000\n"
        "s_and_b32 s51, s51, 0x3ffff\n"
        "s_or_b32 s20, s51, s60\n"
        "v_exp_f32 v88, v88\n"
        "v_exp_f32 v89, v89\n"
        "v_exp_f32 v90, v90\n"
        "v_exp_f32 v91, v91\n"
        "v_cvt_pk_bf16_f32 v236, v88, v89\n"
        "v_cvt_pk_bf16_f32 v237, v90, v91\n"
        "v_mfma_f32_32x32x16_bf16 v[128:143], v[184:187], v[232:235], v[128:143]\n"
        "v_exp_f32 v92, v92\n"
        "v_exp_f32 v93, v93\n"
        "v_exp_f32 v94, v94\n"
        "v_exp_f32 v95, v95\n"
        "v_cvt_pk_bf16_f32 v238, v92, v93\n"
        "v_cvt_pk_bf16_f32 v239, v94, v95\n"
        "v_mfma_f32_32x32x16_bf16 v[80:95], v[176:179], v[32:35], 0\n"
        "v_exp_f32 v64, v64\n"
        "v_exp_f32 v65, v65\n"
        "v_exp_f32 v66, v66\n"
        "v_exp_f32 v67, v67\n"
        "v_cvt_pk_bf16_f32 v224, v64, v65\n"
        "v_cvt_pk_bf16_f32 v225, v66, v67\n"
        "v_mfma_f32_32x32x16_bf16 v[80:95], v[180:183], v[36:39], v[80:95]\n"
        "s_waitcnt vmcnt(10)\n"
        "v_exp_f32 v68, v68\n"
        "v_exp_f32 v69, v69\n"
        "v_exp_f32 v70, v70\n"
        "v_exp_f32 v71, v71\n"
        "v_cvt_pk_bf16_f32 v226, v68, v69\n"
        "v_cvt_pk_bf16_f32 v227, v70, v71\n"
        "v_mfma_f32_32x32x16_bf16 v[128:143], v[188:191], v[236:239], v[128:143]\n"
        "global_load_dwordx4 v[176:179], v1, s[20:21] offset:0 sc1\n"
        "v_exp_f32 v72, v72\n"
        "v_exp_f32 v73, v73\n"
        "v_exp_f32 v74, v74\n"
        "v_exp_f32 v75, v75\n"
        "v_cvt_pk_bf16_f32 v228, v72, v73\n"
        "v_cvt_pk_bf16_f32 v229, v74, v75\n"
        "v_mfma_f32_32x32x16_bf16 v[144:159], v[184:187], v[224:227], v[144:159]\n"
        "global_load_dwordx4 v[180:183], v1, s[20:21] offset:1024 sc1\n"
        "v_exp_f32 v76, v76\n"
        "v_exp_f32 v77, v77\n"
        "v_exp_f32 v78, v78\n"
        "v_exp_f32 v79, v79\n"
        "v_cvt_pk_bf16_f32 v230, v76, v77\n"
        "v_cvt_pk_bf16_f32 v231, v78, v79\n"
        "v_mfma_f32_32x32x16_bf16 v[64:79], v[192:195], v[40:43], 0\n"
        "v_exp_f32 v80, v80\n"
        "v_exp_f32 v81, v81\n"
        "v_exp_f32 v82, v82\n"
        "v_exp_f32 v83, v83\n"
        "v_cvt_pk_bf16_f32 v232, v80, v81\n"
        "v_cvt_pk_bf16_f32 v233, v82, v83\n"
        "v_mfma_f32_32x32x16_bf16 v[64:79], v[196:199], v[44:47], v[64:79]\n"
        "v_exp_f32 v84, v84\n"
        "v_exp_f32 v85, v85\n"
        "v_exp_f32 v86, v86\n"
        "v_exp_f32 v87, v87\n"
        "v_cvt_pk_bf16_f32 v234, v84, v85\n"
        "v_cvt_pk_bf16_f32 v235, v86, v87\n"
        "v_mfma_f32_32x32x16_bf16 v[144:159], v[188:191], v[228:231], v[144:159]\n"
        "v_exp_f32 v88, v88\n"
        "v_exp_f32 v89, v89\n"
        "v_exp_f32 v90, v90\n"
        "v_exp_f32 v91, v91\n"
        "v_cvt_pk_bf16_f32 v236, v88, v89\n"
        "v_cvt_pk_bf16_f32 v237, v90, v91\n"
        "v_mfma_f32_32x32x16_bf16 v[96:111], v[184:187], v[232:235], v[96:111]\n"
        "global_load_dwordx4 v[184:187], v1, s[20:21] offset:2048 sc1\n"
        "v_exp_f32 v92, v92\n"
        "v_exp_f32 v93, v93\n"
        "v_exp_f32 v94, v94\n"
        "v_exp_f32 v95, v95\n"
        "v_cvt_pk_bf16_f32 v238, v92, v93\n"
        "v_cvt_pk_bf16_f32 v239, v94, v95\n"
        "v_mfma_f32_32x32x16_bf16 v[80:95], v[192:195], v[48:51], 0\n"
        "s_add_u32 s51, s51, 0x4000\n"
        "s_and_b32 s51, s51, 0x3ffff\n"
        "v_exp_f32 v64, v64\n"
        "v_exp_f32 v65, v65\n"
        "v_exp_f32 v66, v66\n"
        "v_exp_f32 v67, v67\n"
        "v_cvt_pk_bf16_f32 v224, v64, v65\n"
        "v_cvt_pk_bf16_f32 v225, v66, v67\n"
        "v_mfma_f32_32x32x16_bf16 v[80:95], v[196:199], v[52:55], v[80:95]\n"
        "s_waitcnt vmcnt(11)\n"
        "v_exp_f32 v68, v68\n"
        "v_exp_f32 v69, v69\n"
        "v_exp_f32 v70, v70\n"
        "v_exp_f32 v71, v71\n"
        "v_cvt_pk_bf16_f32 v226, v68, v69\n"
        "v_cvt_pk_bf16_f32 v227, v70, v71\n"
        "v_mfma_f32_32x32x16_bf16 v[96:111], v[188:191], v[236:239], v[96:111]\n"
        "global_load_dwordx4 v[188:191], v1, s[20:21] offset:3072 sc1\n"
        "s_or_b32 s20, s51, s60\n"
        "v_exp_f32 v72, v72\n"
        "v_exp_f32 v73, v73\n"
        "v_exp_f32 v74, v74\n"
        "v_exp_f32 v75, v75\n"
        "v_cvt_pk_bf16_f32 v228, v72, v73\n"
        "v_cvt_pk_bf16_f32 v229, v74, v75\n"
        "v_mfma_f32_32x32x16_bf16 v[112:127], v[200:203], v[224:227], v[112:127]\n"
        "buffer_load_dwordx2 v[240:241], v13, s[8:11], 0 offen sc1\n"
        "v_exp_f32 v76, v76\n"
        "v_exp_f32 v77, v77\n"
        "v_exp_f32 v78, v78\n"
        "v_exp_f32 v79, v79\n"
        "v_cvt_pk_bf16_f32 v230, v76, v77\n"
        "v_cvt_pk_bf16_f32 v231, v78, v79\n"
        "v_mfma_f32_32x32x16_bf16 v[64:79], v[192:195], v[56:59], 0\n"
        "v_exp_f32 v80, v80\n"
        "v_exp_f32 v81, v81\n"
        "v_exp_f32 v82, v82\n"
        "v_exp_f32 v83, v83\n"
        "v_cvt_pk_bf16_f32 v232, v80, v81\n"
        "v_cvt_pk_bf16_f32 v233, v82, v83\n"
        "v_mfma_f32_32x32x16_bf16 v[64:79], v[196:199], v[60:63], v[64:79]\n"
        "v_exp_f32 v84, v84\n"
        "v_exp_f32 v85, v85\n"
        "v_exp_f32 v86, v86\n"
        "v_exp_f32 v87, v87\n"
        "v_cvt_pk_bf16_f32 v234, v84, v85\n"
        "v_cvt_pk_bf16_f32 v235, v86, v87\n"
        "v_mfma_f32_32x32x16_bf16 v[112:127], v[204:207], v[228:231], v[112:127]\n"
        "v_exp_f32 v88, v88\n"
        "v_exp_f32 v89, v89\n"
        "v_exp_f32 v90, v90\n"
        "v_exp_f32 v91, v91\n"
        "v_cvt_pk_bf16_f32 v236, v88, v89\n"
        "v_cvt_pk_bf16_f32 v237, v90, v91\n"
        "v_mfma_f32_32x32x16_bf16 v[128:143], v[200:203], v[232:235], v[128:143]\n"
        "v_exp_f32 v92, v92\n"
        "v_exp_f32 v93, v93\n"
        "v_exp_f32 v94, v94\n"
        "v_exp_f32 v95, v95\n"
        "v_cvt_pk_bf16_f32 v238, v92, v93\n"
        "v_cvt_pk_bf16_f32 v239, v94, v95\n"
        "v_mfma_f32_32x32x16_bf16 v[80:95], v[192:195], v[32:35], 0\n"
        "v_exp_f32 v64, v64\n"
        "v_exp_f32 v65, v65\n"
        "v_exp_f32 v66, v66\n"
        "v_exp_f32 v67, v67\n"
        "v_cvt_pk_bf16_f32 v224, v64, v65\n"
        "v_cvt_pk_bf16_f32 v225, v66, v67\n"
        "v_mfma_f32_32x32x16_bf16 v[80:95], v[196:199], v[36:39], v[80:95]\n"
        "s_waitcnt vmcnt(11)\n"
        "v_exp_f32 v68, v68\n"
        "v_exp_f32 v69, v69\n"
        "v_exp_f32 v70, v70\n"
        "v_exp_f32 v71, v71\n"
        "v_cvt_pk_bf16_f32 v226, v68, v69\n"
        "v_cvt_pk_bf16_f32 v227, v70, v71\n"
        "v_mfma_f32_32x32x16_bf16 v[128:143], v[204:207], v[236:239], v[128:143]\n"
        "global_load_dwordx4 v[192:195], v1, s[20:21] offset:0 sc1\n"
        "v_exp_f32 v72, v72\n"
        "v_exp_f32 v73, v73\n"
        "v_exp_f32 v74, v74\n"
        "v_exp_f32 v75, v75\n"
        "v_cvt_pk_bf16_f32 v228, v72, v73\n"
        "v_cvt_pk_bf16_f32 v229, v74, v75\n"
        "v_mfma_f32_32x32x16_bf16 v[144:159], v[200:203], v[224:227], v[144:159]\n"
        "global_load_dwordx4 v[196:199], v1, s[20:21] offset:1024 sc1\n"
        "v_exp_f32 v76, v76\n"
        "v_exp_f32 v77, v77\n"
        "v_exp_f32 v78, v78\n"
        "v_exp_f32 v79, v79\n"
        "v_cvt_pk_bf16_f32 v230, v76, v77\n"
        "v_cvt_pk_bf16_f32 v231, v78, v79\n"
        "v_mfma_f32_32x32x16_bf16 v[64:79], v[208:211], v[40:43], 0\n"
        "v_exp_f32 v80, v80\n"
        "v_exp_f32 v81, v81\n"
        "v_exp_f32 v82, v82\n"
        "v_exp_f32 v83, v83\n"
        "v_cvt_pk_bf16_f32 v232, v80, v81\n"
        "v_cvt_pk_bf16_f32 v233, v82, v83\n"
        "v_mfma_f32_32x32x16_bf16 v[64:79], v[212:215], v[44:47], v[64:79]\n"
        "v_exp_f32 v84, v84\n"
        "v_exp_f32 v85, v85\n"
        "v_exp_f32 v86, v86\n"
        "v_exp_f32 v87, v87\n"
        "v_cvt_pk_bf16_f32 v234, v84, v85\n"
        "v_cvt_pk_bf16_f32 v235, v86, v87\n"
        "v_mfma_f32_32x32x16_bf16 v[144:159], v[204:207], v[228:231], v[144:159]\n"
        "v_exp_f32 v88, v88\n"
        "v_exp_f32 v89, v89\n"
        "v_exp_f32 v90, v90\n"
        "v_exp_f32 v91, v91\n"
        "v_cvt_pk_bf16_f32 v236, v88, v89\n"
        "v_cvt_pk_bf16_f32 v237, v90, v91\n"
        "v_mfma_f32_32x32x16_bf16 v[96:111], v[200:203], v[232:235], v[96:111]\n"
        "global_load_dwordx4 v[200:203], v1, s[20:21] offset:2048 sc1\n"
        "v_exp_f32 v92, v92\n"
        "v_exp_f32 v93, v93\n"
        "v_exp_f32 v94, v94\n"
        "v_exp_f32 v95, v95\n"
        "v_cvt_pk_bf16_f32 v238, v92, v93\n"
        "v_cvt_pk_bf16_f32 v239, v94, v95\n"
        "v_mfma_f32_32x32x16_bf16 v[80:95], v[208:211], v[48:51], 0\n"
        "s_add_u32 s51, s51, 0x4000\n"
        "s_and_b32 s51, s51, 0x3ffff\n"
        "v_exp_f32 v64, v64\n"
        "v_exp_f32 v65, v65\n"
        "v_exp_f32 v66, v66\n"
        "v_exp_f32 v67, v67\n"
        "v_cvt_pk_bf16_f32 v224, v64, v65\n"
        "v_cvt_pk_bf16_f32 v225, v66, v67\n"
        "v_mfma_f32_32x32x16_bf16 v[80:95], v[212:215], v[52:55], v[80:95]\n"
        "s_waitcnt vmcnt(12)\n"
        "v_exp_f32 v68, v68\n"
        "v_exp_f32 v69, v69\n"
        "v_exp_f32 v70, v70\n"
        "v_exp_f32 v71, v71\n"
        "v_cvt_pk_bf16_f32 v226, v68, v69\n"
        "v_cvt_pk_bf16_f32 v227, v70, v71\n"
        "v_mfma_f32_32x32x16_bf16 v[96:111], v[204:207], v[236:239], v[96:111]\n"
        "global_load_dwordx4 v[204:207], v1, s[20:21] offset:3072 sc1\n"
        "s_or_b32 s20, s51, s60\n"
        "v_exp_f32 v72, v72\n"
        "v_exp_f32 v73, v73\n"
        "v_exp_f32 v74, v74\n"
        "v_exp_f32 v75, v75\n"
        "v_cvt_pk_bf16_f32 v228, v72, v73\n"
        "v_cvt_pk_bf16_f32 v229, v74, v75\n"
        "v_mfma_f32_32x32x16_bf16 v[112:127], v[216:219], v[224:227], v[112:127]\n"
        "v_exp_f32 v76, v76\n"
        "v_exp_f32 v77, v77\n"
        "v_exp_f32 v78, v78\n"
        "v_exp_f32 v79, v79\n"
        "v_cvt_pk_bf16_f32 v230, v76, v77\n"
        "v_cvt_pk_bf16_f32 v231, v78, v79\n"
        "v_mfma_f32_32x32x16_bf16 v[64:79], v[208:211], v[56:59], 0\n"
        "v_exp_f32 v80, v80\n"
        "v_exp_f32 v81, v81\n"
        "v_exp_f32 v82, v82\n"
        "v_exp_f32 v83, v83\n"
        "v_cvt_pk_bf16_f32 v232, v80, v81\n"
        "v_cvt_pk_bf16_f32 v233, v82, v83\n"
        "v_mfma_f32_32x32x16_bf16 v[64:79], v[212:215], v[60:63], v[64:79]\n"
        "v_exp_f32 v84, v84\n"
        "v_exp_f32 v85, v85\n"
        "v_exp_f32 v86, v86\n"
        "v_exp_f32 v87, v87\n"
        "v_cvt_pk_bf16_f32 v234, v84, v85\n"
        "v_cvt_pk_bf16_f32 v235, v86, v87\n"
        "v_mfma_f32_32x32x16_bf16 v[112:127], v[220:223], v[228:231], v[112:127]\n"
        "v_exp_f32 v88, v88\n"
        "v_exp_f32 v89, v89\n"
        "v_exp_f32 v90, v90\n"
        "v_exp_f32 v91, v91\n"
        "v_cvt_pk_bf16_f32 v236, v88, v89\n"
        "v_cvt_pk_bf16_f32 v237, v90, v91\n"
        "v_mfma_f32_32x32x16_bf16 v[128:143], v[216:219], v[232:235], v[128:143]\n"
        "v_exp_f32 v92, v92\n"
        "v_exp_f32 v93, v93\n"
        "v_exp_f32 v94, v94\n"
        "v_exp_f32 v95, v95\n"
        "v_cvt_pk_bf16_f32 v238, v92, v93\n"
        "v_cvt_pk_bf16_f32 v239, v94, v95\n"
        "v_mfma_f32_32x32x16_bf16 v[80:95], v[208:211], v[32:35], 0\n"
        "v_exp_f32 v64, v64\n"
        "v_exp_f32 v65, v65\n"
        "v_exp_f32 v66, v66\n"
        "v_exp_f32 v67, v67\n"
        "v_cvt_pk_bf16_f32 v224, v64, v65\n"
        "v_cvt_pk_bf16_f32 v225, v66, v67\n"
        "v_mfma_f32_32x32x16_bf16 v[80:95], v[212:215], v[36:39], v[80:95]\n"
        "s_waitcnt vmcnt(10)\n"
        "v_exp_f32 v68, v68\n"
        "v_exp_f32 v69, v69\n"
        "v_exp_f32 v70, v70\n"
        "v_exp_f32 v71, v71\n"
        "v_cvt_pk_bf16_f32 v226, v68, v69\n"
        "v_cvt_pk_bf16_f32 v227, v70, v71\n"
        "v_mfma_f32_32x32x16_bf16 v[128:143], v[220:223], v[236:239], v[128:143]\n"
        "global_load_dwordx4 v[208:211], v1, s[20:21] offset:0 sc1\n"
        "v_exp_f32 v72, v72\n"
        "v_exp_f32 v73, v73\n"
        "v_exp_f32 v74, v74\n"
        "v_exp_f32 v75, v75\n"
        "v_cvt_pk_bf16_f32 v228, v72, v73\n"
        "v_cvt_pk_bf16_f32 v229, v74, v75\n"
        "v_mfma_f32_32x32x16_bf16 v[144:159], v[216:219], v[224:227], v[144:159]\n"
        "global_load_dwordx4 v[212:215], v1, s[20:21] offset:1024 sc1\n"
        "v_exp_f32 v76, v76\n"
        "v_exp_f32 v77, v77\n"
        "v_exp_f32 v78, v78\n"
        "v_exp_f32 v79, v79\n"
        "v_cvt_pk_bf16_f32 v230, v76, v77\n"
        "v_cvt_pk_bf16_f32 v231, v78, v79\n"
        "v_mfma_f32_32x32x16_bf16 v[64:79], v[160:163], v[40:43], 0\n"
        "v_exp_f32 v80, v80\n"
        "v_exp_f32 v81, v81\n"
        "v_exp_f32 v82, v82\n"
        "v_exp_f32 v83, v83\n"
        "v_cvt_pk_bf16_f32 v232, v80, v81\n"
        "v_cvt_pk_bf16_f32 v233, v82, v83\n"
        "v_mfma_f32_32x32x16_bf16 v[64:79], v[164:167], v[44:47], v[64:79]\n"
        "v_exp_f32 v84, v84\n"
        "v_exp_f32 v85, v85\n"
        "v_exp_f32 v86, v86\n"
        "v_exp_f32 v87, v87\n"
        "v_cvt_pk_bf16_f32 v234, v84, v85\n"
        "v_cvt_pk_bf16_f32 v235, v86, v87\n"
        "v_mfma_f32_32x32x16_bf16 v[144:159], v[220:223], v[228:231], v[144:159]\n"
        "v_exp_f32 v88, v88\n"
        "v_exp_f32 v89, v89\n"
        "v_exp_f32 v90, v90\n"
        "v_exp_f32 v91, v91\n"
        "v_cvt_pk_bf16_f32 v236, v88, v89\n"
        "v_cvt_pk_bf16_f32 v237, v90, v91\n"
        "v_mfma_f32_32x32x16_bf16 v[96:111], v[216:219], v[232:235], v[96:111]\n"
        "global_load_dwordx4 v[216:219], v1, s[20:21] offset:2048 sc1\n"
        "v_exp_f32 v92, v92\n"
        "v_exp_f32 v93, v93\n"
        "v_exp_f32 v94, v94\n"
        "v_exp_f32 v95, v95\n"
        "v_cvt_pk_bf16_f32 v238, v92, v93\n"
        "v_cvt_pk_bf16_f32 v239, v94, v95\n"
        "s_waitcnt vmcnt(7)\n"
        "v_cmp_ne_u64 vcc, s[96:97], v[240:241]\n"
        "s_cbranch_vccnz .Lupoll\n"
        ".Latt1:\n"
        "s_cmp_lg_u32 s28, 0\n"
        "s_cbranch_scc1 .Latt\n"
        "v_mfma_f32_32x32x16_bf16 v[80:95], v[160:163], v[48:51], 0\n"
        "s_add_u32 s51, s51, 0x4000\n"
        "s_and_b32 s51, s51, 0x3ffff\n"
        "v_exp_f32 v64, v64\n"
        "v_exp_f32 v65, v65\n"
        "v_exp_f32 v66, v66\n"
        "v_exp_f32 v67, v67\n"
        "v_cvt_pk_bf16_f32 v224, v64, v65\n"
        "v_cvt_pk_bf16_f32 v225, v66, v67\n"
        "v_mfma_f32_32x32x16_bf16 v[80:95], v[164:167], v[52:55], v[80:95]\n"
        "s_waitcnt vmcnt(12)\n"
        "v_exp_f32 v68, v68\n"
        "v_exp_f32 v69, v69\n"
        "v_exp_f32 v70, v70\n"
        "v_exp_f32 v71, v71\n"
        "v_cvt_pk_bf16_f32 v226, v68, v69\n"
        "v_cvt_pk_bf16_f32 v227, v70, v71\n"
        "v_mfma_f32_32x32x16_bf16 v[96:111], v[220:223], v[236:239], v[96:111]\n"
        "global_load_dwordx4 v[220:223], v1, s[20:21] offset:3072 sc1\n"
        "s_or_b32 s20, s51, s60\n"
        "v_exp_f32 v72, v72\n"
        "v_exp_f32 v73, v73\n"
        "v_exp_f32 v74, v74\n"
        "v_exp_f32 v75, v75\n"
        "v_cvt_pk_bf16_f32 v228, v72, v73\n"
        "v_cvt_pk_bf16_f32 v229, v74, v75\n"
        "v_mfma_f32_32x32x16_bf16 v[112:127], v[168:171], v[224:227], v[112:127]\n"
        "global_load_dwordx4 a[0:3], v1, s[62:63] offset:0 sc1\n"
        "v_exp_f32 v76, v76\n"
        "v_exp_f32 v77, v77\n"
        "v_exp_f32 v78, v78\n"
        "v_exp_f32 v79, v79\n"
        "v_cvt_pk_bf16_f32 v230, v76, v77\n"
        "v_cvt_pk_bf16_f32 v231, v78, v79\n"
        "v_mfma_f32_32x32x16_bf16 v[64:79], v[160:163], v[56:59], 0\n"
        "global_load_lds_dwordx4 v1, s[22:23] offset:0 sc1\n"
        "v_exp_f32 v80, v80\n"
        "v_exp_f32 v81, v81\n"
        "v_exp_f32 v82, v82\n"
        "v_exp_f32 v83, v83\n"
        "v_cvt_pk_bf16_f32 v232, v80, v81\n"
        "v_cvt_pk_bf16_f32 v233, v82, v83\n"
        "v_mfma_f32_32x32x16_bf16 v[64:79], v[164:167], v[60:63], v[64:79]\n"
        "global_load_dwordx4 a[4:7], v1, s[62:63] offset:1024 sc1\n"
        "v_exp_f32 v84, v84\n"
        "v_exp_f32 v85, v85\n"
        "v_exp_f32 v86, v86\n"
        "v_exp_f32 v87, v87\n"
        "v_cvt_pk_bf16_f32 v234, v84, v85\n"
        "v_cvt_pk_bf16_f32 v235, v86, v87\n"
        "v_mfma_f32_32x32x16_bf16 v[112:127], v[172:175], v[228:231], v[112:127]\n"
        "global_load_lds_dwordx4 v1, s[22:23] offset:1024 sc1\n"
        "v_exp_f32 v88, v88\n"
        "v_exp_f32 v89, v89\n"
        "v_exp_f32 v90, v90\n"
        "v_exp_f32 v91, v91\n"
        "v_cvt_pk_bf16_f32 v236, v88, v89\n"
        "v_cvt_pk_bf16_f32 v237, v90, v91\n"
        "v_mfma_f32_32x32x16_bf16 v[128:143], v[168:171], v[232:235], v[128:143]\n"
        "global_load_dwordx4 a[8:11], v1, s[62:63] offset:2048 sc1\n"
        "v_exp_f32 v92, v92\n"
        "v_exp_f32 v93, v93\n"
        "v_exp_f32 v94, v94\n"
        "v_exp_f32 v95, v95\n"
        "v_cvt_pk_bf16_f32 v238, v92, v93\n"
        "v_cvt_pk_bf16_f32 v239, v94, v95\n"
        "v_mfma_f32_32x32x16_bf16 v[80:95], v[160:163], v[32:35], 0\n"
        "global_load_dwordx4 a[12:15], v1, s[62:63] offset:3072 sc1\n"
        "s_add_u32 s62, s62, 4096\n"
        "s_addc_u32 s63, s63, 0\n"
        "v_exp_f32 v64, v64\n"
        "v_exp_f32 v65, v65\n"
        "v_exp_f32 v66, v66\n"
        "v_exp_f32 v67, v67\n"
        "v_cvt_pk_bf16_f32 v224, v64, v65\n"
        "v_cvt_pk_bf16_f32 v225, v66, v67\n"
        "v_mfma_f32_32x32x16_bf16 v[80:95], v[164:167], v[36:39], v[80:95]\n"
        "s_waitcnt vmcnt(17)\n"
        "v_exp_f32 v68, v68\n"
        "v_exp_f32 v69, v69\n"
        "v_exp_f32 v70, v70\n"
        "v_exp_f32 v71, v71\n"
        "v_cvt_pk_bf16_f32 v226, v68, v69\n"
        "v_cvt_pk_bf16_f32 v227, v70, v71\n"
        "v_mfma_f32_32x32x16_bf16 v[128:143], v[172:175], v[236:239], v[128:143]\n"
        "global_load_dwordx4 v[160:163], v1, s[20:21] offset:0 sc1\n"
        "v_exp_f32 v72, v72\n"
        "v_exp_f32 v73, v73\n"
        "v_exp_f32 v74, v74\n"
        "v_exp_f32 v75, v75\n"
        "v_cvt_pk_bf16_f32 v228, v72, v73\n"
        "v_cvt_pk_bf16_f32 v229, v74, v75\n"
        "v_mfma_f32_32x32x16_bf16 v[144:159], v[168:171], v[224:227], v[144:159]\n"
        "global_load_dwordx4 v[164:167], v1, s[20:21] offset:1024 sc1\n"
        "v_exp_f32 v76, v76\n"
        "v_exp_f32 v77, v77\n"
        "v_exp_f32 v78, v78\n"
        "v_exp_f32 v79, v79\n"
        "v_cvt_pk_bf16_f32 v230, v76, v77\n"
        "v_cvt_pk_bf16_f32 v231, v78, v79\n"
        "v_mfma_f32_32x32x16_bf16 v[64:79], v[176:179], v[40:43], 0\n"
        "v_exp_f32 v80, v80\n"
        "v_exp_f32 v81, v81\n"
        "v_exp_f32 v82, v82\n"
        "v_exp_f32 v83, v83\n"
        "v_cvt_pk_bf16_f32 v232, v80, v81\n"
        "v_cvt_pk_bf16_f32 v233, v82, v83\n"
        "v_mfma_f32_32x32x16_bf16 v[64:79], v[180:183], v[44:47], v[64:79]\n"
        "v_exp_f32 v84, v84\n"
        "v_exp_f32 v85, v85\n"
        "v_exp_f32 v86, v86\n"
        "v_exp_f32 v87, v87\n"
        "v_cvt_pk_bf16_f32 v234, v84, v85\n"
        "v_cvt_pk_bf16_f32 v235, v86, v87\n"
        "v_mfma_f32_32x32x16_bf16 v[144:159], v[172:175], v[228:231], v[144:159]\n"
        "v_exp_f32 v88, v88\n"
        "v_exp_f32 v89, v89\n"
        "v_exp_f32 v90, v90\n"
        "v_exp_f32 v91, v91\n"
        "v_cvt_pk_bf16_f32 v236, v88, v89\n"
        "v_cvt_pk_bf16_f32 v237, v90, v91\n"
        "v_mfma_f32_32x32x16_bf16 v[96:111], v[168:171], v[232:235], v[96:111]\n"
        "global_load_dwordx4 v[168:171], v1, s[20:21] offset:2048 sc1\n"
        "v_exp_f32 v92, v92\n"
        "v_exp_f32 v93, v93\n"
        "v_exp_f32 v94, v94\n"
        "v_exp_f32 v95, v95\n"
        "v_cvt_pk_bf16_f32 v238, v92, v93\n"
        "v_cvt_pk_bf16_f32 v239, v94, v95\n"
        "v_mfma_f32_32x32x16_bf16 v[80:95], v[176:179], v[48:51], 0\n"
        "s_add_u32 s51, s51, 0x4000\n"
        "s_and_b32 s51, s51, 0x3ffff\n"
        "v_exp_f32 v64, v64\n"
        "v_exp_f32 v65, v65\n"
        "v_exp_f32 v66, v66\n"
        "v_exp_f32 v67, v67\n"
        "v_cvt_pk_bf16_f32 v224, v64, v65\n"
        "v_cvt_pk_bf16_f32 v225, v66, v67\n"
        "v_mfma_f32_32x32x16_bf16 v[80:95], v[180:183], v[52:55], v[80:95]\n"
        "s_waitcnt vmcnt(18)\n"
        "v_exp_f32 v68, v68\n"
        "v_exp_f32 v69, v69\n"
        "v_exp_f32 v70, v70\n"
        "v_exp_f32 v71, v71\n"
        "v_cvt_pk_bf16_f32 v226, v68, v69\n"
        "v_cvt_pk_bf16_f32 v227, v70, v71\n"
        "v_mfma_f32_32x32x16_bf16 v[96:111], v[172:175], v[236:239], v[96:111]\n"
        "global_load_dwordx4 v[172:175], v1, s[20:21] offset:3072 sc1\n"
        "s_or_b32 s20, s51, s60\n"
        "v_exp_f32 v72, v72\n"
        "v_exp_f32 v73, v73\n"
        "v_exp_f32 v74, v74\n"
        "v_exp_f32 v75, v75\n"
        "v_cvt_pk_bf16_f32 v228, v72, v73\n"
        "v_cvt_pk_bf16_f32 v229, v74, v75\n"
        "v_mfma_f32_32x32x16_bf16 v[112:127], v[184:187], v[224:227], v[112:127]\n"
        "global_load_dwordx4 a[16:19], v1, s[62:63] offset:0 sc1\n"
        "v_exp_f32 v76, v76\n"
        "v_exp_f32 v77, v77\n"
        "v_exp_f32 v78, v78\n"
        "v_exp_f32 v79, v79\n"
        "v_cvt_pk_bf16_f32 v230, v76, v77\n"
        "v_cvt_pk_bf16_f32 v231, v78, v79\n"
        "v_mfma_f32_32x32x16_bf16 v[64:79], v[176:179], v[56:59], 0\n"
        "global_load_lds_dwordx4 v1, s[22:23] offset:2048 sc1\n"
        "v_exp_f32 v80, v80\n"
        "v_exp_f32 v81, v81\n"
        "v_exp_f32 v82, v82\n"
        "v_exp_f32 v83, v83\n"
        "v_cvt_pk_bf16_f32 v232, v80, v81\n"
        "v_cvt_pk_bf16_f32 v233, v82, v83\n"
        "v_mfma_f32_32x32x16_bf16 v[64:79], v[180:183], v[60:63], v[64:79]\n"
        "global_load_dwordx4 a[20:23], v1, s[62:63] offset:1024 sc1\n"
        "v_exp_f32 v84, v84\n"
        "v_exp_f32 v85, v85\n"
        "v_exp_f32 v86, v86\n"
        "v_exp_f32 v87, v87\n"
        "v_cvt_pk_bf16_f32 v234, v84, v85\n"
        "v_cvt_pk_bf16_f32 v235, v86, v87\n"
        "v_mfma_f32_32x32x16_bf16 v[112:127], v[188:191], v[228:231], v[112:127]\n"
        "global_load_lds_dwordx4 v1, s[22:23] offset:3072 sc1\n"
        "s_add_u32 m0, m0, 4096\n"
        "s_add_u32 s22, s22, 4096\n"
        "s_addc_u32 s23, s23, 0\n"
        "v_exp_f32 v88, v88\n"
        "v_exp_f32 v89, v89\n"
        "v_exp_f32 v90, v90\n"
        "v_exp_f32 v91, v91\n"
        "v_cvt_pk_bf16_f32 v236, v88, v89\n"
        "v_cvt_pk_bf16_f32 v237, v90, v91\n"
        "v_mfma_f32_32x32x16_bf16 v[128:143], v[184:187], v[232:235], v[128:143]\n"
        "global_load_dwordx4 a[24:27], v1, s[62:63] offset:2048 sc1\n"
        "v_exp_f32 v92, v92\n"
        "v_exp_f32 v93, v93\n"
        "v_exp_f32 v94, v94\n"
        "v_exp_f32 v95, v95\n"
        "v_cvt_pk_bf16_f32 v238, v92, v93\n"
        "v_cvt_pk_bf16_f32 v239, v94, v95\n"
        "v_mfma_f32_32x32x16_bf16 v[80:95], v[176:179], v[32:35], 0\n"
        "global_load_dwordx4 a[28:31], v1, s[62:63] offset:3072 sc1\n"
        "s_add_u32 s62, s62, 4096\n"
        "s_addc_u32 s63, s63, 0\n"
        "v_exp_f32 v64, v64\n"
        "v_exp_f32 v65, v65\n"
        "v_exp_f32 v66, v66\n"
        "v_exp_f32 v67, v67\n"
        "v_cvt_pk_bf16_f32 v224, v64, v65\n"
        "v_cvt_pk_bf16_f32 v225, v66, v67\n"
        "v_mfma_f32_32x32x16_bf16 v[80:95], v[180:183], v[36:39], v[80:95]\n"
        "s_waitcnt vmcnt(22)\n"
        "v_exp_f32 v68, v68\n"
        "v_exp_f32 v69, v69\n"
        "v_exp_f32 v70, v70\n"
        "v_exp_f32 v71, v71\n"
        "v_cvt_pk_bf16_f32 v226, v68, v69\n"
        "v_cvt_pk_bf16_f32 v227, v70, v71\n"
        "v_mfma_f32_32x32x16_bf16 v[128:143], v[188:191], v[236:239], v[128:143]\n"
        "global_load_dwordx4 v[176:179], v1, s[20:21] offset:0 sc1\n"
        "v_exp_f32 v72, v72\n"
        "v_exp_f32 v73, v73\n"
        "v_exp_f32 v74, v74\n"
        "v_exp_f32 v75, v75\n"
        "v_cvt_pk_bf16_f32 v228, v72, v73\n"
        "v_cvt_pk_bf16_f32 v229, v74, v75\n"
        "v_mfma_f32_32x32x16_bf16 v[144:159], v[184:187], v[224:227], v[144:159]\n"
        "global_load_dwordx4 v[180:183], v1, s[20:21] offset:1024 sc1\n"
        "v_exp_f32 v76, v76\n"
        "v_exp_f32 v77, v77\n"
        "v_exp_f32 v78, v78\n"
        "v_exp_f32 v79, v79\n"
        "v_cvt_pk_bf16_f32 v230, v76, v77\n"
        "v_cvt_pk_bf16_f32 v231, v78, v79\n"
        "v_mfma_f32_32x32x16_bf16 v[64:79], v[192:195], v[40:43], 0\n"
        "v_exp_f32 v80, v80\n"
        "v_exp_f32 v81, v81\n"
        "v_exp_f32 v82, v82\n"
        "v_exp_f32 v83, v83\n"
        "v_cvt_pk_bf16_f32 v232, v80, v81\n"
        "v_cvt_pk_bf16_f32 v233, v82, v83\n"
        "v_mfma_f32_32x32x16_bf16 v[64:79], v[196:199], v[44:47], v[64:79]\n"
        "v_exp_f32 v84, v84\n"
        "v_exp_f32 v85, v85\n"
        "v_exp_f32 v86, v86\n"
        "v_exp_f32 v87, v87\n"
        "v_cvt_pk_bf16_f32 v234, v84, v85\n"
        "v_cvt_pk_bf16_f32 v235, v86, v87\n"
        "v_mfma_f32_32x32x16_bf16 v[144:159], v[188:191], v[228:231], v[144:159]\n"
        "v_exp_f32 v88, v88\n"
        "v_exp_f32 v89, v89\n"
        "v_exp_f32 v90, v90\n"
        "v_exp_f32 v91, v91\n"
        "v_cvt_pk_bf16_f32 v236, v88, v89\n"
        "v_cvt_pk_bf16_f32 v237, v90, v91\n"
        "v_mfma_f32_32x32x16_bf16 v[96:111], v[184:187], v[232:235], v[96:111]\n"
        "global_load_dwordx4 v[184:187], v1, s[20:21] offset:2048 sc1\n"
        "v_exp_f32 v92, v92\n"
        "v_exp_f32 v93, v93\n"
        "v_exp_f32 v94, v94\n"
        "v_exp_f32 v95, v95\n"
        "v_cvt_pk_bf16_f32 v238, v92, v93\n"
        "v_cvt_pk_bf16_f32 v239, v94, v95\n"
        "v_mfma_f32_32x32x16_bf16 v[80:95], v[192:195], v[48:51], 0\n"
        "s_add_u32 s51, s51, 0x4000\n"
        "s_and_b32 s51, s51, 0x3ffff\n"
        "v_exp_f32 v64, v64\n"
        "v_exp_f32 v65, v65\n"
        "v_exp_f32 v66, v66\n"
        "v_exp_f32 v67, v67\n"
        "v_cvt_pk_bf16_f32 v224, v64, v65\n"
        "v_cvt_pk_bf16_f32 v225, v66, v67\n"
        "v_mfma_f32_32x32x16_bf16 v[80:95], v[196:199], v[52:55], v[80:95]\n"
        "s_waitcnt vmcnt(23)\n"
        "v_exp_f32 v68, v68\n"
        "v_exp_f32 v69, v69\n"
        "v_exp_f32 v70, v70\n"
        "v_exp_f32 v71, v71\n"
        "v_cvt_pk_bf16_f32 v226, v68, v69\n"
        "v_cvt_pk_bf16_f32 v227, v70, v71\n"
        "v_mfma_f32_32x32x16_bf16 v[96:111], v[188:191], v[236:239], v[96:111]\n"
        "global_load_dwordx4 v[188:191], v1, s[20:21] offset:3072 sc1\n"
        "s_or_b32 s20, s51, s60\n"
        "v_exp_f32 v72, v72\n"
        "v_exp_f32 v73, v73\n"
        "v_exp_f32 v74, v74\n"
        "v_exp_f32 v75, v75\n"
        "v_cvt_pk_bf16_f32 v228, v72, v73\n"
        "v_cvt_pk_bf16_f32 v229, v74, v75\n"
        "v_mfma_f32_32x32x16_bf16 v[112:127], v[200:203], v[224:227], v[112:127]\n"
        "global_load_dwordx4 a[32:35], v1, s[62:63] offset:0 sc1\n"
        "v_exp_f32 v76, v76\n"
        "v_exp_f32 v77, v77\n"
        "v_exp_f32 v78, v78\n"
        "v_exp_f32 v79, v79\n"
        "v_cvt_pk_bf16_f32 v230, v76, v77\n"
        "v_cvt_pk_bf16_f32 v231, v78, v79\n"
        "v_mfma_f32_32x32x16_bf16 v[64:79], v[192:195], v[56:59], 0\n"
        "global_load_lds_dwordx4 v1, s[22:23] offset:0 sc1\n"
        "v_exp_f32 v80, v80\n"
        "v_exp_f32 v81, v81\n"
        "v_exp_f32 v82, v82\n"
        "v_exp_f32 v83, v83\n"
        "v_cvt_pk_bf16_f32 v232, v80, v81\n"
        "v_cvt_pk_bf16_f32 v233, v82, v83\n"
        "v_mfma_f32_32x32x16_bf16 v[64:79], v[196:199], v[60:63], v[64:79]\n"
        "global_load_dwordx4 a[36:39], v1, s[62:63] offset:1024 sc1\n"
        "v_exp_f32 v84, v84\n"
        "v_exp_f32 v85, v85\n"
        "v_exp_f32 v86, v86\n"
        "v_exp_f32 v87, v87\n"
        "v_cvt_pk_bf16_f32 v234, v84, v85\n"
        "v_cvt_pk_bf16_f32 v235, v86, v87\n"
        "v_mfma_f32_32x32x16_bf16 v[112:127], v[204:207], v[228:231], v[112:127]\n"
        "global_load_lds_dwordx4 v1, s[22:23] offset:1024 sc1\n"
        "v_exp_f32 v88, v88\n"
        "v_exp_f32 v89, v89\n"
        "v_exp_f32 v90, v90\n"
        "v_exp_f32 v91, v91\n"
        "v_cvt_pk_bf16_f32 v236, v88, v89\n"
        "v_cvt_pk_bf16_f32 v237, v90, v91\n"
        "v_mfma_f32_32x32x16_bf16 v[128:143], v[200:203], v[232:235], v[128:143]\n"
        "global_load_dwordx4 a[40:43], v1, s[62:63] offset:2048 sc1\n"
        "v_exp_f32 v92, v92\n"
        "v_exp_f32 v93, v93\n"
        "v_exp_f32 v94, v94\n"
        "v_exp_f32 v95, v95\n"
        "v_cvt_pk_bf16_f32 v238, v92, v93\n"
        "v_cvt_pk_bf16_f32 v239, v94, v95\n"
        "v_mfma_f32_32x32x16_bf16 v[80:95], v[192:195], v[32:35], 0\n"
        "global_load_dwordx4 a[44:47], v1, s[62:63] offset:3072 sc1\n"
        "s_add_u32 s62, s62, 4096\n"
        "s_addc_u32 s63, s63, 0\n"
        "v_exp_f32 v64, v64\n"
        "v_exp_f32 v65, v65\n"
        "v_exp_f32 v66, v66\n"
        "v_exp_f32 v67, v67\n"
        "v_cvt_pk_bf16_f32 v224, v64, v65\n"
        "v_cvt_pk_bf16_f32 v225, v66, v67\n"
        "v_mfma_f32_32x32x16_bf16 v[80:95], v[196:199], v[36:39], v[80:95]\n"
        "s_waitcnt vmcnt(28)\n"
        "v_exp_f32 v68, v68\n"
        "v_exp_f32 v69, v69\n"
        "v_exp_f32 v70, v70\n"
        "v_exp_f32 v71, v71\n"
        "v_cvt_pk_bf16_f32 v226, v68, v69\n"
        "v_cvt_pk_bf16_f32 v227, v70, v71\n"
        "v_mfma_f32_32x32x16_bf16 v[128:143], v[204:207], v[236:239], v[128:143]\n"
        "global_load_dwordx4 v[192:195], v1, s[20:21] offset:0 sc1\n"
        "v_exp_f32 v72, v72\n"
        "v_exp_f32 v73, v73\n"
        "v_exp_f32 v74, v74\n"
        "v_exp_f32 v75, v75\n"
        "v_cvt_pk_bf16_f32 v228, v72, v73\n"
        "v_cvt_pk_bf16_f32 v229, v74, v75\n"
        "v_mfma_f32_32x32x16_bf16 v[144:159], v[200:203], v[224:227], v[144:159]\n"
        "global_load_dwordx4 v[196:199], v1, s[20:21] offset:1024 sc1\n"
        "v_exp_f32 v76, v76\n"
        "v_exp_f32 v77, v77\n"
        "v_exp_f32 v78, v78\n"
        "v_exp_f32 v79, v79\n"
        "v_cvt_pk_bf16_f32 v230, v76, v77\n"
        "v_cvt_pk_bf16_f32 v231, v78, v79\n"
        "v_mfma_f32_32x32x16_bf16 v[64:79], v[208:211], v[40:43], 0\n"
        "v_exp_f32 v80, v80\n"
        "v_exp_f32 v81, v81\n"
        "v_exp_f32 v82, v82\n"
        "v_exp_f32 v83, v83\n"
        "v_cvt_pk_bf16_f32 v232, v80, v81\n"
        "v_cvt_pk_bf16_f32 v233, v82, v83\n"
        "v_mfma_f32_32x32x16_bf16 v[64:79], v[212:215], v[44:47], v[64:79]\n"
        "v_exp_f32 v84, v84\n"
        "v_exp_f32 v85, v85\n"
        "v_exp_f32 v86, v86\n"
        "v_exp_f32 v87, v87\n"
        "v_cvt_pk_bf16_f32 v234, v84, v85\n"
        "v_cvt_pk_bf16_f32 v235, v86, v87\n"
        "v_mfma_f32_32x32x16_bf16 v[144:159], v[204:207], v[228:231], v[144:159]\n"
        "v_exp_f32 v88, v88\n"
        "v_exp_f32 v89, v89\n"
        "v_exp_f32 v90, v90\n"
        "v_exp_f32 v91, v91\n"
        "v_cvt_pk_bf16_f32 v236, v88, v89\n"
        "v_cvt_pk_bf16_f32 v237, v90, v91\n"
        "v_mfma_f32_32x32x16_bf16 v[96:111], v[200:203], v[232:235], v[96:111]\n"
        "global_load_dwordx4 v[200:203], v1, s[20:21] offset:2048 sc1\n"
        "v_exp_f32 v92, v92\n"
        "v_exp_f32 v93, v93\n"
        "v_exp_f32 v94, v94\n"
        "v_exp_f32 v95, v95\n"
        "v_cvt_pk_bf16_f32 v238, v92, v93\n"
        "v_cvt_pk_bf16_f32 v239, v94, v95\n"
        "v_mfma_f32_32x32x16_bf16 v[80:95], v[208:211], v[48:51], 0\n"
        "s_add_u32 s51, s51, 0x4000\n"
        "s_and_b32 s51, s51, 0x3ffff\n"
        "v_exp_f32 v64, v64\n"
        "v_exp_f32 v65, v65\n"
        "v_exp_f32 v66, v66\n"
        "v_exp_f32 v67, v67\n"
        "v_cvt_pk_bf16_f32 v224, v64, v65\n"
        "v_cvt_pk_bf16_f32 v225, v66, v67\n"
        "v_mfma_f32_32x32x16_bf16 v[80:95], v[212:215], v[52:55], v[80:95]\n"
        "s_waitcnt vmcnt(29)\n"
        "v_exp_f32 v68, v68\n"
        "v_exp_f32 v69, v69\n"
        "v_exp_f32 v70, v70\n"
        "v_exp_f32 v71, v71\n"
        "v_cvt_pk_bf16_f32 v226, v68, v69\n"
        "v_cvt_pk_bf16_f32 v227, v70, v71\n"
        "v_mfma_f32_32x32x16_bf16 v[96:111], v[204:207], v[236:239], v[96:111]\n"
        "global_load_dwordx4 v[204:207], v1, s[20:21] offset:3072 sc1\n"
        "s_or_b32 s20, s51, s60\n"
        "v_exp_f32 v72, v72\n"
        "v_exp_f32 v73, v73\n"
        "v_exp_f32 v74, v74\n"
        "v_exp_f32 v75, v75\n"
        "v_cvt_pk_bf16_f32 v228, v72, v73\n"
        "v_cvt_pk_bf16_f32 v229, v74, v75\n"
        "v_mfma_f32_32x32x16_bf16 v[112:127], v[216:219], v[224:227], v[112:127]\n"
        "global_load_dwordx4 a[48:51], v1, s[62:63] offset:0 sc1\n"
        "v_exp_f32 v76, v76\n"
        "v_exp_f32 v77, v77\n"
        "v_exp_f32 v78, v78\n"
        "v_exp_f32 v79, v79\n"
        "v_cvt_pk_bf16_f32 v230, v76, v77\n"
        "v_cvt_pk_bf16_f32 v231, v78, v79\n"
        "v_mfma_f32_32x32x16_bf16 v[64:79], v[208:211], v[56:59], 0\n"
        "global_load_lds_dwordx4 v1, s[22:23] offset:2048 sc1\n"
        "v_exp_f32 v80, v80\n"
        "v_exp_f32 v81, v81\n"
        "v_exp_f32 v82, v82\n"
        "v_exp_f32 v83, v83\n"
        "v_cvt_pk_bf16_f32 v232, v80, v81\n"
        "v_cvt_pk_bf16_f32 v233, v82, v83\n"
        "v_mfma_f32_32x32x16_bf16 v[64:79], v[212:215], v[60:63], v[64:79]\n"
        "global_load_dwordx4 a[52:55], v1, s[62:63] offset:1024 sc1\n"
        "v_exp_f32 v84, v84\n"
        "v_exp_f32 v85, v85\n"
        "v_exp_f32 v86, v86\n"
        "v_exp_f32 v87, v87\n"
        "v_cvt_pk_bf16_f32 v234, v84, v85\n"
        "v_cvt_pk_bf16_f32 v235, v86, v87\n"
        "v_mfma_f32_32x32x16_bf16 v[112:127], v[220:223], v[228:231], v[112:127]\n"
        "global_load_lds_dwordx4 v1, s[22:23] offset:3072 sc1\n"
        "s_add_u32 m0, m0, 4096\n"
        "s_add_u32 s22, s22, 4096\n"
        "s_addc_u32 s23, s23, 0\n"
        "v_exp_f32 v88, v88\n"
        "v_exp_f32 v89, v89\n"
        "v_exp_f32 v90, v90\n"
        "v_exp_f32 v91, v91\n"
        "v_cvt_pk_bf16_f32 v236, v88, v89\n"
        "v_cvt_pk_bf16_f32 v237, v90, v91\n"
        "v_mfma_f32_32x32x16_bf16 v[128:143], v[216:219], v[232:235], v[128:143]\n"
        "global_load_dwordx4 a[56:59], v1, s[62:63] offset:2048 sc1\n"
        "v_exp_f32 v92, v92\n"
        "v_exp_f32 v93, v93\n"
        "v_exp_f32 v94, v94\n"
        "v_exp_f32 v95, v95\n"
        "v_cvt_pk_bf16_f32 v238, v92, v93\n"
        "v_cvt_pk_bf16_f32 v239, v94, v95\n"
        "v_mfma_f32_32x32x16_bf16 v[80:95], v[208:211], v[32:35], 0\n"
        "global_load_dwordx4 a[60:63], v1, s[62:63] offset:3072 sc1\n"
        "s_add_u32 s62, s62, 4096\n"
        "s_addc_u32 s63, s63, 0\n"
        "v_exp_f32 v64, v64\n"
        "v_exp_f32 v65, v65\n"
        "v_exp_f32 v66, v66\n"
        "v_exp_f32 v67, v67\n"
        "v_cvt_pk_bf16_f32 v224, v64, v65\n"
        "v_cvt_pk_bf16_f32 v225, v66, v67\n"
        "v_mfma_f32_32x32x16_bf16 v[80:95], v[212:215], v[36:39], v[80:95]\n"
        "s_waitcnt vmcnt(28)\n"
        "v_exp_f32 v68, v68\n"
        "v_exp_f32 v69, v69\n"
        "v_exp_f32 v70, v70\n"
        "v_exp_f32 v71, v71\n"
        "v_cvt_pk_bf16_f32 v226, v68, v69\n"
        "v_cvt_pk_bf16_f32 v227, v70, v71\n"
        "v_mfma_f32_32x32x16_bf16 v[128:143], v[220:223], v[236:239], v[128:143]\n"
        "global_load_dwordx4 v[208:211], v1, s[20:21] offset:0 sc1\n"
        "v_exp_f32 v72, v72\n"
        "v_exp_f32 v73, v73\n"
        "v_exp_f32 v74, v74\n"
        "v_exp_f32 v75, v75\n"
        "v_cvt_pk_bf16_f32 v228, v72, v73\n"
        "v_cvt_pk_bf16_f32 v229, v74, v75\n"
        "v_mfma_f32_32x32x16_bf16 v[144:159], v[216:219], v[224:227], v[144:159]\n"
        "global_load_dwordx4 v[212:215], v1, s[20:21] offset:1024 sc1\n"
        "v_exp_f32 v76, v76\n"
        "v_exp_f32 v77, v77\n"
        "v_exp_f32 v78, v78\n"
        "v_exp_f32 v79, v79\n"
        "v_cvt_pk_bf16_f32 v230, v76, v77\n"
        "v_cvt_pk_bf16_f32 v231, v78, v79\n"
        "v_mfma_f32_32x32x16_bf16 v[64:79], v[160:163], v[40:43], 0\n"
        "v_exp_f32 v80, v80\n"
        "v_exp_f32 v81, v81\n"
        "v_exp_f32 v82, v82\n"
        "v_exp_f32 v83, v83\n"
        "v_cvt_pk_bf16_f32 v232, v80, v81\n"
        "v_cvt_pk_bf16_f32 v233, v82, v83\n"
        "v_mfma_f32_32x32x16_bf16 v[64:79], v[164:167], v[44:47], v[64:79]\n"
        "v_exp_f32 v84, v84\n"
        "v_exp_f32 v85, v85\n"
        "v_exp_f32 v86, v86\n"
        "v_exp_f32 v87, v87\n"
        "v_cvt_pk_bf16_f32 v234, v84, v85\n"
        "v_cvt_pk_bf16_f32 v235, v86, v87\n"
        "v_mfma_f32_32x32x16_bf16 v[144:159], v[220:223], v[228:231], v[144:159]\n"
        "v_exp_f32 v88, v88\n"
        "v_exp_f32 v89, v89\n"
        "v_exp_f32 v90, v90\n"
        "v_exp_f32 v91, v91\n"
        "v_cvt_pk_bf16_f32 v236, v88, v89\n"
        "v_cvt_pk_bf16_f32 v237, v90, v91\n"
        "v_mfma_f32_32x32x16_bf16 v[96:111], v[216:219], v[232:235], v[96:111]\n"
        "global_load_dwordx4 v[216:219], v1, s[20:21] offset:2048 sc1\n"
        "v_exp_f32 v92, v92\n"
        "v_exp_f32 v93, v93\n"
        "v_exp_f32 v94, v94\n"
        "v_exp_f32 v95, v95\n"
        "v_cvt_pk_bf16_f32 v238, v92, v93\n"
        "v_cvt_pk_bf16_f32 v239, v94, v95\n"
        "v_mfma_f32_32x32x16_bf16 v[80:95], v[160:163], v[48:51], 0\n"
        "s_add_u32 s51, s51, 0x4000\n"
        "s_and_b32 s51, s51, 0x3ffff\n"
        "v_exp_f32 v64, v64\n"
        "v_exp_f32 v65, v65\n"
        "v_exp_f32 v66, v66\n"
        "v_exp_f32 v67, v67\n"
        "v_cvt_pk_bf16_f32 v224, v64, v65\n"
        "v_cvt_pk_bf16_f32 v225, v66, v67\n"
        "v_mfma_f32_32x32x16_bf16 v[80:95], v[164:167], v[52:55], v[80:95]\n"
        "s_waitcnt vmcnt(29)\n"
        "v_exp_f32 v68, v68\n"
        "v_exp_f32 v69, v69\n"
        "v_exp_f32 v70, v70\n"
        "v_exp_f32 v71, v71\n"
        "v_cvt_pk_bf16_f32 v226, v68, v69\n"
        "v_cvt_pk_bf16_f32 v227, v70, v71\n"
        "v_mfma_f32_32x32x16_bf16 v[96:111], v[220:223], v[236:239], v[96:111]\n"
        "global_load_dwordx4 v[220:223], v1, s[20:21] offset:3072 sc1\n"
        "s_or_b32 s20, s51, s60\n"
        "v_exp_f32 v72, v72\n"
        "v_exp_f32 v73, v73\n"
        "v_exp_f32 v74, v74\n"
        "v_exp_f32 v75, v75\n"
        "v_cvt_pk_bf16_f32 v228, v72, v73\n"
        "v_cvt_pk_bf16_f32 v229, v74, v75\n"
        "v_mfma_f32_32x32x16_bf16 v[112:127], v[168:171], v[224:227], v[112:127]\n"
        "global_load_dwordx4 a[64:67], v1, s[62:63] offset:0 sc1\n"
        "v_exp_f32 v76, v76\n"
        "v_exp_f32 v77, v77\n"
        "v_exp_f32 v78, v78\n"
        "v_exp_f32 v79, v79\n"
        "v_cvt_pk_bf16_f32 v230, v76, v77\n"
        "v_cvt_pk_bf16_f32 v231, v78, v79\n"
        "v_mfma_f32_32x32x16_bf16 v[64:79], v[160:163], v[56:59], 0\n"
        "global_load_lds_dwordx4 v1, s[22:23] offset:0 sc1\n"
        "v_exp_f32 v80, v80\n"
        "v_exp_f32 v81, v81\n"
        "v_exp_f32 v82, v82\n"
        "v_exp_f32 v83, v83\n"
        "v_cvt_pk_bf16_f32 v232, v80, v81\n"
        "v_cvt_pk_bf16_f32 v233, v82, v83\n"
        "v_mfma_f32_32x32x16_bf16 v[64:79], v[164:167], v[60:63], v[64:79]\n"
        "global_load_dwordx4 a[68:71], v1, s[62:63] offset:1024 sc1\n"
        "v_exp_f32 v84, v84\n"
        "v_exp_f32 v85, v85\n"
        "v_exp_f32 v86, v86\n"
        "v_exp_f32 v87, v87\n"
        "v_cvt_pk_bf16_f32 v234, v84, v85\n"
        "v_cvt_pk_bf16_f32 v235, v86, v87\n"
        "v_mfma_f32_32x32x16_bf16 v[112:127], v[172:175], v[228:231], v[112:127]\n"
        "global_load_lds_dwordx4 v1, s[22:23] offset:1024 sc1\n"
        "v_exp_f32 v88, v88\n"
        "v_exp_f32 v89, v89\n"
        "v_exp_f32 v90, v90\n"
        "v_exp_f32 v91, v91\n"
        "v_cvt_pk_bf16_f32 v236, v88, v89\n"
        "v_cvt_pk_bf16_f32 v237, v90, v91\n"
        "v_mfma_f32_32x32x16_bf16 v[128:143], v[168:171], v[232:235], v[128:143]\n"
        "global_load_dwordx4 a[72:75], v1, s[62:63] offset:2048 sc1\n"
        "v_exp_f32 v92, v92\n"
        "v_exp_f32 v93, v93\n"
        "v_exp_f32 v94, v94\n"
        "v_exp_f32 v95, v95\n"
        "v_cvt_pk_bf16_f32 v238, v92, v93\n"
        "v_cvt_pk_bf16_f32 v239, v94, v95\n"
        "v_mfma_f32_32x32x16_bf16 v[80:95], v[160:163], v[32:35], 0\n"
        "global_load_dwordx4 a[76:79], v1, s[62:63] offset:3072 sc1\n"
        "s_add_u32 s62, s62, 4096\n"
        "s_addc_u32 s63, s63, 0\n"
        "v_exp_f32 v64, v64\n"
        "v_exp_f32 v65, v65\n"
        "v_exp_f32 v66, v66\n"
        "v_exp_f32 v67, v67\n"
        "v_cvt_pk_bf16_f32 v224, v64, v65\n"
        "v_cvt_pk_bf16_f32 v225, v66, v67\n"
        "v_mfma_f32_32x32x16_bf16 v[80:95], v[164:167], v[36:39], v[80:95]\n"
        "s_waitcnt vmcnt(28)\n"
        "v_exp_f32 v68, v68\n"
        "v_exp_f32 v69, v69\n"
        "v_exp_f32 v70, v70\n"
        "v_exp_f32 v71, v71\n"
        "v_cvt_pk_bf16_f32 v226, v68, v69\n"
        "v_cvt_pk_bf16_f32 v227, v70, v71\n"
        "v_mfma_f32_32x32x16_bf16 v[128:143], v[172:175], v[236:239], v[128:143]\n"
        "global_load_dwordx4 v[160:163], v1, s[20:21] offset:0 sc1\n"
        "v_exp_f32 v72, v72\n"
        "v_exp_f32 v73, v73\n"
        "v_exp_f32 v74, v74\n"
        "v_exp_f32 v75, v75\n"
        "v_cvt_pk_bf16_f32 v228, v72, v73\n"
        "v_cvt_pk_bf16_f32 v229, v74, v75\n"
        "v_mfma_f32_32x32x16_bf16 v[144:159], v[168:171], v[224:227], v[144:159]\n"
        "global_load_dwordx4 v[164:167], v1, s[20:21] offset:1024 sc1\n"
        "v_exp_f32 v76, v76\n"
        "v_exp_f32 v77, v77\n"
        "v_exp_f32 v78, v78\n"
        "v_exp_f32 v79, v79\n"
        "v_cvt_pk_bf16_f32 v230, v76, v77\n"
        "v_cvt_pk_bf16_f32 v231, v78, v79\n"
        "v_mfma_f32_32x32x16_bf16 v[64:79], v[176:179], v[40:43], 0\n"
        "v_exp_f32 v80, v80\n"
        "v_exp_f32 v81, v81\n"
        "v_exp_f32 v82, v82\n"
        "v_exp_f32 v83, v83\n"
        "v_cvt_pk_bf16_f32 v232, v80, v81\n"
        "v_cvt_pk_bf16_f32 v233, v82, v83\n"
        "v_mfma_f32_32x32x16_bf16 v[64:79], v[180:183], v[44:47], v[64:79]\n"
        "v_exp_f32 v84, v84\n"
        "v_exp_f32 v85, v85\n"
        "v_exp_f32 v86, v86\n"
        "v_exp_f32 v87, v87\n"
        "v_cvt_pk_bf16_f32 v234, v84, v85\n"
        "v_cvt_pk_bf16_f32 v235, v86, v87\n"
        "v_mfma_f32_32x32x16_bf16 v[144:159], v[172:175], v[228:231], v[144:159]\n"
        "v_exp_f32 v88, v88\n"
        "v_exp_f32 v89, v89\n"
        "v_exp_f32 v90, v90\n"
        "v_exp_f32 v91, v91\n"
        "v_cvt_pk_bf16_f32 v236, v88, v89\n"
        "v_cvt_pk_bf16_f32 v237, v90, v91\n"
        "v_mfma_f32_32x32x16_bf16 v[96:111], v[168:171], v[232:235], v[96:111]\n"
        "global_load_dwordx4 v[168:171], v1, s[20:21] offset:2048 sc1\n"
        "v_exp_f32 v92, v92\n"
        "v_exp_f32 v93, v93\n"
        "v_exp_f32 v94, v94\n"
        "v_exp_f32 v95, v95\n"
        "v_cvt_pk_bf16_f32 v238, v92, v93\n"
        "v_cvt_pk_bf16_f32 v239, v94, v95\n"
        "v_mfma_f32_32x32x16_bf16 v[80:95], v[176:179], v[48:51], 0\n"
        "s_add_u32 s51, s51, 0x4000\n"
        "s_and_b32 s51, s51, 0x3ffff\n"
        "v_exp_f32 v64, v64\n"
        "v_exp_f32 v65, v65\n"
        "v_exp_f32 v66, v66\n"
        "v_exp_f32 v67, v67\n"
        "v_cvt_pk_bf16_f32 v224, v64, v65\n"
        "v_cvt_pk_bf16_f32 v225, v66, v67\n"
        "v_mfma_f32_32x32x16_bf16 v[80:95], v[180:183], v[52:55], v[80:95]\n"
        "s_waitcnt vmcnt(29)\n"
        "v_exp_f32 v68, v68\n"
        "v_exp_f32 v69, v69\n"
        "v_exp_f32 v70, v70\n"
        "v_exp_f32 v71, v71\n"
        "v_cvt_pk_bf16_f32 v226, v68, v69\n"
        "v_cvt_pk_bf16_f32 v227, v70, v71\n"
        "v_mfma_f32_32x32x16_bf16 v[96:111], v[172:175], v[236:239], v[96:111]\n"
        "global_load_dwordx4 v[172:175], v1, s[20:21] offset:3072 sc1\n"
        "s_or_b32 s20, s51, s60\n"
        "v_exp_f32 v72, v72\n"
        "v_exp_f32 v73, v73\n"
        "v_exp_f32 v74, v74\n"
        "v_exp_f32 v75, v75\n"
        "v_cvt_pk_bf16_f32 v228, v72, v73\n"
        "v_cvt_pk_bf16_f32 v229, v74, v75\n"
        "v_mfma_f32_32x32x16_bf16 v[112:127], v[184:187], v[224:227], v[112:127]\n"
        "global_load_dwordx4 a[80:83], v1, s[62:63] offset:0 sc1\n"
        "v_exp_f32 v76, v76\n"
        "v_exp_f32 v77, v77\n"
        "v_exp_f32 v78, v78\n"
        "v_exp_f32 v79, v79\n"
        "v_cvt_pk_bf16_f32 v230, v76, v77\n"
        "v_cvt_pk_bf16_f32 v231, v78, v79\n"
        "v_mfma_f32_32x32x16_bf16 v[64:79], v[176:179], v[56:59], 0\n"
        "global_load_lds_dwordx4 v1, s[22:23] offset:2048 sc1\n"
        "v_exp_f32 v80, v80\n"
        "v_exp_f32 v81, v81\n"
        "v_exp_f32 v82, v82\n"
        "v_exp_f32 v83, v83\n"
        "v_cvt_pk_bf16_f32 v232, v80, v81\n"
        "v_cvt_pk_bf16_f32 v233, v82, v83\n"
        "v_mfma_f32_32x32x16_bf16 v[64:79], v[180:183], v[60:63], v[64:79]\n"
        "global_load_dwordx4 a[84:87], v1, s[62:63] offset:1024 sc1\n"
        "v_exp_f32 v84, v84\n"
        "v_exp_f32 v85, v85\n"
        "v_exp_f32 v86, v86\n"
        "v_exp_f32 v87, v87\n"
        "v_cvt_pk_bf16_f32 v234, v84, v85\n"
        "v_cvt_pk_bf16_f32 v235, v86, v87\n"
        "v_mfma_f32_32x32x16_bf16 v[112:127], v[188:191], v[228:231], v[112:127]\n"
        "global_load_lds_dwordx4 v1, s[22:23] offset:3072 sc1\n"
        "s_add_u32 m0, m0, 4096\n"
        "s_add_u32 s22, s22, 4096\n"
        "s_addc_u32 s23, s23, 0\n"
        "v_exp_f32 v88, v88\n"
        "v_exp_f32 v89, v89\n"
        "v_exp_f32 v90, v90\n"
        "v_exp_f32 v91, v91\n"
        "v_cvt_pk_bf16_f32 v236, v88, v89\n"
        "v_cvt_pk_bf16_f32 v237, v90, v91\n"
        "v_mfma_f32_32x32x16_bf16 v[128:143], v[184:187], v[232:235], v[128:143]\n"
        "global_load_dwordx4 a[88:91], v1, s[62:63] offset:2048 sc1\n"
        "v_exp_f32 v92, v92\n"
        "v_exp_f32 v93, v93\n"
        "v_exp_f32 v94, v94\n"
        "v_exp_f32 v95, v95\n"
        "v_cvt_pk_bf16_f32 v238, v92, v93\n"
        "v_cvt_pk_bf16_f32 v239, v94, v95\n"
        "v_mfma_f32_32x32x16_bf16 v[80:95], v[176:179], v[32:35], 0\n"
        "global_load_dwordx4 a[92:95], v1, s[62:63] offset:3072 sc1\n"
        "s_add_u32 s62, s62, 4096\n"
        "s_addc_u32 s63, s63, 0\n"
        "v_exp_f32 v64, v64\n"
        "v_exp_f32 v65, v65\n"
        "v_exp_f32 v66, v66\n"
        "v_exp_f32 v67, v67\n"
        "v_cvt_pk_bf16_f32 v224, v64, v65\n"
        "v_cvt_pk_bf16_f32 v225, v66, v67\n"
        "v_mfma_f32_32x32x16_bf16 v[80:95], v[180:183], v[36:39], v[80:95]\n"
        "s_waitcnt vmcnt(28)\n"
        "v_exp_f32 v68, v68\n"
        "v_exp_f32 v69, v69\n"
        "v_exp_f32 v70, v70\n"
        "v_exp_f32 v71, v71\n"
        "v_cvt_pk_bf16_f32 v226, v68, v69\n"
        "v_cvt_pk_bf16_f32 v227, v70, v71\n"
        "v_mfma_f32_32x32x16_bf16 v[128:143], v[188:191], v[236:239], v[128:143]\n"
        "global_load_dwordx4 v[176:179], v1, s[20:21] offset:0 sc1\n"
        "v_exp_f32 v72, v72\n"
        "v_exp_f32 v73, v73\n"
        "v_exp_f32 v74, v74\n"
        "v_exp_f32 v75, v75\n"
        "v_cvt_pk_bf16_f32 v228, v72, v73\n"
        "v_cvt_pk_bf16_f32 v229, v74, v75\n"
        "v_mfma_f32_32x32x16_bf16 v[144:159], v[184:187], v[224:227], v[144:159]\n"
        "global_load_dwordx4 v[180:183], v1, s[20:21] offset:1024 sc1\n"
        "v_exp_f32 v76, v76\n"
        "v_exp_f32 v77, v77\n"
        "v_exp_f32 v78, v78\n"
        "v_exp_f32 v79, v79\n"
        "v_cvt_pk_bf16_f32 v230, v76, v77\n"
        "v_cvt_pk_bf16_f32 v231, v78, v79\n"
        "v_mfma_f32_32x32x16_bf16 v[64:79], v[192:195], v[40:43], 0\n"
        "v_exp_f32 v80, v80\n"
        "v_exp_f32 v81, v81\n"
        "v_exp_f32 v82, v82\n"
        "v_exp_f32 v83, v83\n"
        "v_cvt_pk_bf16_f32 v232, v80, v81\n"
        "v_cvt_pk_bf16_f32 v233, v82, v83\n"
        "v_mfma_f32_32x32x16_bf16 v[64:79], v[196:199], v[44:47], v[64:79]\n"
        "v_exp_f32 v84, v84\n"
        "v_exp_f32 v85, v85\n"
        "v_exp_f32 v86, v86\n"
        "v_exp_f32 v87, v87\n"
        "v_cvt_pk_bf16_f32 v234, v84, v85\n"
        "v_cvt_pk_bf16_f32 v235, v86, v87\n"
        "v_mfma_f32_32x32x16_bf16 v[144:159], v[188:191], v[228:231], v[144:159]\n"
        "v_exp_f32 v88, v88\n"
        "v_exp_f32 v89, v89\n"
        "v_exp_f32 v90, v90\n"
        "v_exp_f32 v91, v91\n"
        "v_cvt_pk_bf16_f32 v236, v88, v89\n"
        "v_cvt_pk_bf16_f32 v237, v90, v91\n"
        "v_mfma_f32_32x32x16_bf16 v[96:111], v[184:187], v[232:235], v[96:111]\n"
        "global_load_dwordx4 v[184:187], v1, s[20:21] offset:2048 sc1\n"
        "v_exp_f32 v92, v92\n"
        "v_exp_f32 v93, v93\n"
        "v_exp_f32 v94, v94\n"
        "v_exp_f32 v95, v95\n"
        "v_cvt_pk_bf16_f32 v238, v92, v93\n"
        "v_cvt_pk_bf16_f32 v239, v94, v95\n"
        "v_mfma_f32_32x32x16_bf16 v[80:95], v[192:195], v[48:51], 0\n"
        "s_add_u32 s51, s51, 0x4000\n"
        "s_and_b32 s51, s51, 0x3ffff\n"
        "v_exp_f32 v64, v64\n"
        "v_exp_f32 v65, v65\n"
        "v_exp_f32 v66, v66\n"
        "v_exp_f32 v67, v67\n"
        "v_cvt_pk_bf16_f32 v224, v64, v65\n"
        "v_cvt_pk_bf16_f32 v225, v66, v67\n"
        "v_mfma_f32_32x32x16_bf16 v[80:95], v[196:199], v[52:55], v[80:95]\n"
        "s_waitcnt vmcnt(29)\n"
        "v_exp_f32 v68, v68\n"
        "v_exp_f32 v69, v69\n"
        "v_exp_f32 v70, v70\n"
        "v_exp_f32 v71, v71\n"
        "v_cvt_pk_bf16_f32 v226, v68, v69\n"
        "v_cvt_pk_bf16_f32 v227, v70, v71\n"
        "v_mfma_f32_32x32x16_bf16 v[96:111], v[188:191], v[236:239], v[96:111]\n"
        "global_load_dwordx4 v[188:191], v1, s[20:21] offset:3072 sc1\n"
        "s_or_b32 s20, s51, s60\n"
        "v_exp_f32 v72, v72\n"
        "v_exp_f32 v73, v73\n"
        "v_exp_f32 v74, v74\n"
        "v_exp_f32 v75, v75\n"
        "v_cvt_pk_bf16_f32 v228, v72, v73\n"
        "v_cvt_pk_bf16_f32 v229, v74, v75\n"
        "v_mfma_f32_32x32x16_bf16 v[112:127], v[200:203], v[224:227], v[112:127]\n"
        "global_load_dwordx4 a[96:99], v1, s[62:63] offset:0 sc1\n"
        "v_exp_f32 v76, v76\n"
        "v_exp_f32 v77, v77\n"
        "v_exp_f32 v78, v78\n"
        "v_exp_f32 v79, v79\n"
        "v_cvt_pk_bf16_f32 v230, v76, v77\n"
        "v_cvt_pk_bf16_f32 v231, v78, v79\n"
        "v_mfma_f32_32x32x16_bf16 v[64:79], v[192:195], v[56:59], 0\n"
        "global_load_lds_dwordx4 v1, s[22:23] offset:0 sc1\n"
        "v_exp_f32 v80, v80\n"
        "v_exp_f32 v81, v81\n"
        "v_exp_f32 v82, v82\n"
        "v_exp_f32 v83, v83\n"
        "v_cvt_pk_bf16_f32 v232, v80, v81\n"
        "v_cvt_pk_bf16_f32 v233, v82, v83\n"
        "v_mfma_f32_32x32x16_bf16 v[64:79], v[196:199], v[60:63], v[64:79]\n"
        "global_load_dwordx4 a[100:103], v1, s[62:63] offset:1024 sc1\n"
        "v_exp_f32 v84, v84\n"
        "v_exp_f32 v85, v85\n"
        "v_exp_f32 v86, v86\n"
        "v_exp_f32 v87, v87\n"
        "v_cvt_pk_bf16_f32 v234, v84, v85\n"
        "v_cvt_pk_bf16_f32 v235, v86, v87\n"
        "v_mfma_f32_32x32x16_bf16 v[112:127], v[204:207], v[228:231], v[112:127]\n"
        "global_load_lds_dwordx4 v1, s[22:23] offset:1024 sc1\n"
        "v_exp_f32 v88, v88\n"
        "v_exp_f32 v89, v89\n"
        "v_exp_f32 v90, v90\n"
        "v_exp_f32 v91, v91\n"
        "v_cvt_pk_bf16_f32 v236, v88, v89\n"
        "v_cvt_pk_bf16_f32 v237, v90, v91\n"
        "v_mfma_f32_32x32x16_bf16 v[128:143], v[200:203], v[232:235], v[128:143]\n"
        "global_load_dwordx4 a[104:107], v1, s[62:63] offset:2048 sc1\n"
        "v_exp_f32 v92, v92\n"
        "v_exp_f32 v93, v93\n"
        "v_exp_f32 v94, v94\n"
        "v_exp_f32 v95, v95\n"
        "v_cvt_pk_bf16_f32 v238, v92, v93\n"
        "v_cvt_pk_bf16_f32 v239, v94, v95\n"
        "v_mfma_f32_32x32x16_bf16 v[80:95], v[192:195], v[32:35], 0\n"
        "global_load_dwordx4 a[108:111], v1, s[62:63] offset:3072 sc1\n"
        "s_add_u32 s62, s62, s61\n"
        "s_addc_u32 s63, s63, s75\n"
        "v_exp_f32 v64, v64\n"
        "v_exp_f32 v65, v65\n"
        "v_exp_f32 v66, v66\n"
        "v_exp_f32 v67, v67\n"
        "v_cvt_pk_bf16_f32 v224, v64, v65\n"
        "v_cvt_pk_bf16_f32 v225, v66, v67\n"
        "v_mfma_f32_32x32x16_bf16 v[80:95], v[196:199], v[36:39], v[80:95]\n"
        "s_waitcnt vmcnt(28)\n"
        "v_exp_f32 v68, v68\n"
        "v_exp_f32 v69, v69\n"
        "v_exp_f32 v70, v70\n"
        "v_exp_f32 v71, v71\n"
        "v_cvt_pk_bf16_f32 v226, v68, v69\n"
        "v_cvt_pk_bf16_f32 v227, v70, v71\n"
        "v_mfma_f32_32x32x16_bf16 v[128:143], v[204:207], v[236:239], v[128:143]\n"
        "global_load_dwordx4 v[192:195], v1, s[20:21] offset:0 sc1\n"
        "v_exp_f32 v72, v72\n"
        "v_exp_f32 v73, v73\n"
        "v_exp_f32 v74, v74\n"
        "v_exp_f32 v75, v75\n"
        "v_cvt_pk_bf16_f32 v228, v72, v73\n"
        "v_cvt_pk_bf16_f32 v229, v74, v75\n"
        "v_mfma_f32_32x32x16_bf16 v[144:159], v[200:203], v[224:227], v[144:159]\n"
        "global_load_dwordx4 v[196:199], v1, s[20:21] offset:1024 sc1\n"
        "v_exp_f32 v76, v76\n"
        "v_exp_f32 v77, v77\n"
        "v_exp_f32 v78, v78\n"
        "v_exp_f32 v79, v79\n"
        "v_cvt_pk_bf16_f32 v230, v76, v77\n"
        "v_cvt_pk_bf16_f32 v231, v78, v79\n"
        "v_mfma_f32_32x32x16_bf16 v[64:79], v[208:211], v[40:43], 0\n"
        "v_exp_f32 v80, v80\n"
        "v_exp_f32 v81, v81\n"
        "v_exp_f32 v82, v82\n"
        "v_exp_f32 v83, v83\n"
        "v_cvt_pk_bf16_f32 v232, v80, v81\n"
        "v_cvt_pk_bf16_f32 v233, v82, v83\n"
        "v_mfma_f32_32x32x16_bf16 v[64:79], v[212:215], v[44:47], v[64:79]\n"
        "v_exp_f32 v84, v84\n"
        "v_exp_f32 v85, v85\n"
        "v_exp_f32 v86, v86\n"
        "v_exp_f32 v87, v87\n"
        "v_cvt_pk_bf16_f32 v234, v84, v85\n"
        "v_cvt_pk_bf16_f32 v235, v86, v87\n"
        "v_mfma_f32_32x32x16_bf16 v[144:159], v[204:207], v[228:231], v[144:159]\n"
        "v_exp_f32 v88, v88\n"
        "v_exp_f32 v89, v89\n"
        "v_exp_f32 v90, v90\n"
        "v_exp_f32 v91, v91\n"
        "v_cvt_pk_bf16_f32 v236, v88, v89\n"
        "v_cvt_pk_bf16_f32 v237, v90, v91\n"
        "v_mfma_f32_32x32x16_bf16 v[96:111], v[200:203], v[232:235], v[96:111]\n"
        "global_load_dwordx4 v[200:203], v1, s[20:21] offset:2048 sc1\n"
        "v_exp_f32 v92, v92\n"
        "v_exp_f32 v93, v93\n"
        "v_exp_f32 v94, v94\n"
        "v_exp_f32 v95, v95\n"
        "v_cvt_pk_bf16_f32 v238, v92, v93\n"
        "v_cvt_pk_bf16_f32 v239, v94, v95\n"
        "v_mfma_f32_32x32x16_bf16 v[80:95], v[208:211], v[48:51], 0\n"
        "s_add_u32 s51, s51, 0x4000\n"
        "s_and_b32 s51, s51, 0x3ffff\n"
        "v_exp_f32 v64, v64\n"
        "v_exp_f32 v65, v65\n"
        "v_exp_f32 v66, v66\n"
        "v_exp_f32 v67, v67\n"
        "v_cvt_pk_bf16_f32 v224, v64, v65\n"
        "v_cvt_pk_bf16_f32 v225, v66, v67\n"
        "v_mfma_f32_32x32x16_bf16 v[80:95], v[212:215], v[52:55], v[80:95]\n"
        "s_waitcnt vmcnt(29)\n"
        "v_exp_f32 v68, v68\n"
        "v_exp_f32 v69, v69\n"
        "v_exp_f32 v70, v70\n"
        "v_exp_f32 v71, v71\n"
        "v_cvt_pk_bf16_f32 v226, v68, v69\n"
        "v_cvt_pk_bf16_f32 v227, v70, v71\n"
        "v_mfma_f32_32x32x16_bf16 v[96:111], v[204:207], v[236:239], v[96:111]\n"
        "global_load_dwordx4 v[204:207], v1, s[20:21] offset:3072 sc1\n"
        "s_or_b32 s20, s51, s60\n"
        "v_exp_f32 v72, v72\n"
        "v_exp_f32 v73, v73\n"
        "v_exp_f32 v74, v74\n"
        "v_exp_f32 v75, v75\n"
        "v_cvt_pk_bf16_f32 v228, v72, v73\n"
        "v_cvt_pk_bf16_f32 v229, v74, v75\n"
        "v_mfma_f32_32x32x16_bf16 v[112:127], v[216:219], v[224:227], v[112:127]\n"
        "global_load_dwordx4 a[112:115], v1, s[62:63] offset:0 sc1\n"
        "v_exp_f32 v76, v76\n"
        "v_exp_f32 v77, v77\n"
        "v_exp_f32 v78, v78\n"
        "v_exp_f32 v79, v79\n"
        "v_cvt_pk_bf16_f32 v230, v76, v77\n"
        "v_cvt_pk_bf16_f32 v231, v78, v79\n"
        "v_mfma_f32_32x32x16_bf16 v[64:79], v[208:211], v[56:59], 0\n"
        "global_load_lds_dwordx4 v1, s[22:23] offset:2048 sc1\n"
        "v_exp_f32 v80, v80\n"
        "v_exp_f32 v81, v81\n"
        "v_exp_f32 v82, v82\n"
        "v_exp_f32 v83, v83\n"
        "v_cvt_pk_bf16_f32 v232, v80, v81\n"
        "v_cvt_pk_bf16_f32 v233, v82, v83\n"
        "v_mfma_f32_32x32x16_bf16 v[64:79], v[212:215], v[60:63], v[64:79]\n"
        "global_load_dwordx4 a[116:119], v1, s[62:63] offset:1024 sc1\n"
        "v_exp_f32 v84, v84\n"
        "v_exp_f32 v85, v85\n"
        "v_exp_f32 v86, v86\n"
        "v_exp_f32 v87, v87\n"
        "v_cvt_pk_bf16_f32 v234, v84, v85\n"
        "v_cvt_pk_bf16_f32 v235, v86, v87\n"
        "v_mfma_f32_32x32x16_bf16 v[112:127], v[220:223], v[228:231], v[112:127]\n"
        "global_load_lds_dwordx4 v1, s[22:23] offset:3072 sc1\n"
        "s_add_u32 m0, m0, 4096\n"
        "s_add_u32 s22, s22, 4096\n"
        "s_addc_u32 s23, s23, 0\n"
        "v_exp_f32 v88, v88\n"
        "v_exp_f32 v89, v89\n"
        "v_exp_f32 v90, v90\n"
        "v_exp_f32 v91, v91\n"
        "v_cvt_pk_bf16_f32 v236, v88, v89\n"
        "v_cvt_pk_bf16_f32 v237, v90, v91\n"
        "v_mfma_f32_32x32x16_bf16 v[128:143], v[216:219], v[232:235], v[128:143]\n"
        "global_load_dwordx4 a[120:123], v1, s[62:63] offset:2048 sc1\n"
        "v_exp_f32 v92, v92\n"
        "v_exp_f32 v93, v93\n"
        "v_exp_f32 v94, v94\n"
        "v_exp_f32 v95, v95\n"
        "v_cvt_pk_bf16_f32 v238, v92, v93\n"
        "v_cvt_pk_bf16_f32 v239, v94, v95\n"
        "v_mfma_f32_32x32x16_bf16 v[80:95], v[208:211], v[32:35], 0\n"
        "global_load_dwordx4 a[124:127], v1, s[62:63] offset:3072 sc1\n"
        "s_add_u32 s62, s62, 4096\n"
        "s_addc_u32 s63, s63, 0\n"
        "v_exp_f32 v64, v64\n"
        "v_exp_f32 v65, v65\n"
        "v_exp_f32 v66, v66\n"
        "v_exp_f32 v67, v67\n"
        "v_cvt_pk_bf16_f32 v224, v64, v65\n"
        "v_cvt_pk_bf16_f32 v225, v66, v67\n"
        "v_mfma_f32_32x32x16_bf16 v[80:95], v[212:215], v[36:39], v[80:95]\n"
        "s_waitcnt vmcnt(28)\n"
        "v_exp_f32 v68, v68\n"
        "v_exp_f32 v69, v69\n"
        "v_exp_f32 v70, v70\n"
        "v_exp_f32 v71, v71\n"
        "v_cvt_pk_bf16_f32 v226, v68, v69\n"
        "v_cvt_pk_bf16_f32 v227, v70, v71\n"
        "v_mfma_f32_32x32x16_bf16 v[128:143], v[220:223], v[236:239], v[128:143]\n"
        "global_load_dwordx4 v[208:211], v1, s[20:21] offset:0 sc1\n"
        "v_exp_f32 v72, v72\n"
        "v_exp_f32 v73, v73\n"
        "v_exp_f32 v74, v74\n"
        "v_exp_f32 v75, v75\n"
        "v_cvt_pk_bf16_f32 v228, v72, v73\n"
        "v_cvt_pk_bf16_f32 v229, v74, v75\n"
        "v_mfma_f32_32x32x16_bf16 v[144:159], v[216:219], v[224:227], v[144:159]\n"
        "global_load_dwordx4 v[212:215], v1, s[20:21] offset:1024 sc1\n"
        "v_exp_f32 v76, v76\n"
        "v_exp_f32 v77, v77\n"
        "v_exp_f32 v78, v78\n"
        "v_exp_f32 v79, v79\n"
        "v_cvt_pk_bf16_f32 v230, v76, v77\n"
        "v_cvt_pk_bf16_f32 v231, v78, v79\n"
        "v_mfma_f32_32x32x16_bf16 v[64:79], v[160:163], v[40:43], 0\n"
        "v_exp_f32 v80, v80\n"
        "v_exp_f32 v81, v81\n"
        "v_exp_f32 v82, v82\n"
        "v_exp_f32 v83, v83\n"
        "v_cvt_pk_bf16_f32 v232, v80, v81\n"
        "v_cvt_pk_bf16_f32 v233, v82, v83\n"
        "v_mfma_f32_32x32x16_bf16 v[64:79], v[164:167], v[44:47], v[64:79]\n"
        "v_exp_f32 v84, v84\n"
        "v_exp_f32 v85, v85\n"
        "v_exp_f32 v86, v86\n"
        "v_exp_f32 v87, v87\n"
        "v_cvt_pk_bf16_f32 v234, v84, v85\n"
        "v_cvt_pk_bf16_f32 v235, v86, v87\n"
        "v_mfma_f32_32x32x16_bf16 v[144:159], v[220:223], v[228:231], v[144:159]\n"
        "v_exp_f32 v88, v88\n"
        "v_exp_f32 v89, v89\n"
        "v_exp_f32 v90, v90\n"
        "v_exp_f32 v91, v91\n"
        "v_cvt_pk_bf16_f32 v236, v88, v89\n"
        "v_cvt_pk_bf16_f32 v237, v90, v91\n"
        "v_mfma_f32_32x32x16_bf16 v[96:111], v[216:219], v[232:235], v[96:111]\n"
        "global_load_dwordx4 v[216:219], v1, s[20:21] offset:2048 sc1\n"
        "v_exp_f32 v92, v92\n"
        "v_exp_f32 v93, v93\n"
        "v_exp_f32 v94, v94\n"
        "v_exp_f32 v95, v95\n"
        "v_cvt_pk_bf16_f32 v238, v92, v93\n"
        "v_cvt_pk_bf16_f32 v239, v94, v95\n"
        "v_mfma_f32_32x32x16_bf16 v[80:95], v[160:163], v[48:51], 0\n"
        "global_load_dwordx4 a[128:131], v1, s[62:63] offset:0 sc1\n"
        "v_exp_f32 v64, v64\n"
        "v_exp_f32 v65, v65\n"
        "v_exp_f32 v66, v66\n"
        "v_exp_f32 v67, v67\n"
        "v_cvt_pk_bf16_f32 v224, v64, v65\n"
        "v_cvt_pk_bf16_f32 v225, v66, v67\n"
        "v_mfma_f32_32x32x16_bf16 v[80:95], v[164:167], v[52:55], v[80:95]\n"
        "s_waitcnt vmcnt(30)\n"
        "v_exp_f32 v68, v68\n"
        "v_exp_f32 v69, v69\n"
        "v_exp_f32 v70, v70\n"
        "v_exp_f32 v71, v71\n"
        "v_cvt_pk_bf16_f32 v226, v68, v69\n"
        "v_cvt_pk_bf16_f32 v227, v70, v71\n"
        "v_mfma_f32_32x32x16_bf16 v[96:111], v[220:223], v[236:239], v[96:111]\n"
        "global_load_dwordx4 v[220:223], v1, s[20:21] offset:3072 sc1\n"
        "v_exp_f32 v72, v72\n"
        "v_exp_f32 v73, v73\n"
        "v_exp_f32 v74, v74\n"
        "v_exp_f32 v75, v75\n"
        "v_cvt_pk_bf16_f32 v228, v72, v73\n"
        "v_cvt_pk_bf16_f32 v229, v74, v75\n"
        "v_mfma_f32_32x32x16_bf16 v[112:127], v[168:171], v[224:227], v[112:127]\n"
        "global_load_lds_dwordx4 v1, s[22:23] offset:0 sc1\n"
        "v_exp_f32 v76, v76\n"
        "v_exp_f32 v77, v77\n"
        "v_exp_f32 v78, v78\n"
        "v_exp_f32 v79, v79\n"
        "v_cvt_pk_bf16_f32 v230, v76, v77\n"
        "v_cvt_pk_bf16_f32 v231, v78, v79\n"
        "v_mfma_f32_32x32x16_bf16 v[64:79], v[160:163], v[56:59], 0\n"
        "global_load_dwordx4 a[132:135], v1, s[62:63] offset:1024 sc1\n"
        "v_exp_f32 v80, v80\n"
        "v_exp_f32 v81, v81\n"
        "v_exp_f32 v82, v82\n"
        "v_exp_f32 v83, v83\n"
        "v_cvt_pk_bf16_f32 v232, v80, v81\n"
        "v_cvt_pk_bf16_f32 v233, v82, v83\n"
        "v_mfma_f32_32x32x16_bf16 v[64:79], v[164:167], v[60:63], v[64:79]\n"
        "global_load_lds_dwordx4 v1, s[22:23] offset:1024 sc1\n"
        "v_exp_f32 v84, v84\n"
        "v_exp_f32 v85, v85\n"
        "v_exp_f32 v86, v86\n"
        "v_exp_f32 v87, v87\n"
        "v_cvt_pk_bf16_f32 v234, v84, v85\n"
        "v_cvt_pk_bf16_f32 v235, v86, v87\n"
        "v_mfma_f32_32x32x16_bf16 v[112:127], v[172:175], v[228:231], v[112:127]\n"
        "global_load_dwordx4 a[136:139], v1, s[62:63] offset:2048 sc1\n"
        "v_exp_f32 v88, v88\n"
        "v_exp_f32 v89, v89\n"
        "v_exp_f32 v90, v90\n"
        "v_exp_f32 v91, v91\n"
        "v_cvt_pk_bf16_f32 v236, v88, v89\n"
        "v_cvt_pk_bf16_f32 v237, v90, v91\n"
        "v_mfma_f32_32x32x16_bf16 v[128:143], v[168:171], v[232:235], v[128:143]\n"
        "global_load_lds_dwordx4 v1, s[22:23] offset:2048 sc1\n"
        "v_exp_f32 v92, v92\n"
        "v_exp_f32 v93, v93\n"
        "v_exp_f32 v94, v94\n"
        "v_exp_f32 v95, v95\n"
        "v_cvt_pk_bf16_f32 v238, v92, v93\n"
        "v_cvt_pk_bf16_f32 v239, v94, v95\n"
        "v_mfma_f32_32x32x16_bf16 v[80:95], v[160:163], v[32:35], 0\n"
        "global_load_dwordx4 a[140:143], v1, s[62:63] offset:3072 sc1\n"
        "s_add_u32 s62, s62, 4096\n"
        "s_addc_u32 s63, s63, 0\n"
        "v_exp_f32 v64, v64\n"
        "v_exp_f32 v65, v65\n"
        "v_exp_f32 v66, v66\n"
        "v_exp_f32 v67, v67\n"
        "v_cvt_pk_bf16_f32 v224, v64, v65\n"
        "v_cvt_pk_bf16_f32 v225, v66, v67\n"
        "v_mfma_f32_32x32x16_bf16 v[80:95], v[164:167], v[36:39], v[80:95]\n"
        "s_waitcnt vmcnt(29)\n"
        "v_exp_f32 v68, v68\n"
        "v_exp_f32 v69, v69\n"
        "v_exp_f32 v70, v70\n"
        "v_exp_f32 v71, v71\n"
        "v_cvt_pk_bf16_f32 v226, v68, v69\n"
        "v_cvt_pk_bf16_f32 v227, v70, v71\n"
        "v_mfma_f32_32x32x16_bf16 v[128:143], v[172:175], v[236:239], v[128:143]\n"
        "global_load_dwordx4 a[144:147], v1, s[62:63] offset:0 sc1\n"
        "v_exp_f32 v72, v72\n"
        "v_exp_f32 v73, v73\n"
        "v_exp_f32 v74, v74\n"
        "v_exp_f32 v75, v75\n"
        "v_cvt_pk_bf16_f32 v228, v72, v73\n"
        "v_cvt_pk_bf16_f32 v229, v74, v75\n"
        "v_mfma_f32_32x32x16_bf16 v[144:159], v[168:171], v[224:227], v[144:159]\n"
        "global_load_dwordx4 a[148:151], v1, s[62:63] offset:1024 sc1\n"
        "v_exp_f32 v76, v76\n"
        "v_exp_f32 v77, v77\n"
        "v_exp_f32 v78, v78\n"
        "v_exp_f32 v79, v79\n"
        "v_cvt_pk_bf16_f32 v230, v76, v77\n"
        "v_cvt_pk_bf16_f32 v231, v78, v79\n"
        "v_mfma_f32_32x32x16_bf16 v[64:79], v[176:179], v[40:43], 0\n"
        "global_load_dwordx4 a[152:155], v1, s[62:63] offset:2048 sc1\n"
        "v_exp_f32 v80, v80\n"
        "v_exp_f32 v81, v81\n"
        "v_exp_f32 v82, v82\n"
        "v_exp_f32 v83, v83\n"
        "v_cvt_pk_bf16_f32 v232, v80, v81\n"
        "v_cvt_pk_bf16_f32 v233, v82, v83\n"
        "v_mfma_f32_32x32x16_bf16 v[64:79], v[180:183], v[44:47], v[64:79]\n"
        "v_exp_f32 v84, v84\n"
        "v_exp_f32 v85, v85\n"
        "v_exp_f32 v86, v86\n"
        "v_exp_f32 v87, v87\n"
        "v_cvt_pk_bf16_f32 v234, v84, v85\n"
        "v_cvt_pk_bf16_f32 v235, v86, v87\n"
        "v_mfma_f32_32x32x16_bf16 v[144:159], v[172:175], v[228:231], v[144:159]\n"
        "v_exp_f32 v88, v88\n"
        "v_exp_f32 v89, v89\n"
        "v_exp_f32 v90, v90\n"
        "v_exp_f32 v91, v91\n"
        "v_cvt_pk_bf16_f32 v236, v88, v89\n"
        "v_cvt_pk_bf16_f32 v237, v90, v91\n"
        "v_mfma_f32_32x32x16_bf16 v[96:111], v[168:171], v[232:235], v[96:111]\n"
        "v_exp_f32 v92, v92\n"
        "v_exp_f32 v93, v93\n"
        "v_exp_f32 v94, v94\n"
        "v_exp_f32 v95, v95\n"
        "v_cvt_pk_bf16_f32 v238, v92, v93\n"
        "v_cvt_pk_bf16_f32 v239, v94, v95\n"
        "v_mfma_f32_32x32x16_bf16 v[80:95], v[176:179], v[48:51], 0\n"
        "global_load_dwordx4 a[156:159], v1, s[62:63] offset:3072 sc1\n"
        "s_add_u32 s62, s62, 4096\n"
        "s_addc_u32 s63, s63, 0\n"
        "v_exp_f32 v64, v64\n"
        "v_exp_f32 v65, v65\n"
        "v_exp_f32 v66, v66\n"
        "v_exp_f32 v67, v67\n"
        "v_cvt_pk_bf16_f32 v224, v64, v65\n"
        "v_cvt_pk_bf16_f32 v225, v66, v67\n"
        "v_mfma_f32_32x32x16_bf16 v[80:95], v[180:183], v[52:55], v[80:95]\n"
        "s_waitcnt vmcnt(31)\n"
        "v_exp_f32 v68, v68\n"
        "v_exp_f32 v69, v69\n"
        "v_exp_f32 v70, v70\n"
        "v_exp_f32 v71, v71\n"
        "v_cvt_pk_bf16_f32 v226, v68, v69\n"
        "v_cvt_pk_bf16_f32 v227, v70, v71\n"
        "v_mfma_f32_32x32x16_bf16 v[96:111], v[172:175], v[236:239], v[96:111]\n"
        "global_load_lds_dwordx4 v1, s[22:23] offset:3072 sc1\n"
        "s_add_u32 m0, m0, 4096\n"
        "s_add_u32 s22, s22, 4096\n"
        "s_addc_u32 s23, s23, 0\n"
        "v_exp_f32 v72, v72\n"
        "v_exp_f32 v73, v73\n"
        "v_exp_f32 v74, v74\n"
        "v_exp_f32 v75, v75\n"
        "v_cvt_pk_bf16_f32 v228, v72, v73\n"
        "v_cvt_pk_bf16_f32 v229, v74, v75\n"
        "v_mfma_f32_32x32x16_bf16 v[112:127], v[184:187], v[224:227], v[112:127]\n"
        "global_load_dwordx4 a[160:163], v1, s[62:63] offset:0 sc1\n"
        "v_exp_f32 v76, v76\n"
        "v_exp_f32 v77, v77\n"
        "v_exp_f32 v78, v78\n"
        "v_exp_f32 v79, v79\n"
        "v_cvt_pk_bf16_f32 v230, v76, v77\n"
        "v_cvt_pk_bf16_f32 v231, v78, v79\n"
        "v_mfma_f32_32x32x16_bf16 v[64:79], v[176:179], v[56:59], 0\n"
        "global_load_lds_dwordx4 v1, s[22:23] offset:0 sc1\n"
        "v_exp_f32 v80, v80\n"
        "v_exp_f32 v81, v81\n"
        "v_exp_f32 v82, v82\n"
        "v_exp_f32 v83, v83\n"
        "v_cvt_pk_bf16_f32 v232, v80, v81\n"
        "v_cvt_pk_bf16_f32 v233, v82, v83\n"
        "v_mfma_f32_32x32x16_bf16 v[64:79], v[180:183], v[60:63], v[64:79]\n"
        "global_load_dwordx4 a[164:167], v1, s[62:63] offset:1024 sc1\n"
        "v_exp_f32 v84, v84\n"
        "v_exp_f32 v85, v85\n"
        "v_exp_f32 v86, v86\n"
        "v_exp_f32 v87, v87\n"
        "v_cvt_pk_bf16_f32 v234, v84, v85\n"
        "v_cvt_pk_bf16_f32 v235, v86, v87\n"
        "v_mfma_f32_32x32x16_bf16 v[112:127], v[188:191], v[228:231], v[112:127]\n"
        "global_load_lds_dwordx4 v1, s[22:23] offset:1024 sc1\n"
        "v_exp_f32 v88, v88\n"
        "v_exp_f32 v89, v89\n"
        "v_exp_f32 v90, v90\n"
        "v_exp_f32 v91, v91\n"
        "v_cvt_pk_bf16_f32 v236, v88, v89\n"
        "v_cvt_pk_bf16_f32 v237, v90, v91\n"
        "v_mfma_f32_32x32x16_bf16 v[128:143], v[184:187], v[232:235], v[128:143]\n"
        "global_load_dwordx4 a[168:171], v1, s[62:63] offset:2048 sc1\n"
        "v_exp_f32 v92, v92\n"
        "v_exp_f32 v93, v93\n"
        "v_exp_f32 v94, v94\n"
        "v_exp_f32 v95, v95\n"
        "v_cvt_pk_bf16_f32 v238, v92, v93\n"
        "v_cvt_pk_bf16_f32 v239, v94, v95\n"
        "v_mfma_f32_32x32x16_bf16 v[80:95], v[176:179], v[32:35], 0\n"
        "global_load_dwordx4 a[172:175], v1, s[62:63] offset:3072 sc1\n"
        "s_add_u32 s62, s62, 4096\n"
        "s_addc_u32 s63, s63, 0\n"
        "v_exp_f32 v64, v64\n"
        "v_exp_f32 v65, v65\n"
        "v_exp_f32 v66, v66\n"
        "v_exp_f32 v67, v67\n"
        "v_cvt_pk_bf16_f32 v224, v64, v65\n"
        "v_cvt_pk_bf16_f32 v225, v66, v67\n"
        "v_mfma_f32_32x32x16_bf16 v[80:95], v[180:183], v[36:39], v[80:95]\n"
        "s_waitcnt vmcnt(30)\n"
        "v_exp_f32 v68, v68\n"
        "v_exp_f32 v69, v69\n"
        "v_exp_f32 v70, v70\n"
        "v_exp_f32 v71, v71\n"
        "v_cvt_pk_bf16_f32 v226, v68, v69\n"
        "v_cvt_pk_bf16_f32 v227, v70, v71\n"
        "v_mfma_f32_32x32x16_bf16 v[128:143], v[188:191], v[236:239], v[128:143]\n"
        "global_load_dwordx4 a[176:179], v1, s[62:63] offset:0 sc1\n"
        "v_exp_f32 v72, v72\n"
        "v_exp_f32 v73, v73\n"
        "v_exp_f32 v74, v74\n"
        "v_exp_f32 v75, v75\n"
        "v_cvt_pk_bf16_f32 v228, v72, v73\n"
        "v_cvt_pk_bf16_f32 v229, v74, v75\n"
        "v_mfma_f32_32x32x16_bf16 v[144:159], v[184:187], v[224:227], v[144:159]\n"
        "global_load_dwordx4 a[180:183], v1, s[62:63] offset:1024 sc1\n"
        "v_exp_f32 v76, v76\n"
        "v_exp_f32 v77, v77\n"
        "v_exp_f32 v78, v78\n"
        "v_exp_f32 v79, v79\n"
        "v_cvt_pk_bf16_f32 v230, v76, v77\n"
        "v_cvt_pk_bf16_f32 v231, v78, v79\n"
        "v_mfma_f32_32x32x16_bf16 v[64:79], v[192:195], v[40:43], 0\n"
        "v_exp_f32 v80, v80\n"
        "v_exp_f32 v81, v81\n"
        "v_exp_f32 v82, v82\n"
        "v_exp_f32 v83, v83\n"
        "v_cvt_pk_bf16_f32 v232, v80, v81\n"
        "v_cvt_pk_bf16_f32 v233, v82, v83\n"
        "v_mfma_f32_32x32x16_bf16 v[64:79], v[196:199], v[44:47], v[64:79]\n"
        "v_exp_f32 v84, v84\n"
        "v_exp_f32 v85, v85\n"
        "v_exp_f32 v86, v86\n"
        "v_exp_f32 v87, v87\n"
        "v_cvt_pk_bf16_f32 v234, v84, v85\n"
        "v_cvt_pk_bf16_f32 v235, v86, v87\n"
        "v_mfma_f32_32x32x16_bf16 v[144:159], v[188:191], v[228:231], v[144:159]\n"
        "v_exp_f32 v88, v88\n"
        "v_exp_f32 v89, v89\n"
        "v_exp_f32 v90, v90\n"
        "v_exp_f32 v91, v91\n"
        "v_cvt_pk_bf16_f32 v236, v88, v89\n"
        "v_cvt_pk_bf16_f32 v237, v90, v91\n"
        "v_mfma_f32_32x32x16_bf16 v[96:111], v[184:187], v[232:235], v[96:111]\n"
        "v_exp_f32 v92, v92\n"
        "v_exp_f32 v93, v93\n"
        "v_exp_f32 v94, v94\n"
        "v_exp_f32 v95, v95\n"
        "v_cvt_pk_bf16_f32 v238, v92, v93\n"
        "v_cvt_pk_bf16_f32 v239, v94, v95\n"
        "v_mfma_f32_32x32x16_bf16 v[80:95], v[192:195], v[48:51], 0\n"
        "global_load_dwordx4 a[184:187], v1, s[62:63] offset:2048 sc1\n"
        "v_exp_f32 v64, v64\n"
        "v_exp_f32 v65, v65\n"
        "v_exp_f32 v66, v66\n"
        "v_exp_f32 v67, v67\n"
        "v_cvt_pk_bf16_f32 v224, v64, v65\n"
        "v_cvt_pk_bf16_f32 v225, v66, v67\n"
        "v_mfma_f32_32x32x16_bf16 v[80:95], v[196:199], v[52:55], v[80:95]\n"
        "s_waitcnt vmcnt(31)\n"
        "v_exp_f32 v68, v68\n"
        "v_exp_f32 v69, v69\n"
        "v_exp_f32 v70, v70\n"
        "v_exp_f32 v71, v71\n"
        "v_cvt_pk_bf16_f32 v226, v68, v69\n"
        "v_cvt_pk_bf16_f32 v227, v70, v71\n"
        "v_mfma_f32_32x32x16_bf16 v[96:111], v[188:191], v[236:239], v[96:111]\n"
        "global_load_lds_dwordx4 v1, s[22:23] offset:2048 sc1\n"
        "v_exp_f32 v72, v72\n"
        "v_exp_f32 v73, v73\n"
        "v_exp_f32 v74, v74\n"
        "v_exp_f32 v75, v75\n"
        "v_cvt_pk_bf16_f32 v228, v72, v73\n"
        "v_cvt_pk_bf16_f32 v229, v74, v75\n"
        "v_mfma_f32_32x32x16_bf16 v[112:127], v[200:203], v[224:227], v[112:127]\n"
        "global_load_dwordx4 a[188:191], v1, s[62:63] offset:3072 sc1\n"
        "s_add_u32 s62, s62, 4096\n"
        "s_addc_u32 s63, s63, 0\n"
        "v_exp_f32 v76, v76\n"
        "v_exp_f32 v77, v77\n"
        "v_exp_f32 v78, v78\n"
        "v_exp_f32 v79, v79\n"
        "v_cvt_pk_bf16_f32 v230, v76, v77\n"
        "v_cvt_pk_bf16_f32 v231, v78, v79\n"
        "v_mfma_f32_32x32x16_bf16 v[64:79], v[192:195], v[56:59], 0\n"
        "global_load_lds_dwordx4 v1, s[22:23] offset:3072 sc1\n"
        "s_add_u32 m0, m0, 4096\n"
        "s_add_u32 s22, s22, 4096\n"
        "s_addc_u32 s23, s23, 0\n"
        "v_exp_f32 v80, v80\n"
        "v_exp_f32 v81, v81\n"
        "v_exp_f32 v82, v82\n"
        "v_exp_f32 v83, v83\n"
        "v_cvt_pk_bf16_f32 v232, v80, v81\n"
        "v_cvt_pk_bf16_f32 v233, v82, v83\n"
        "v_mfma_f32_32x32x16_bf16 v[64:79], v[196:199], v[60:63], v[64:79]\n"
        "global_load_dwordx4 a[192:195], v1, s[62:63] offset:0 sc1\n"
        "v_exp_f32 v84, v84\n"
        "v_exp_f32 v85, v85\n"
        "v_exp_f32 v86, v86\n"
        "v_exp_f32 v87, v87\n"
        "v_cvt_pk_bf16_f32 v234, v84, v85\n"
        "v_cvt_pk_bf16_f32 v235, v86, v87\n"
        "v_mfma_f32_32x32x16_bf16 v[112:127], v[204:207], v[228:231], v[112:127]\n"
        "global_load_lds_dwordx4 v1, s[22:23] offset:0 sc1\n"
        "v_exp_f32 v88, v88\n"
        "v_exp_f32 v89, v89\n"
        "v_exp_f32 v90, v90\n"
        "v_exp_f32 v91, v91\n"
        "v_cvt_pk_bf16_f32 v236, v88, v89\n"
        "v_cvt_pk_bf16_f32 v237, v90, v91\n"
        "v_mfma_f32_32x32x16_bf16 v[128:143], v[200:203], v[232:235], v[128:143]\n"
        "global_load_dwordx4 a[196:199], v1, s[62:63] offset:1024 sc1\n"
        "v_exp_f32 v92, v92\n"
        "v_exp_f32 v93, v93\n"
        "v_exp_f32 v94, v94\n"
        "v_exp_f32 v95, v95\n"
        "v_cvt_pk_bf16_f32 v238, v92, v93\n"
        "v_cvt_pk_bf16_f32 v239, v94, v95\n"
        "v_mfma_f32_32x32x16_bf16 v[80:95], v[192:195], v[32:35], 0\n"
        "global_load_dwordx4 a[200:203], v1, s[62:63] offset:2048 sc1\n"
        "v_exp_f32 v64, v64\n"
        "v_exp_f32 v65, v65\n"
        "v_exp_f32 v66, v66\n"
        "v_exp_f32 v67, v67\n"
        "v_cvt_pk_bf16_f32 v224, v64, v65\n"
        "v_cvt_pk_bf16_f32 v225, v66, v67\n"
        "v_mfma_f32_32x32x16_bf16 v[80:95], v[196:199], v[36:39], v[80:95]\n"
        "s_waitcnt vmcnt(30)\n"
        "v_exp_f32 v68, v68\n"
        "v_exp_f32 v69, v69\n"
        "v_exp_f32 v70, v70\n"
        "v_exp_f32 v71, v71\n"
        "v_cvt_pk_bf16_f32 v226, v68, v69\n"
        "v_cvt_pk_bf16_f32 v227, v70, v71\n"
        "v_mfma_f32_32x32x16_bf16 v[128:143], v[204:207], v[236:239], v[128:143]\n"
        "global_load_dwordx4 a[204:207], v1, s[62:63] offset:3072 sc1\n"
        "s_add_u32 s62, s62, 4096\n"
        "s_addc_u32 s63, s63, 0\n"
        "v_exp_f32 v72, v72\n"
        "v_exp_f32 v73, v73\n"
        "v_exp_f32 v74, v74\n"
        "v_exp_f32 v75, v75\n"
        "v_cvt_pk_bf16_f32 v228, v72, v73\n"
        "v_cvt_pk_bf16_f32 v229, v74, v75\n"
        "v_mfma_f32_32x32x16_bf16 v[144:159], v[200:203], v[224:227], v[144:159]\n"
        "global_load_dwordx4 a[208:211], v1, s[62:63] offset:0 sc1\n"
        "v_exp_f32 v76, v76\n"
        "v_exp_f32 v77, v77\n"
        "v_exp_f32 v78, v78\n"
        "v_exp_f32 v79, v79\n"
        "v_cvt_pk_bf16_f32 v230, v76, v77\n"
        "v_cvt_pk_bf16_f32 v231, v78, v79\n"
        "v_mfma_f32_32x32x16_bf16 v[64:79], v[208:211], v[40:43], 0\n"
        "v_exp_f32 v80, v80\n"
        "v_exp_f32 v81, v81\n"
        "v_exp_f32 v82, v82\n"
        "v_exp_f32 v83, v83\n"
        "v_cvt_pk_bf16_f32 v232, v80, v81\n"
        "v_cvt_pk_bf16_f32 v233, v82, v83\n"
        "v_mfma_f32_32x32x16_bf16 v[64:79], v[212:215], v[44:47], v[64:79]\n"
        "v_exp_f32 v84, v84\n"
        "v_exp_f32 v85, v85\n"
        "v_exp_f32 v86, v86\n"
        "v_exp_f32 v87, v87\n"
        "v_cvt_pk_bf16_f32 v234, v84, v85\n"
        "v_cvt_pk_bf16_f32 v235, v86, v87\n"
        "v_mfma_f32_32x32x16_bf16 v[144:159], v[204:207], v[228:231], v[144:159]\n"
        "v_exp_f32 v88, v88\n"
        "v_exp_f32 v89, v89\n"
        "v_exp_f32 v90, v90\n"
        "v_exp_f32 v91, v91\n"
        "v_cvt_pk_bf16_f32 v236, v88, v89\n"
        "v_cvt_pk_bf16_f32 v237, v90, v91\n"
        "v_mfma_f32_32x32x16_bf16 v[96:111], v[200:203], v[232:235], v[96:111]\n"
        "v_exp_f32 v92, v92\n"
        "v_exp_f32 v93, v93\n"
        "v_exp_f32 v94, v94\n"
        "v_exp_f32 v95, v95\n"
        "v_cvt_pk_bf16_f32 v238, v92, v93\n"
        "v_cvt_pk_bf16_f32 v239, v94, v95\n"
        "v_mfma_f32_32x32x16_bf16 v[80:95], v[208:211], v[48:51], 0\n"
        "global_load_dwordx4 a[212:215], v1, s[62:63] offset:1024 sc1\n"
        "v_exp_f32 v64, v64\n"
        "v_exp_f32 v65, v65\n"
        "v_exp_f32 v66, v66\n"
        "v_exp_f32 v67, v67\n"
        "v_cvt_pk_bf16_f32 v224, v64, v65\n"
        "v_cvt_pk_bf16_f32 v225, v66, v67\n"
        "v_mfma_f32_32x32x16_bf16 v[80:95], v[212:215], v[52:55], v[80:95]\n"
        "s_waitcnt vmcnt(30)\n"
        "v_exp_f32 v68, v68\n"
        "v_exp_f32 v69, v69\n"
        "v_exp_f32 v70, v70\n"
        "v_exp_f32 v71, v71\n"
        "v_cvt_pk_bf16_f32 v226, v68, v69\n"
        "v_cvt_pk_bf16_f32 v227, v70, v71\n"
        "v_mfma_f32_32x32x16_bf16 v[96:111], v[204:207], v[236:239], v[96:111]\n"
        "global_load_lds_dwordx4 v1, s[22:23] offset:1024 sc1\n"
        "v_exp_f32 v72, v72\n"
        "v_exp_f32 v73, v73\n"
        "v_exp_f32 v74, v74\n"
        "v_exp_f32 v75, v75\n"
        "v_cvt_pk_bf16_f32 v228, v72, v73\n"
        "v_cvt_pk_bf16_f32 v229, v74, v75\n"
        "v_mfma_f32_32x32x16_bf16 v[112:127], v[216:219], v[224:227], v[112:127]\n"
        "global_load_dwordx4 a[216:219], v1, s[62:63] offset:2048 sc1\n"
        "v_exp_f32 v76, v76\n"
        "v_exp_f32 v77, v77\n"
        "v_exp_f32 v78, v78\n"
        "v_exp_f32 v79, v79\n"
        "v_cvt_pk_bf16_f32 v230, v76, v77\n"
        "v_cvt_pk_bf16_f32 v231, v78, v79\n"
        "v_mfma_f32_32x32x16_bf16 v[64:79], v[208:211], v[56:59], 0\n"
        "global_load_dwordx4 a[220:223], v1, s[62:63] offset:3072 sc1\n"
        "s_add_u32 s62, s62, 4096\n"
        "s_addc_u32 s63, s63, 0\n"
        "v_exp_f32 v80, v80\n"
        "v_exp_f32 v81, v81\n"
        "v_exp_f32 v82, v82\n"
        "v_exp_f32 v83, v83\n"
        "v_cvt_pk_bf16_f32 v232, v80, v81\n"
        "v_cvt_pk_bf16_f32 v233, v82, v83\n"
        "v_mfma_f32_32x32x16_bf16 v[64:79], v[212:215], v[60:63], v[64:79]\n"
        "v_exp_f32 v84, v84\n"
        "v_exp_f32 v85, v85\n"
        "v_exp_f32 v86, v86\n"
        "v_exp_f32 v87, v87\n"
        "v_cvt_pk_bf16_f32 v234, v84, v85\n"
        "v_cvt_pk_bf16_f32 v235, v86, v87\n"
        "v_mfma_f32_32x32x16_bf16 v[112:127], v[220:223], v[228:231], v[112:127]\n"
        "v_exp_f32 v88, v88\n"
        "v_exp_f32 v89, v89\n"
        "v_exp_f32 v90, v90\n"
        "v_exp_f32 v91, v91\n"
        "v_cvt_pk_bf16_f32 v236, v88, v89\n"
        "v_cvt_pk_bf16_f32 v237, v90, v91\n"
        "v_mfma_f32_32x32x16_bf16 v[128:143], v[216:219], v[232:235], v[128:143]\n"
        "v_exp_f32 v92, v92\n"
        "v_exp_f32 v93, v93\n"
        "v_exp_f32 v94, v94\n"
        "v_exp_f32 v95, v95\n"
        "v_cvt_pk_bf16_f32 v238, v92, v93\n"
        "v_cvt_pk_bf16_f32 v239, v94, v95\n"
        "v_mfma_f32_32x32x16_bf16 v[80:95], v[208:211], v[32:35], 0\n"
        "ds_write_b128 v21, v[112:115] offset:38912\n"
        "v_exp_f32 v64, v64\n"
        "v_exp_f32 v65, v65\n"
        "v_exp_f32 v66, v66\n"
        "v_exp_f32 v67, v67\n"
        "v_cvt_pk_bf16_f32 v224, v64, v65\n"
        "v_cvt_pk_bf16_f32 v225, v66, v67\n"
        "v_mfma_f32_32x32x16_bf16 v[80:95], v[212:215], v[36:39], v[80:95]\n"
        "s_waitcnt vmcnt(63)\n"
        "v_exp_f32 v68, v68\n"
        "v_exp_f32 v69, v69\n"
        "v_exp_f32 v70, v70\n"
        "v_exp_f32 v71, v71\n"
        "v_cvt_pk_bf16_f32 v226, v68, v69\n"
        "v_cvt_pk_bf16_f32 v227, v70, v71\n"
        "v_mfma_f32_32x32x16_bf16 v[128:143], v[220:223], v[236:239], v[128:143]\n"
        "ds_write_b128 v21, v[116:119] offset:39936\n"
        "v_exp_f32 v72, v72\n"
        "v_exp_f32 v73, v73\n"
        "v_exp_f32 v74, v74\n"
        "v_exp_f32 v75, v75\n"
        "v_cvt_pk_bf16_f32 v228, v72, v73\n"
        "v_cvt_pk_bf16_f32 v229, v74, v75\n"
        "v_mfma_f32_32x32x16_bf16 v[144:159], v[216:219], v[224:227], v[144:159]\n"
        "ds_write_b128 v21, v[120:123] offset:40960\n"
        "v_exp_f32 v76, v76\n"
        "v_exp_f32 v77, v77\n"
        "v_exp_f32 v78, v78\n"
        "v_exp_f32 v79, v79\n"
        "v_cvt_pk_bf16_f32 v230, v76, v77\n"
        "v_cvt_pk_bf16_f32 v231, v78, v79\n"
        "ds_write_b128 v21, v[124:127] offset:41984\n"
        "v_exp_f32 v80, v80\n"
        "v_exp_f32 v81, v81\n"
        "v_exp_f32 v82, v82\n"
        "v_exp_f32 v83, v83\n"
        "v_cvt_pk_bf16_f32 v232, v80, v81\n"
        "v_cvt_pk_bf16_f32 v233, v82, v83\n"
        "ds_write_b128 v22, v[128:131] offset:43008\n"
        "v_exp_f32 v84, v84\n"
        "v_exp_f32 v85, v85\n"
        "v_exp_f32 v86, v86\n"
        "v_exp_f32 v87, v87\n"
        "v_cvt_pk_bf16_f32 v234, v84, v85\n"
        "v_cvt_pk_bf16_f32 v235, v86, v87\n"
        "v_mfma_f32_32x32x16_bf16 v[144:159], v[220:223], v[228:231], v[144:159]\n"
        "ds_write_b128 v22, v[132:135] offset:44032\n"
        "v_exp_f32 v88, v88\n"
        "v_exp_f32 v89, v89\n"
        "v_exp_f32 v90, v90\n"
        "v_exp_f32 v91, v91\n"
        "v_cvt_pk_bf16_f32 v236, v88, v89\n"
        "v_cvt_pk_bf16_f32 v237, v90, v91\n"
        "v_mfma_f32_32x32x16_bf16 v[96:111], v[216:219], v[232:235], v[96:111]\n"
        "ds_write_b128 v22, v[136:139] offset:45056\n"
        "v_exp_f32 v92, v92\n"
        "v_exp_f32 v93, v93\n"
        "v_exp_f32 v94, v94\n"
        "v_exp_f32 v95, v95\n"
        "v_cvt_pk_bf16_f32 v238, v92, v93\n"
        "v_cvt_pk_bf16_f32 v239, v94, v95\n"
        "ds_write_b128 v22, v[140:143] offset:46080\n"
        "s_branch .Latt2\n"
        ".Latt:\n"
        "s_mov_b32 s50, 0\n"
        ".Latt3:\n"
        "v_mfma_f32_32x32x16_bf16 v[80:95], v[160:163], v[48:51], 0\n"
        "s_add_u32 s51, s51, 0x4000\n"
        "s_and_b32 s51, s51, 0x3ffff\n"
        "v_exp_f32 v64, v64\n"
        "v_exp_f32 v65, v65\n"
        "v_exp_f32 v66, v66\n"
        "v_exp_f32 v67, v67\n"
        "v_cvt_pk_bf16_f32 v224, v64, v65\n"
        "v_cvt_pk_bf16_f32 v225, v66, v67\n"
        "v_mfma_f32_32x32x16_bf16 v[80:95], v[164:167], v[52:55], v[80:95]\n"
        "s_waitcnt vmcnt(12)\n"
        "v_exp_f32 v68, v68\n"
        "v_exp_f32 v69, v69\n"
        "v_exp_f32 v70, v70\n"
        "v_exp_f32 v71, v71\n"
        "v_cvt_pk_bf16_f32 v226, v68, v69\n"
        "v_cvt_pk_bf16_f32 v227, v70, v71\n"
        "v_mfma_f32_32x32x16_bf16 v[96:111], v[220:223], v[236:239], v[96:111]\n"
        "global_load_dwordx4 v[220:223], v1, s[20:21] offset:3072 sc1\n"
        "s_or_b32 s20, s51, s60\n"
        "v_exp_f32 v72, v72\n"
        "v_exp_f32 v73, v73\n"
        "v_exp_f32 v74, v74\n"
        "v_exp_f32 v75, v75\n"
        "v_cvt_pk_bf16_f32 v228, v72, v73\n"
        "v_cvt_pk_bf16_f32 v229, v74, v75\n"
        "v_mfma_f32_32x32x16_bf16 v[112:127], v[168:171], v[224:227], v[112:127]\n"
        "global_load_lds_dwordx4 v1, s[22:23] offset:0 sc1\n"
        "v_exp_f32 v76, v76\n"
        "v_exp_f32 v77, v77\n"
        "v_exp_f32 v78, v78\n"
        "v_exp_f32 v79, v79\n"
        "v_cvt_pk_bf16_f32 v230, v76, v77\n"
        "v_cvt_pk_bf16_f32 v231, v78, v79\n"
        "v_mfma_f32_32x32x16_bf16 v[64:79], v[160:163], v[56:59], 0\n"
        "global_load_lds_dwordx4 v1, s[22:23] offset:1024 sc1\n"
        "v_exp_f32 v80, v80\n"
        "v_exp_f32 v81, v81\n"
        "v_exp_f32 v82, v82\n"
        "v_exp_f32 v83, v83\n"
        "v_cvt_pk_bf16_f32 v232, v80, v81\n"
        "v_cvt_pk_bf16_f32 v233, v82, v83\n"
        "v_mfma_f32_32x32x16_bf16 v[64:79], v[164:167], v[60:63], v[64:79]\n"
        "v_exp_f32 v84, v84\n"
        "v_exp_f32 v85, v85\n"
        "v_exp_f32 v86, v86\n"
        "v_exp_f32 v87, v87\n"
        "v_cvt_pk_bf16_f32 v234, v84, v85\n"
        "v_cvt_pk_bf16_f32 v235, v86, v87\n"
        "v_mfma_f32_32x32x16_bf16 v[112:127], v[172:175], v[228:231], v[112:127]\n"
        "v_exp_f32 v88, v88\n"
        "v_exp_f32 v89, v89\n"
        "v_exp_f32 v90, v90\n"
        "v_exp_f32 v91, v91\n"
        "v_cvt_pk_bf16_f32 v236, v88, v89\n"
        "v_cvt_pk_bf16_f32 v237, v90, v91\n"
        "v_mfma_f32_32x32x16_bf16 v[128:143], v[168:171], v[232:235], v[128:143]\n"
        "v_exp_f32 v92, v92\n"
        "v_exp_f32 v93, v93\n"
        "v_exp_f32 v94, v94\n"
        "v_exp_f32 v95, v95\n"
        "v_cvt_pk_bf16_f32 v238, v92, v93\n"
        "v_cvt_pk_bf16_f32 v239, v94, v95\n"
        "v_mfma_f32_32x32x16_bf16 v[80:95], v[160:163], v[32:35], 0\n"
        "v_exp_f32 v64, v64\n"
        "v_exp_f32 v65, v65\n"
        "v_exp_f32 v66, v66\n"
        "v_exp_f32 v67, v67\n"
        "v_cvt_pk_bf16_f32 v224, v64, v65\n"
        "v_cvt_pk_bf16_f32 v225, v66, v67\n"
        "v_mfma_f32_32x32x16_bf16 v[80:95], v[164:167], v[36:39], v[80:95]\n"
        "s_waitcnt vmcnt(13)\n"
        "v_exp_f32 v68, v68\n"
        "v_exp_f32 v69, v69\n"
        "v_exp_f32 v70, v70\n"
        "v_exp_f32 v71, v71\n"
        "v_cvt_pk_bf16_f32 v226, v68, v69\n"
        "v_cvt_pk_bf16_f32 v227, v70, v71\n"
        "v_mfma_f32_32x32x16_bf16 v[128:143], v[172:175], v[236:239], v[128:143]\n"
        "global_load_dwordx4 v[160:163], v1, s[20:21] offset:0 sc1\n"
        "v_exp_f32 v72, v72\n"
        "v_exp_f32 v73, v73\n"
        "v_exp_f32 v74, v74\n"
        "v_exp_f32 v75, v75\n"
        "v_cvt_pk_bf16_f32 v228, v72, v73\n"
        "v_cvt_pk_bf16_f32 v229, v74, v75\n"
        "v_mfma_f32_32x32x16_bf16 v[144:159], v[168:171], v[224:227], v[144:159]\n"
        "global_load_dwordx4 v[164:167], v1, s[20:21] offset:1024 sc1\n"
        "v_exp_f32 v76, v76\n"
        "v_exp_f32 v77, v77\n"
        "v_exp_f32 v78, v78\n"
        "v_exp_f32 v79, v79\n"
        "v_cvt_pk_bf16_f32 v230, v76, v77\n"
        "v_cvt_pk_bf16_f32 v231, v78, v79\n"
        "v_mfma_f32_32x32x16_bf16 v[64:79], v[176:179], v[40:43], 0\n"
        "v_exp_f32 v80, v80\n"
        "v_exp_f32 v81, v81\n"
        "v_exp_f32 v82, v82\n"
        "v_exp_f32 v83, v83\n"
        "v_cvt_pk_bf16_f32 v232, v80, v81\n"
        "v_cvt_pk_bf16_f32 v233, v82, v83\n"
        "v_mfma_f32_32x32x16_bf16 v[64:79], v[180:183], v[44:47], v[64:79]\n"
        "v_exp_f32 v84, v84\n"
        "v_exp_f32 v85, v85\n"
        "v_exp_f32 v86, v86\n"
        "v_exp_f32 v87, v87\n"
        "v_cvt_pk_bf16_f32 v234, v84, v85\n"
        "v_cvt_pk_bf16_f32 v235, v86, v87\n"
        "v_mfma_f32_32x32x16_bf16 v[144:159], v[172:175], v[228:231], v[144:159]\n"
        "v_exp_f32 v88, v88\n"
        "v_exp_f32 v89, v89\n"
        "v_exp_f32 v90, v90\n"
        "v_exp_f32 v91, v91\n"
        "v_cvt_pk_bf16_f32 v236, v88, v89\n"
        "v_cvt_pk_bf16_f32 v237, v90, v91\n"
        "v_mfma_f32_32x32x16_bf16 v[96:111], v[168:171], v[232:235], v[96:111]\n"
        "global_load_dwordx4 v[168:171], v1, s[20:21] offset:2048 sc1\n"
        "v_exp_f32 v92, v92\n"
        "v_exp_f32 v93, v93\n"
        "v_exp_f32 v94, v94\n"
        "v_exp_f32 v95, v95\n"
        "v_cvt_pk_bf16_f32 v238, v92, v93\n"
        "v_cvt_pk_bf16_f32 v239, v94, v95\n"
        "v_mfma_f32_32x32x16_bf16 v[80:95], v[176:179], v[48:51], 0\n"
        "s_add_u32 s51, s51, 0x4000\n"
        "s_and_b32 s51, s51, 0x3ffff\n"
        "v_exp_f32 v64, v64\n"
        "v_exp_f32 v65, v65\n"
        "v_exp_f32 v66, v66\n"
        "v_exp_f32 v67, v67\n"
        "v_cvt_pk_bf16_f32 v224, v64, v65\n"
        "v_cvt_pk_bf16_f32 v225, v66, v67\n"
        "v_mfma_f32_32x32x16_bf16 v[80:95], v[180:183], v[52:55], v[80:95]\n"
        "s_waitcnt vmcnt(14)\n"
        "v_exp_f32 v68, v68\n"
        "v_exp_f32 v69, v69\n"
        "v_exp_f32 v70, v70\n"
        "v_exp_f32 v71, v71\n"
        "v_cvt_pk_bf16_f32 v226, v68, v69\n"
        "v_cvt_pk_bf16_f32 v227, v70, v71\n"
        "v_mfma_f32_32x32x16_bf16 v[96:111], v[172:175], v[236:239], v[96:111]\n"
        "global_load_dwordx4 v[172:175], v1, s[20:21] offset:3072 sc1\n"
        "s_or_b32 s20, s51, s60\n"
        "v_exp_f32 v72, v72\n"
        "v_exp_f32 v73, v73\n"
        "v_exp_f32 v74, v74\n"
        "v_exp_f32 v75, v75\n"
        "v_cvt_pk_bf16_f32 v228, v72, v73\n"
        "v_cvt_pk_bf16_f32 v229, v74, v75\n"
        "v_mfma_f32_32x32x16_bf16 v[112:127], v[184:187], v[224:227], v[112:127]\n"
        "global_load_lds_dwordx4 v1, s[22:23] offset:2048 sc1\n"
        "v_exp_f32 v76, v76\n"
        "v_exp_f32 v77, v77\n"
        "v_exp_f32 v78, v78\n"
        "v_exp_f32 v79, v79\n"
        "v_cvt_pk_bf16_f32 v230, v76, v77\n"
        "v_cvt_pk_bf16_f32 v231, v78, v79\n"
        "v_mfma_f32_32x32x16_bf16 v[64:79], v[176:179], v[56:59], 0\n"
        "global_load_lds_dwordx4 v1, s[22:23] offset:3072 sc1\n"
        "s_add_u32 m0, m0, 4096\n"
        "s_add_u32 s22, s22, 4096\n"
        "s_addc_u32 s23, s23, 0\n"
        "v_exp_f32 v80, v80\n"
        "v_exp_f32 v81, v81\n"
        "v_exp_f32 v82, v82\n"
        "v_exp_f32 v83, v83\n"
        "v_cvt_pk_bf16_f32 v232, v80, v81\n"
        "v_cvt_pk_bf16_f32 v233, v82, v83\n"
        "v_mfma_f32_32x32x16_bf16 v[64:79], v[180:183], v[60:63], v[64:79]\n"
        "v_exp_f32 v84, v84\n"
        "v_exp_f32 v85, v85\n"
        "v_exp_f32 v86, v86\n"
        "v_exp_f32 v87, v87\n"
        "v_cvt_pk_bf16_f32 v234, v84, v85\n"
        "v_cvt_pk_bf16_f32 v235, v86, v87\n"
        "v_mfma_f32_32x32x16_bf16 v[112:127], v[188:191], v[228:231], v[112:127]\n"
        "v_exp_f32 v88, v88\n"
        "v_exp_f32 v89, v89\n"
        "v_exp_f32 v90, v90\n"
        "v_exp_f32 v91, v91\n"
        "v_cvt_pk_bf16_f32 v236, v88, v89\n"
        "v_cvt_pk_bf16_f32 v237, v90, v91\n"
        "v_mfma_f32_32x32x16_bf16 v[128:143], v[184:187], v[232:235], v[128:143]\n"
        "v_exp_f32 v92, v92\n"
        "v_exp_f32 v93, v93\n"
        "v_exp_f32 v94, v94\n"
        "v_exp_f32 v95, v95\n"
        "v_cvt_pk_bf16_f32 v238, v92, v93\n"
        "v_cvt_pk_bf16_f32 v239, v94, v95\n"
        "v_mfma_f32_32x32x16_bf16 v[80:95], v[176:179], v[32:35], 0\n"
        "v_exp_f32 v64, v64\n"
        "v_exp_f32 v65, v65\n"
        "v_exp_f32 v66, v66\n"
        "v_exp_f32 v67, v67\n"
        "v_cvt_pk_bf16_f32 v224, v64, v65\n"
        "v_cvt_pk_bf16_f32 v225, v66, v67\n"
        "v_mfma_f32_32x32x16_bf16 v[80:95], v[180:183], v[36:39], v[80:95]\n"
        "s_waitcnt vmcnt(14)\n"
        "v_exp_f32 v68, v68\n"
        "v_exp_f32 v69, v69\n"
        "v_exp_f32 v70, v70\n"
        "v_exp_f32 v71, v71\n"
        "v_cvt_pk_bf16_f32 v226, v68, v69\n"
        "v_cvt_pk_bf16_f32 v227, v70, v71\n"
        "v_mfma_f32_32x32x16_bf16 v[128:143], v[188:191], v[236:239], v[128:143]\n"
        "global_load_dwordx4 v[176:179], v1, s[20:21] offset:0 sc1\n"
        "v_exp_f32 v72, v72\n"
        "v_exp_f32 v73, v73\n"
        "v_exp_f32 v74, v74\n"
        "v_exp_f32 v75, v75\n"
        "v_cvt_pk_bf16_f32 v228, v72, v73\n"
        "v_cvt_pk_bf16_f32 v229, v74, v75\n"
        "v_mfma_f32_32x32x16_bf16 v[144:159], v[184:187], v[224:227], v[144:159]\n"
        "global_load_dwordx4 v[180:183], v1, s[20:21] offset:1024 sc1\n"
        "v_exp_f32 v76, v76\n"
        "v_exp_f32 v77, v77\n"
        "v_exp_f32 v78, v78\n"
        "v_exp_f32 v79, v79\n"
        "v_cvt_pk_bf16_f32 v230, v76, v77\n"
        "v_cvt_pk_bf16_f32 v231, v78, v79\n"
        "v_mfma_f32_32x32x16_bf16 v[64:79], v[192:195], v[40:43], 0\n"
        "v_exp_f32 v80, v80\n"
        "v_exp_f32 v81, v81\n"
        "v_exp_f32 v82, v82\n"
        "v_exp_f32 v83, v83\n"
        "v_cvt_pk_bf16_f32 v232, v80, v81\n"
        "v_cvt_pk_bf16_f32 v233, v82, v83\n"
        "v_mfma_f32_32x32x16_bf16 v[64:79], v[196:199], v[44:47], v[64:79]\n"
        "v_exp_f32 v84, v84\n"
        "v_exp_f32 v85, v85\n"
        "v_exp_f32 v86, v86\n"
        "v_exp_f32 v87, v87\n"
        "v_cvt_pk_bf16_f32 v234, v84, v85\n"
        "v_cvt_pk_bf16_f32 v235, v86, v87\n"
        "v_mfma_f32_32x32x16_bf16 v[144:159], v[188:191], v[228:231], v[144:159]\n"
        "v_exp_f32 v88, v88\n"
        "v_exp_f32 v89, v89\n"
        "v_exp_f32 v90, v90\n"
        "v_exp_f32 v91, v91\n"
        "v_cvt_pk_bf16_f32 v236, v88, v89\n"
        "v_cvt_pk_bf16_f32 v237, v90, v91\n"
        "v_mfma_f32_32x32x16_bf16 v[96:111], v[184:187], v[232:235], v[96:111]\n"
        "global_load_dwordx4 v[184:187], v1, s[20:21] offset:2048 sc1\n"
        "v_exp_f32 v92, v92\n"
        "v_exp_f32 v93, v93\n"
        "v_exp_f32 v94, v94\n"
        "v_exp_f32 v95, v95\n"
        "v_cvt_pk_bf16_f32 v238, v92, v93\n"
        "v_cvt_pk_bf16_f32 v239, v94, v95\n"
        "v_mfma_f32_32x32x16_bf16 v[80:95], v[192:195], v[48:51], 0\n"
        "s_add_u32 s51, s51, 0x4000\n"
        "s_and_b32 s51, s51, 0x3ffff\n"
        "v_exp_f32 v64, v64\n"
        "v_exp_f32 v65, v65\n"
        "v_exp_f32 v66, v66\n"
        "v_exp_f32 v67, v67\n"
        "v_cvt_pk_bf16_f32 v224, v64, v65\n"
        "v_cvt_pk_bf16_f32 v225, v66, v67\n"
        "v_mfma_f32_32x32x16_bf16 v[80:95], v[196:199], v[52:55], v[80:95]\n"
        "s_waitcnt vmcnt(15)\n"
        "v_exp_f32 v68, v68\n"
        "v_exp_f32 v69, v69\n"
        "v_exp_f32 v70, v70\n"
        "v_exp_f32 v71, v71\n"
        "v_cvt_pk_bf16_f32 v226, v68, v69\n"
        "v_cvt_pk_bf16_f32 v227, v70, v71\n"
        "v_mfma_f32_32x32x16_bf16 v[96:111], v[188:191], v[236:239], v[96:111]\n"
        "global_load_dwordx4 v[188:191], v1, s[20:21] offset:3072 sc1\n"
        "s_or_b32 s20, s51, s60\n"
        "v_exp_f32 v72, v72\n"
        "v_exp_f32 v73, v73\n"
        "v_exp_f32 v74, v74\n"
        "v_exp_f32 v75, v75\n"
        "v_cvt_pk_bf16_f32 v228, v72, v73\n"
        "v_cvt_pk_bf16_f32 v229, v74, v75\n"
        "v_mfma_f32_32x32x16_bf16 v[112:127], v[200:203], v[224:227], v[112:127]\n"
        "global_load_lds_dwordx4 v1, s[22:23] offset:0 sc1\n"
        "v_exp_f32 v76, v76\n"
        "v_exp_f32 v77, v77\n"
        "v_exp_f32 v78, v78\n"
        "v_exp_f32 v79, v79\n"
        "v_cvt_pk_bf16_f32 v230, v76, v77\n"
        "v_cvt_pk_bf16_f32 v231, v78, v79\n"
        "v_mfma_f32_32x32x16_bf16 v[64:79], v[192:195], v[56:59], 0\n"
        "global_load_lds_dwordx4 v1, s[22:23] offset:1024 sc1\n"
        "v_exp_f32 v80, v80\n"
        "v_exp_f32 v81, v81\n"
        "v_exp_f32 v82, v82\n"
        "v_exp_f32 v83, v83\n"
        "v_cvt_pk_bf16_f32 v232, v80, v81\n"
        "v_cvt_pk_bf16_f32 v233, v82, v83\n"
        "v_mfma_f32_32x32x16_bf16 v[64:79], v[196:199], v[60:63], v[64:79]\n"
        "v_exp_f32 v84, v84\n"
        "v_exp_f32 v85, v85\n"
        "v_exp_f32 v86, v86\n"
        "v_exp_f32 v87, v87\n"
        "v_cvt_pk_bf16_f32 v234, v84, v85\n"
        "v_cvt_pk_bf16_f32 v235, v86, v87\n"
        "v_mfma_f32_32x32x16_bf16 v[112:127], v[204:207], v[228:231], v[112:127]\n"
        "v_exp_f32 v88, v88\n"
        "v_exp_f32 v89, v89\n"
        "v_exp_f32 v90, v90\n"
        "v_exp_f32 v91, v91\n"
        "v_cvt_pk_bf16_f32 v236, v88, v89\n"
        "v_cvt_pk_bf16_f32 v237, v90, v91\n"
        "v_mfma_f32_32x32x16_bf16 v[128:143], v[200:203], v[232:235], v[128:143]\n"
        "v_exp_f32 v92, v92\n"
        "v_exp_f32 v93, v93\n"
        "v_exp_f32 v94, v94\n"
        "v_exp_f32 v95, v95\n"
        "v_cvt_pk_bf16_f32 v238, v92, v93\n"
        "v_cvt_pk_bf16_f32 v239, v94, v95\n"
        "v_mfma_f32_32x32x16_bf16 v[80:95], v[192:195], v[32:35], 0\n"
        "v_exp_f32 v64, v64\n"
        "v_exp_f32 v65, v65\n"
        "v_exp_f32 v66, v66\n"
        "v_exp_f32 v67, v67\n"
        "v_cvt_pk_bf16_f32 v224, v64, v65\n"
        "v_cvt_pk_bf16_f32 v225, v66, v67\n"
        "v_mfma_f32_32x32x16_bf16 v[80:95], v[196:199], v[36:39], v[80:95]\n"
        "s_waitcnt vmcnt(16)\n"
        "v_exp_f32 v68, v68\n"
        "v_exp_f32 v69, v69\n"
        "v_exp_f32 v70, v70\n"
        "v_exp_f32 v71, v71\n"
        "v_cvt_pk_bf16_f32 v226, v68, v69\n"
        "v_cvt_pk_bf16_f32 v227, v70, v71\n"
        "v_mfma_f32_32x32x16_bf16 v[128:143], v[204:207], v[236:239], v[128:143]\n"
        "global_load_dwordx4 v[192:195], v1, s[20:21] offset:0 sc1\n"
        "v_exp_f32 v72, v72\n"
        "v_exp_f32 v73, v73\n"
        "v_exp_f32 v74, v74\n"
        "v_exp_f32 v75, v75\n"
        "v_cvt_pk_bf16_f32 v228, v72, v73\n"
        "v_cvt_pk_bf16_f32 v229, v74, v75\n"
        "v_mfma_f32_32x32x16_bf16 v[144:159], v[200:203], v[224:227], v[144:159]\n"
        "global_load_dwordx4 v[196:199], v1, s[20:21] offset:1024 sc1\n"
        "v_exp_f32 v76, v76\n"
        "v_exp_f32 v77, v77\n"
        "v_exp_f32 v78, v78\n"
        "v_exp_f32 v79, v79\n"
        "v_cvt_pk_bf16_f32 v230, v76, v77\n"
        "v_cvt_pk_bf16_f32 v231, v78, v79\n"
        "v_mfma_f32_32x32x16_bf16 v[64:79], v[208:211], v[40:43], 0\n"
        "v_exp_f32 v80, v80\n"
        "v_exp_f32 v81, v81\n"
        "v_exp_f32 v82, v82\n"
        "v_exp_f32 v83, v83\n"
        "v_cvt_pk_bf16_f32 v232, v80, v81\n"
        "v_cvt_pk_bf16_f32 v233, v82, v83\n"
        "v_mfma_f32_32x32x16_bf16 v[64:79], v[212:215], v[44:47], v[64:79]\n"
        "v_exp_f32 v84, v84\n"
        "v_exp_f32 v85, v85\n"
        "v_exp_f32 v86, v86\n"
        "v_exp_f32 v87, v87\n"
        "v_cvt_pk_bf16_f32 v234, v84, v85\n"
        "v_cvt_pk_bf16_f32 v235, v86, v87\n"
        "v_mfma_f32_32x32x16_bf16 v[144:159], v[204:207], v[228:231], v[144:159]\n"
        "v_exp_f32 v88, v88\n"
        "v_exp_f32 v89, v89\n"
        "v_exp_f32 v90, v90\n"
        "v_exp_f32 v91, v91\n"
        "v_cvt_pk_bf16_f32 v236, v88, v89\n"
        "v_cvt_pk_bf16_f32 v237, v90, v91\n"
        "v_mfma_f32_32x32x16_bf16 v[96:111], v[200:203], v[232:235], v[96:111]\n"
        "global_load_dwordx4 v[200:203], v1, s[20:21] offset:2048 sc1\n"
        "v_exp_f32 v92, v92\n"
        "v_exp_f32 v93, v93\n"
        "v_exp_f32 v94, v94\n"
        "v_exp_f32 v95, v95\n"
        "v_cvt_pk_bf16_f32 v238, v92, v93\n"
        "v_cvt_pk_bf16_f32 v239, v94, v95\n"
        "v_mfma_f32_32x32x16_bf16 v[80:95], v[208:211], v[48:51], 0\n"
        "s_add_u32 s51, s51, 0x4000\n"
        "s_and_b32 s51, s51, 0x3ffff\n"
        "v_exp_f32 v64, v64\n"
        "v_exp_f32 v65, v65\n"
        "v_exp_f32 v66, v66\n"
        "v_exp_f32 v67, v67\n"
        "v_cvt_pk_bf16_f32 v224, v64, v65\n"
        "v_cvt_pk_bf16_f32 v225, v66, v67\n"
        "v_mfma_f32_32x32x16_bf16 v[80:95], v[212:215], v[52:55], v[80:95]\n"
        "s_waitcnt vmcnt(17)\n"
        "v_exp_f32 v68, v68\n"
        "v_exp_f32 v69, v69\n"
        "v_exp_f32 v70, v70\n"
        "v_exp_f32 v71, v71\n"
        "v_cvt_pk_bf16_f32 v226, v68, v69\n"
        "v_cvt_pk_bf16_f32 v227, v70, v71\n"
        "v_mfma_f32_32x32x16_bf16 v[96:111], v[204:207], v[236:239], v[96:111]\n"
        "global_load_dwordx4 v[204:207], v1, s[20:21] offset:3072 sc1\n"
        "s_or_b32 s20, s51, s60\n"
        "v_exp_f32 v72, v72\n"
        "v_exp_f32 v73, v73\n"
        "v_exp_f32 v74, v74\n"
        "v_exp_f32 v75, v75\n"
        "v_cvt_pk_bf16_f32 v228, v72, v73\n"
        "v_cvt_pk_bf16_f32 v229, v74, v75\n"
        "v_mfma_f32_32x32x16_bf16 v[112:127], v[216:219], v[224:227], v[112:127]\n"
        "global_load_lds_dwordx4 v1, s[22:23] offset:2048 sc1\n"
        "v_exp_f32 v76, v76\n"
        "v_exp_f32 v77, v77\n"
        "v_exp_f32 v78, v78\n"
        "v_exp_f32 v79, v79\n"
        "v_cvt_pk_bf16_f32 v230, v76, v77\n"
        "v_cvt_pk_bf16_f32 v231, v78, v79\n"
        "v_mfma_f32_32x32x16_bf16 v[64:79], v[208:211], v[56:59], 0\n"
        "global_load_lds_dwordx4 v1, s[22:23] offset:3072 sc1\n"
        "s_add_u32 m0, m0, 4096\n"
        "s_add_u32 s22, s22, 4096\n"
        "s_addc_u32 s23, s23, 0\n"
        "v_exp_f32 v80, v80\n"
        "v_exp_f32 v81, v81\n"
        "v_exp_f32 v82, v82\n"
        "v_exp_f32 v83, v83\n"
        "v_cvt_pk_bf16_f32 v232, v80, v81\n"
        "v_cvt_pk_bf16_f32 v233, v82, v83\n"
        "v_mfma_f32_32x32x16_bf16 v[64:79], v[212:215], v[60:63], v[64:79]\n"
        "v_exp_f32 v84, v84\n"
        "v_exp_f32 v85, v85\n"
        "v_exp_f32 v86, v86\n"
        "v_exp_f32 v87, v87\n"
        "v_cvt_pk_bf16_f32 v234, v84, v85\n"
        "v_cvt_pk_bf16_f32 v235, v86, v87\n"
        "v_mfma_f32_32x32x16_bf16 v[112:127], v[220:223], v[228:231], v[112:127]\n"
        "v_exp_f32 v88, v88\n"
        "v_exp_f32 v89, v89\n"
        "v_exp_f32 v90, v90\n"
        "v_exp_f32 v91, v91\n"
        "v_cvt_pk_bf16_f32 v236, v88, v89\n"
        "v_cvt_pk_bf16_f32 v237, v90, v91\n"
        "v_mfma_f32_32x32x16_bf16 v[128:143], v[216:219], v[232:235], v[128:143]\n"
        "v_exp_f32 v92, v92\n"
        "v_exp_f32 v93, v93\n"
        "v_exp_f32 v94, v94\n"
        "v_exp_f32 v95, v95\n"
        "v_cvt_pk_bf16_f32 v238, v92, v93\n"
        "v_cvt_pk_bf16_f32 v239, v94, v95\n"
        "v_mfma_f32_32x32x16_bf16 v[80:95], v[208:211], v[32:35], 0\n"
        "v_exp_f32 v64, v64\n"
        "v_exp_f32 v65, v65\n"
        "v_exp_f32 v66, v66\n"
        "v_exp_f32 v67, v67\n"
        "v_cvt_pk_bf16_f32 v224, v64, v65\n"
        "v_cvt_pk_bf16_f32 v225, v66, v67\n"
        "v_mfma_f32_32x32x16_bf16 v[80:95], v[212:215], v[36:39], v[80:95]\n"
        "s_waitcnt vmcnt(16)\n"
        "v_exp_f32 v68, v68\n"
        "v_exp_f32 v69, v69\n"
        "v_exp_f32 v70, v70\n"
        "v_exp_f32 v71, v71\n"
        "v_cvt_pk_bf16_f32 v226, v68, v69\n"
        "v_cvt_pk_bf16_f32 v227, v70, v71\n"
        "v_mfma_f32_32x32x16_bf16 v[128:143], v[220:223], v[236:239], v[128:143]\n"
        "global_load_dwordx4 v[208:211], v1, s[20:21] offset:0 sc1\n"
        "v_exp_f32 v72, v72\n"
        "v_exp_f32 v73, v73\n"
        "v_exp_f32 v74, v74\n"
        "v_exp_f32 v75, v75\n"
        "v_cvt_pk_bf16_f32 v228, v72, v73\n"
        "v_cvt_pk_bf16_f32 v229, v74, v75\n"
        "v_mfma_f32_32x32x16_bf16 v[144:159], v[216:219], v[224:227], v[144:159]\n"
        "global_load_dwordx4 v[212:215], v1, s[20:21] offset:1024 sc1\n"
        "v_exp_f32 v76, v76\n"
        "v_exp_f32 v77, v77\n"
        "v_exp_f32 v78, v78\n"
        "v_exp_f32 v79, v79\n"
        "v_cvt_pk_bf16_f32 v230, v76, v77\n"
        "v_cvt_pk_bf16_f32 v231, v78, v79\n"
        "v_mfma_f32_32x32x16_bf16 v[64:79], v[160:163], v[40:43], 0\n"
        "v_exp_f32 v80, v80\n"
        "v_exp_f32 v81, v81\n"
        "v_exp_f32 v82, v82\n"
        "v_exp_f32 v83, v83\n"
        "v_cvt_pk_bf16_f32 v232, v80, v81\n"
        "v_cvt_pk_bf16_f32 v233, v82, v83\n"
        "v_mfma_f32_32x32x16_bf16 v[64:79], v[164:167], v[44:47], v[64:79]\n"
        "v_exp_f32 v84, v84\n"
        "v_exp_f32 v85, v85\n"
        "v_exp_f32 v86, v86\n"
        "v_exp_f32 v87, v87\n"
        "v_cvt_pk_bf16_f32 v234, v84, v85\n"
        "v_cvt_pk_bf16_f32 v235, v86, v87\n"
        "v_mfma_f32_32x32x16_bf16 v[144:159], v[220:223], v[228:231], v[144:159]\n"
        "v_exp_f32 v88, v88\n"
        "v_exp_f32 v89, v89\n"
        "v_exp_f32 v90, v90\n"
        "v_exp_f32 v91, v91\n"
        "v_cvt_pk_bf16_f32 v236, v88, v89\n"
        "v_cvt_pk_bf16_f32 v237, v90, v91\n"
        "v_mfma_f32_32x32x16_bf16 v[96:111], v[216:219], v[232:235], v[96:111]\n"
        "global_load_dwordx4 v[216:219], v1, s[20:21] offset:2048 sc1\n"
        "v_exp_f32 v92, v92\n"
        "v_exp_f32 v93, v93\n"
        "v_exp_f32 v94, v94\n"
        "v_exp_f32 v95, v95\n"
        "v_cvt_pk_bf16_f32 v238, v92, v93\n"
        "v_cvt_pk_bf16_f32 v239, v94, v95\n"
        "s_add_u32 s50, s50, 1\n"
        "s_cmp_lg_u32 s50, 2\n"
        "s_cbranch_scc1 .Latt3\n"
        "v_mfma_f32_32x32x16_bf16 v[80:95], v[160:163], v[48:51], 0\n"
        "global_load_lds_dwordx4 v1, s[22:23] offset:0 sc1\n"
        "v_exp_f32 v64, v64\n"
        "v_exp_f32 v65, v65\n"
        "v_exp_f32 v66, v66\n"
        "v_exp_f32 v67, v67\n"
        "v_cvt_pk_bf16_f32 v224, v64, v65\n"
        "v_cvt_pk_bf16_f32 v225, v66, v67\n"
        "v_mfma_f32_32x32x16_bf16 v[80:95], v[164:167], v[52:55], v[80:95]\n"
        "s_waitcnt vmcnt(18)\n"
        "v_exp_f32 v68, v68\n"
        "v_exp_f32 v69, v69\n"
        "v_exp_f32 v70, v70\n"
        "v_exp_f32 v71, v71\n"
        "v_cvt_pk_bf16_f32 v226, v68, v69\n"
        "v_cvt_pk_bf16_f32 v227, v70, v71\n"
        "v_mfma_f32_32x32x16_bf16 v[96:111], v[220:223], v[236:239], v[96:111]\n"
        "global_load_dwordx4 v[220:223], v1, s[20:21] offset:3072 sc1\n"
        "v_exp_f32 v72, v72\n"
        "v_exp_f32 v73, v73\n"
        "v_exp_f32 v74, v74\n"
        "v_exp_f32 v75, v75\n"
        "v_cvt_pk_bf16_f32 v228, v72, v73\n"
        "v_cvt_pk_bf16_f32 v229, v74, v75\n"
        "v_mfma_f32_32x32x16_bf16 v[112:127], v[168:171], v[224:227], v[112:127]\n"
        "global_load_lds_dwordx4 v1, s[22:23] offset:1024 sc1\n"
        "v_exp_f32 v76, v76\n"
        "v_exp_f32 v77, v77\n"
        "v_exp_f32 v78, v78\n"
        "v_exp_f32 v79, v79\n"
        "v_cvt_pk_bf16_f32 v230, v76, v77\n"
        "v_cvt_pk_bf16_f32 v231, v78, v79\n"
        "v_mfma_f32_32x32x16_bf16 v[64:79], v[160:163], v[56:59], 0\n"
        "global_load_lds_dwordx4 v1, s[22:23] offset:2048 sc1\n"
        "v_exp_f32 v80, v80\n"
        "v_exp_f32 v81, v81\n"
        "v_exp_f32 v82, v82\n"
        "v_exp_f32 v83, v83\n"
        "v_cvt_pk_bf16_f32 v232, v80, v81\n"
        "v_cvt_pk_bf16_f32 v233, v82, v83\n"
        "v_mfma_f32_32x32x16_bf16 v[64:79], v[164:167], v[60:63], v[64:79]\n"
        "v_exp_f32 v84, v84\n"
        "v_exp_f32 v85, v85\n"
        "v_exp_f32 v86, v86\n"
        "v_exp_f32 v87, v87\n"
        "v_cvt_pk_bf16_f32 v234, v84, v85\n"
        "v_cvt_pk_bf16_f32 v235, v86, v87\n"
        "v_mfma_f32_32x32x16_bf16 v[112:127], v[172:175], v[228:231], v[112:127]\n"
        "v_exp_f32 v88, v88\n"
        "v_exp_f32 v89, v89\n"
        "v_exp_f32 v90, v90\n"
        "v_exp_f32 v91, v91\n"
        "v_cvt_pk_bf16_f32 v236, v88, v89\n"
        "v_cvt_pk_bf16_f32 v237, v90, v91\n"
        "v_mfma_f32_32x32x16_bf16 v[128:143], v[168:171], v[232:235], v[128:143]\n"
        "v_exp_f32 v92, v92\n"
        "v_exp_f32 v93, v93\n"
        "v_exp_f32 v94, v94\n"
        "v_exp_f32 v95, v95\n"
        "v_cvt_pk_bf16_f32 v238, v92, v93\n"
        "v_cvt_pk_bf16_f32 v239, v94, v95\n"
        "v_mfma_f32_32x32x16_bf16 v[80:95], v[160:163], v[32:35], 0\n"
        "v_exp_f32 v64, v64\n"
        "v_exp_f32 v65, v65\n"
        "v_exp_f32 v66, v66\n"
        "v_exp_f32 v67, v67\n"
        "v_cvt_pk_bf16_f32 v224, v64, v65\n"
        "v_cvt_pk_bf16_f32 v225, v66, v67\n"
        "v_mfma_f32_32x32x16_bf16 v[80:95], v[164:167], v[36:39], v[80:95]\n"
        "s_waitcnt vmcnt(17)\n"
        "v_exp_f32 v68, v68\n"
        "v_exp_f32 v69, v69\n"
        "v_exp_f32 v70, v70\n"
        "v_exp_f32 v71, v71\n"
        "v_cvt_pk_bf16_f32 v226, v68, v69\n"
        "v_cvt_pk_bf16_f32 v227, v70, v71\n"
        "v_mfma_f32_32x32x16_bf16 v[128:143], v[172:175], v[236:239], v[128:143]\n"
        "v_exp_f32 v72, v72\n"
        "v_exp_f32 v73, v73\n"
        "v_exp_f32 v74, v74\n"
        "v_exp_f32 v75, v75\n"
        "v_cvt_pk_bf16_f32 v228, v72, v73\n"
        "v_cvt_pk_bf16_f32 v229, v74, v75\n"
        "v_mfma_f32_32x32x16_bf16 v[144:159], v[168:171], v[224:227], v[144:159]\n"
        "v_exp_f32 v76, v76\n"
        "v_exp_f32 v77, v77\n"
        "v_exp_f32 v78, v78\n"
        "v_exp_f32 v79, v79\n"
        "v_cvt_pk_bf16_f32 v230, v76, v77\n"
        "v_cvt_pk_bf16_f32 v231, v78, v79\n"
        "v_mfma_f32_32x32x16_bf16 v[64:79], v[176:179], v[40:43], 0\n"
        "v_exp_f32 v80, v80\n"
        "v_exp_f32 v81, v81\n"
        "v_exp_f32 v82, v82\n"
        "v_exp_f32 v83, v83\n"
        "v_cvt_pk_bf16_f32 v232, v80, v81\n"
        "v_cvt_pk_bf16_f32 v233, v82, v83\n"
        "v_mfma_f32_32x32x16_bf16 v[64:79], v[180:183], v[44:47], v[64:79]\n"
        "v_exp_f32 v84, v84\n"
        "v_exp_f32 v85, v85\n"
        "v_exp_f32 v86, v86\n"
        "v_exp_f32 v87, v87\n"
        "v_cvt_pk_bf16_f32 v234, v84, v85\n"
        "v_cvt_pk_bf16_f32 v235, v86, v87\n"
        "v_mfma_f32_32x32x16_bf16 v[144:159], v[172:175], v[228:231], v[144:159]\n"
        "v_exp_f32 v88, v88\n"
        "v_exp_f32 v89, v89\n"
        "v_exp_f32 v90, v90\n"
        "v_exp_f32 v91, v91\n"
        "v_cvt_pk_bf16_f32 v236, v88, v89\n"
        "v_cvt_pk_bf16_f32 v237, v90, v91\n"
        "v_mfma_f32_32x32x16_bf16 v[96:111], v[168:171], v[232:235], v[96:111]\n"
        "v_exp_f32 v92, v92\n"
        "v_exp_f32 v93, v93\n"
        "v_exp_f32 v94, v94\n"
        "v_exp_f32 v95, v95\n"
        "v_cvt_pk_bf16_f32 v238, v92, v93\n"
        "v_cvt_pk_bf16_f32 v239, v94, v95\n"
        "v_mfma_f32_32x32x16_bf16 v[80:95], v[176:179], v[48:51], 0\n"
        "global_load_lds_dwordx4 v1, s[22:23] offset:3072 sc1\n"
        "s_add_u32 m0, m0, 4096\n"
        "s_add_u32 s22, s22, 4096\n"
        "s_addc_u32 s23, s23, 0\n"
        "v_exp_f32 v64, v64\n"
        "v_exp_f32 v65, v65\n"
        "v_exp_f32 v66, v66\n"
        "v_exp_f32 v67, v67\n"
        "v_cvt_pk_bf16_f32 v224, v64, v65\n"
        "v_cvt_pk_bf16_f32 v225, v66, v67\n"
        "v_mfma_f32_32x32x16_bf16 v[80:95], v[180:183], v[52:55], v[80:95]\n"
        "s_waitcnt vmcnt(16)\n"
        "v_exp_f32 v68, v68\n"
        "v_exp_f32 v69, v69\n"
        "v_exp_f32 v70, v70\n"
        "v_exp_f32 v71, v71\n"
        "v_cvt_pk_bf16_f32 v226, v68, v69\n"
        "v_cvt_pk_bf16_f32 v227, v70, v71\n"
        "v_mfma_f32_32x32x16_bf16 v[96:111], v[172:175], v[236:239], v[96:111]\n"
        "global_load_lds_dwordx4 v1, s[22:23] offset:0 sc1\n"
        "v_exp_f32 v72, v72\n"
        "v_exp_f32 v73, v73\n"
        "v_exp_f32 v74, v74\n"
        "v_exp_f32 v75, v75\n"
        "v_cvt_pk_bf16_f32 v228, v72, v73\n"
        "v_cvt_pk_bf16_f32 v229, v74, v75\n"
        "v_mfma_f32_32x32x16_bf16 v[112:127], v[184:187], v[224:227], v[112:127]\n"
        "global_load_lds_dwordx4 v1, s[22:23] offset:1024 sc1\n"
        "v_exp_f32 v76, v76\n"
        "v_exp_f32 v77, v77\n"
        "v_exp_f32 v78, v78\n"
        "v_exp_f32 v79, v79\n"
        "v_cvt_pk_bf16_f32 v230, v76, v77\n"
        "v_cvt_pk_bf16_f32 v231, v78, v79\n"
        "v_mfma_f32_32x32x16_bf16 v[64:79], v[176:179], v[56:59], 0\n"
        "v_exp_f32 v80, v80\n"
        "v_exp_f32 v81, v81\n"
        "v_exp_f32 v82, v82\n"
        "v_exp_f32 v83, v83\n"
        "v_cvt_pk_bf16_f32 v232, v80, v81\n"
        "v_cvt_pk_bf16_f32 v233, v82, v83\n"
        "v_mfma_f32_32x32x16_bf16 v[64:79], v[180:183], v[60:63], v[64:79]\n"
        "v_exp_f32 v84, v84\n"
        "v_exp_f32 v85, v85\n"
        "v_exp_f32 v86, v86\n"
        "v_exp_f32 v87, v87\n"
        "v_cvt_pk_bf16_f32 v234, v84, v85\n"
        "v_cvt_pk_bf16_f32 v235, v86, v87\n"
        "v_mfma_f32_32x32x16_bf16 v[112:127], v[188:191], v[228:231], v[112:127]\n"
        "v_exp_f32 v88, v88\n"
        "v_exp_f32 v89, v89\n"
        "v_exp_f32 v90, v90\n"
        "v_exp_f32 v91, v91\n"
        "v_cvt_pk_bf16_f32 v236, v88, v89\n"
        "v_cvt_pk_bf16_f32 v237, v90, v91\n"
        "v_mfma_f32_32x32x16_bf16 v[128:143], v[184:187], v[232:235], v[128:143]\n"
        "v_exp_f32 v92, v92\n"
        "v_exp_f32 v93, v93\n"
        "v_exp_f32 v94, v94\n"
        "v_exp_f32 v95, v95\n"
        "v_cvt_pk_bf16_f32 v238, v92, v93\n"
        "v_cvt_pk_bf16_f32 v239, v94, v95\n"
        "v_mfma_f32_32x32x16_bf16 v[80:95], v[176:179], v[32:35], 0\n"
        "v_exp_f32 v64, v64\n"
        "v_exp_f32 v65, v65\n"
        "v_exp_f32 v66, v66\n"
        "v_exp_f32 v67, v67\n"
        "v_cvt_pk_bf16_f32 v224, v64, v65\n"
        "v_cvt_pk_bf16_f32 v225, v66, v67\n"
        "v_mfma_f32_32x32x16_bf16 v[80:95], v[180:183], v[36:39], v[80:95]\n"
        "s_waitcnt vmcnt(14)\n"
        "v_exp_f32 v68, v68\n"
        "v_exp_f32 v69, v69\n"
        "v_exp_f32 v70, v70\n"
        "v_exp_f32 v71, v71\n"
        "v_cvt_pk_bf16_f32 v226, v68, v69\n"
        "v_cvt_pk_bf16_f32 v227, v70, v71\n"
        "v_mfma_f32_32x32x16_bf16 v[128:143], v[188:191], v[236:239], v[128:143]\n"
        "v_exp_f32 v72, v72\n"
        "v_exp_f32 v73, v73\n"
        "v_exp_f32 v74, v74\n"
        "v_exp_f32 v75, v75\n"
        "v_cvt_pk_bf16_f32 v228, v72, v73\n"
        "v_cvt_pk_bf16_f32 v229, v74, v75\n"
        "v_mfma_f32_32x32x16_bf16 v[144:159], v[184:187], v[224:227], v[144:159]\n"
        "v_exp_f32 v76, v76\n"
        "v_exp_f32 v77, v77\n"
        "v_exp_f32 v78, v78\n"
        "v_exp_f32 v79, v79\n"
        "v_cvt_pk_bf16_f32 v230, v76, v77\n"
        "v_cvt_pk_bf16_f32 v231, v78, v79\n"
        "v_mfma_f32_32x32x16_bf16 v[64:79], v[192:195], v[40:43], 0\n"
        "v_exp_f32 v80, v80\n"
        "v_exp_f32 v81, v81\n"
        "v_exp_f32 v82, v82\n"
        "v_exp_f32 v83, v83\n"
        "v_cvt_pk_bf16_f32 v232, v80, v81\n"
        "v_cvt_pk_bf16_f32 v233, v82, v83\n"
        "v_mfma_f32_32x32x16_bf16 v[64:79], v[196:199], v[44:47], v[64:79]\n"
        "v_exp_f32 v84, v84\n"
        "v_exp_f32 v85, v85\n"
        "v_exp_f32 v86, v86\n"
        "v_exp_f32 v87, v87\n"
        "v_cvt_pk_bf16_f32 v234, v84, v85\n"
        "v_cvt_pk_bf16_f32 v235, v86, v87\n"
        "v_mfma_f32_32x32x16_bf16 v[144:159], v[188:191], v[228:231], v[144:159]\n"
        "v_exp_f32 v88, v88\n"
        "v_exp_f32 v89, v89\n"
        "v_exp_f32 v90, v90\n"
        "v_exp_f32 v91, v91\n"
        "v_cvt_pk_bf16_f32 v236, v88, v89\n"
        "v_cvt_pk_bf16_f32 v237, v90, v91\n"
        "v_mfma_f32_32x32x16_bf16 v[96:111], v[184:187], v[232:235], v[96:111]\n"
        "v_exp_f32 v92, v92\n"
        "v_exp_f32 v93, v93\n"
        "v_exp_f32 v94, v94\n"
        "v_exp_f32 v95, v95\n"
        "v_cvt_pk_bf16_f32 v238, v92, v93\n"
        "v_cvt_pk_bf16_f32 v239, v94, v95\n"
        "v_mfma_f32_32x32x16_bf16 v[80:95], v[192:195], v[48:51], 0\n"
        "global_load_lds_dwordx4 v1, s[22:23] offset:2048 sc1\n"
        "v_exp_f32 v64, v64\n"
        "v_exp_f32 v65, v65\n"
        "v_exp_f32 v66, v66\n"
        "v_exp_f32 v67, v67\n"
        "v_cvt_pk_bf16_f32 v224, v64, v65\n"
        "v_cvt_pk_bf16_f32 v225, v66, v67\n"
        "v_mfma_f32_32x32x16_bf16 v[80:95], v[196:199], v[52:55], v[80:95]\n"
        "s_waitcnt vmcnt(13)\n"
        "v_exp_f32 v68, v68\n"
        "v_exp_f32 v69, v69\n"
        "v_exp_f32 v70, v70\n"
        "v_exp_f32 v71, v71\n"
        "v_cvt_pk_bf16_f32 v226, v68, v69\n"
        "v_cvt_pk_bf16_f32 v227, v70, v71\n"
        "v_mfma_f32_32x32x16_bf16 v[96:111], v[188:191], v[236:239], v[96:111]\n"
        "global_load_lds_dwordx4 v1, s[22:23] offset:3072 sc1\n"
        "s_add_u32 m0, m0, 4096\n"
        "s_add_u32 s22, s22, 4096\n"
        "s_addc_u32 s23, s23, 0\n"
        "v_exp_f32 v72, v72\n"
        "v_exp_f32 v73, v73\n"
        "v_exp_f32 v74, v74\n"
        "v_exp_f32 v75, v75\n"
        "v_cvt_pk_bf16_f32 v228, v72, v73\n"
        "v_cvt_pk_bf16_f32 v229, v74, v75\n"
        "v_mfma_f32_32x32x16_bf16 v[112:127], v[200:203], v[224:227], v[112:127]\n"
        "global_load_lds_dwordx4 v1, s[22:23] offset:0 sc1\n"
        "v_exp_f32 v76, v76\n"
        "v_exp_f32 v77, v77\n"
        "v_exp_f32 v78, v78\n"
        "v_exp_f32 v79, v79\n"
        "v_cvt_pk_bf16_f32 v230, v76, v77\n"
        "v_cvt_pk_bf16_f32 v231, v78, v79\n"
        "v_mfma_f32_32x32x16_bf16 v[64:79], v[192:195], v[56:59], 0\n"
        "v_exp_f32 v80, v80\n"
        "v_exp_f32 v81, v81\n"
        "v_exp_f32 v82, v82\n"
        "v_exp_f32 v83, v83\n"
        "v_cvt_pk_bf16_f32 v232, v80, v81\n"
        "v_cvt_pk_bf16_f32 v233, v82, v83\n"
        "v_mfma_f32_32x32x16_bf16 v[64:79], v[196:199], v[60:63], v[64:79]\n"
        "v_exp_f32 v84, v84\n"
        "v_exp_f32 v85, v85\n"
        "v_exp_f32 v86, v86\n"
        "v_exp_f32 v87, v87\n"
        "v_cvt_pk_bf16_f32 v234, v84, v85\n"
        "v_cvt_pk_bf16_f32 v235, v86, v87\n"
        "v_mfma_f32_32x32x16_bf16 v[112:127], v[204:207], v[228:231], v[112:127]\n"
        "v_exp_f32 v88, v88\n"
        "v_exp_f32 v89, v89\n"
        "v_exp_f32 v90, v90\n"
        "v_exp_f32 v91, v91\n"
        "v_cvt_pk_bf16_f32 v236, v88, v89\n"
        "v_cvt_pk_bf16_f32 v237, v90, v91\n"
        "v_mfma_f32_32x32x16_bf16 v[128:143], v[200:203], v[232:235], v[128:143]\n"
        "v_exp_f32 v92, v92\n"
        "v_exp_f32 v93, v93\n"
        "v_exp_f32 v94, v94\n"
        "v_exp_f32 v95, v95\n"
        "v_cvt_pk_bf16_f32 v238, v92, v93\n"
        "v_cvt_pk_bf16_f32 v239, v94, v95\n"
        "v_mfma_f32_32x32x16_bf16 v[80:95], v[192:195], v[32:35], 0\n"
        "v_exp_f32 v64, v64\n"
        "v_exp_f32 v65, v65\n"
        "v_exp_f32 v66, v66\n"
        "v_exp_f32 v67, v67\n"
        "v_cvt_pk_bf16_f32 v224, v64, v65\n"
        "v_cvt_pk_bf16_f32 v225, v66, v67\n"
        "v_mfma_f32_32x32x16_bf16 v[80:95], v[196:199], v[36:39], v[80:95]\n"
        "s_waitcnt vmcnt(11)\n"
        "v_exp_f32 v68, v68\n"
        "v_exp_f32 v69, v69\n"
        "v_exp_f32 v70, v70\n"
        "v_exp_f32 v71, v71\n"
        "v_cvt_pk_bf16_f32 v226, v68, v69\n"
        "v_cvt_pk_bf16_f32 v227, v70, v71\n"
        "v_mfma_f32_32x32x16_bf16 v[128:143], v[204:207], v[236:239], v[128:143]\n"
        "v_exp_f32 v72, v72\n"
        "v_exp_f32 v73, v73\n"
        "v_exp_f32 v74, v74\n"
        "v_exp_f32 v75, v75\n"
        "v_cvt_pk_bf16_f32 v228, v72, v73\n"
        "v_cvt_pk_bf16_f32 v229, v74, v75\n"
        "v_mfma_f32_32x32x16_bf16 v[144:159], v[200:203], v[224:227], v[144:159]\n"
        "v_exp_f32 v76, v76\n"
        "v_exp_f32 v77, v77\n"
        "v_exp_f32 v78, v78\n"
        "v_exp_f32 v79, v79\n"
        "v_cvt_pk_bf16_f32 v230, v76, v77\n"
        "v_cvt_pk_bf16_f32 v231, v78, v79\n"
        "v_mfma_f32_32x32x16_bf16 v[64:79], v[208:211], v[40:43], 0\n"
        "v_exp_f32 v80, v80\n"
        "v_exp_f32 v81, v81\n"
        "v_exp_f32 v82, v82\n"
        "v_exp_f32 v83, v83\n"
        "v_cvt_pk_bf16_f32 v232, v80, v81\n"
        "v_cvt_pk_bf16_f32 v233, v82, v83\n"
        "v_mfma_f32_32x32x16_bf16 v[64:79], v[212:215], v[44:47], v[64:79]\n"
        "v_exp_f32 v84, v84\n"
        "v_exp_f32 v85, v85\n"
        "v_exp_f32 v86, v86\n"
        "v_exp_f32 v87, v87\n"
        "v_cvt_pk_bf16_f32 v234, v84, v85\n"
        "v_cvt_pk_bf16_f32 v235, v86, v87\n"
        "v_mfma_f32_32x32x16_bf16 v[144:159], v[204:207], v[228:231], v[144:159]\n"
        "v_exp_f32 v88, v88\n"
        "v_exp_f32 v89, v89\n"
        "v_exp_f32 v90, v90\n"
        "v_exp_f32 v91, v91\n"
        "v_cvt_pk_bf16_f32 v236, v88, v89\n"
        "v_cvt_pk_bf16_f32 v237, v90, v91\n"
        "v_mfma_f32_32x32x16_bf16 v[96:111], v[200:203], v[232:235], v[96:111]\n"
        "v_exp_f32 v92, v92\n"
        "v_exp_f32 v93, v93\n"
        "v_exp_f32 v94, v94\n"
        "v_exp_f32 v95, v95\n"
        "v_cvt_pk_bf16_f32 v238, v92, v93\n"
        "v_cvt_pk_bf16_f32 v239, v94, v95\n"
        "v_mfma_f32_32x32x16_bf16 v[80:95], v[208:211], v[48:51], 0\n"
        "global_load_lds_dwordx4 v1, s[22:23] offset:1024 sc1\n"
        "v_exp_f32 v64, v64\n"
        "v_exp_f32 v65, v65\n"
        "v_exp_f32 v66, v66\n"
        "v_exp_f32 v67, v67\n"
        "v_cvt_pk_bf16_f32 v224, v64, v65\n"
        "v_cvt_pk_bf16_f32 v225, v66, v67\n"
        "v_mfma_f32_32x32x16_bf16 v[80:95], v[212:215], v[52:55], v[80:95]\n"
        "s_waitcnt vmcnt(9)\n"
        "v_exp_f32 v68, v68\n"
        "v_exp_f32 v69, v69\n"
        "v_exp_f32 v70, v70\n"
        "v_exp_f32 v71, v71\n"
        "v_cvt_pk_bf16_f32 v226, v68, v69\n"
        "v_cvt_pk_bf16_f32 v227, v70, v71\n"
        "v_mfma_f32_32x32x16_bf16 v[96:111], v[204:207], v[236:239], v[96:111]\n"
        "v_exp_f32 v72, v72\n"
        "v_exp_f32 v73, v73\n"
        "v_exp_f32 v74, v74\n"
        "v_exp_f32 v75, v75\n"
        "v_cvt_pk_bf16_f32 v228, v72, v73\n"
        "v_cvt_pk_bf16_f32 v229, v74, v75\n"
        "v_mfma_f32_32x32x16_bf16 v[112:127], v[216:219], v[224:227], v[112:127]\n"
        "v_exp_f32 v76, v76\n"
        "v_exp_f32 v77, v77\n"
        "v_exp_f32 v78, v78\n"
        "v_exp_f32 v79, v79\n"
        "v_cvt_pk_bf16_f32 v230, v76, v77\n"
        "v_cvt_pk_bf16_f32 v231, v78, v79\n"
        "v_mfma_f32_32x32x16_bf16 v[64:79], v[208:211], v[56:59], 0\n"
        "v_exp_f32 v80, v80\n"
        "v_exp_f32 v81, v81\n"
        "v_exp_f32 v82, v82\n"
        "v_exp_f32 v83, v83\n"
        "v_cvt_pk_bf16_f32 v232, v80, v81\n"
        "v_cvt_pk_bf16_f32 v233, v82, v83\n"
        "v_mfma_f32_32x32x16_bf16 v[64:79], v[212:215], v[60:63], v[64:79]\n"
        "v_exp_f32 v84, v84\n"
        "v_exp_f32 v85, v85\n"
        "v_exp_f32 v86, v86\n"
        "v_exp_f32 v87, v87\n"
        "v_cvt_pk_bf16_f32 v234, v84, v85\n"
        "v_cvt_pk_bf16_f32 v235, v86, v87\n"
        "v_mfma_f32_32x32x16_bf16 v[112:127], v[220:223], v[228:231], v[112:127]\n"
        "v_exp_f32 v88, v88\n"
        "v_exp_f32 v89, v89\n"
        "v_exp_f32 v90, v90\n"
        "v_exp_f32 v91, v91\n"
        "v_cvt_pk_bf16_f32 v236, v88, v89\n"
        "v_cvt_pk_bf16_f32 v237, v90, v91\n"
        "v_mfma_f32_32x32x16_bf16 v[128:143], v[216:219], v[232:235], v[128:143]\n"
        "v_exp_f32 v92, v92\n"
        "v_exp_f32 v93, v93\n"
        "v_exp_f32 v94, v94\n"
        "v_exp_f32 v95, v95\n"
        "v_cvt_pk_bf16_f32 v238, v92, v93\n"
        "v_cvt_pk_bf16_f32 v239, v94, v95\n"
        "v_mfma_f32_32x32x16_bf16 v[80:95], v[208:211], v[32:35], 0\n"
        "ds_write_b128 v21, v[112:115] offset:38912\n"
        "v_exp_f32 v64, v64\n"
        "v_exp_f32 v65, v65\n"
        "v_exp_f32 v66, v66\n"
        "v_exp_f32 v67, v67\n"
        "v_cvt_pk_bf16_f32 v224, v64, v65\n"
        "v_cvt_pk_bf16_f32 v225, v66, v67\n"
        "v_mfma_f32_32x32x16_bf16 v[80:95], v[212:215], v[36:39], v[80:95]\n"
        "s_waitcnt vmcnt(63)\n"
        "v_exp_f32 v68, v68\n"
        "v_exp_f32 v69, v69\n"
        "v_exp_f32 v70, v70\n"
        "v_exp_f32 v71, v71\n"
        "v_cvt_pk_bf16_f32 v226, v68, v69\n"
        "v_cvt_pk_bf16_f32 v227, v70, v71\n"
        "v_mfma_f32_32x32x16_bf16 v[128:143], v[220:223], v[236:239], v[128:143]\n"
        "ds_write_b128 v21, v[116:119] offset:39936\n"
        "v_exp_f32 v72, v72\n"
        "v_exp_f32 v73, v73\n"
        "v_exp_f32 v74, v74\n"
        "v_exp_f32 v75, v75\n"
        "v_cvt_pk_bf16_f32 v228, v72, v73\n"
        "v_cvt_pk_bf16_f32 v229, v74, v75\n"
        "v_mfma_f32_32x32x16_bf16 v[144:159], v[216:219], v[224:227], v[144:159]\n"
        "ds_write_b128 v21, v[120:123] offset:40960\n"
        "v_exp_f32 v76, v76\n"
        "v_exp_f32 v77, v77\n"
        "v_exp_f32 v78, v78\n"
        "v_exp_f32 v79, v79\n"
        "v_cvt_pk_bf16_f32 v230, v76, v77\n"
        "v_cvt_pk_bf16_f32 v231, v78, v79\n"
        "ds_write_b128 v21, v[124:127] offset:41984\n"
        "v_exp_f32 v80, v80\n"
        "v_exp_f32 v81, v81\n"
        "v_exp_f32 v82, v82\n"
        "v_exp_f32 v83, v83\n"
        "v_cvt_pk_bf16_f32 v232, v80, v81\n"
        "v_cvt_pk_bf16_f32 v233, v82, v83\n"
        "ds_write_b128 v22, v[128:131] offset:43008\n"
        "v_exp_f32 v84, v84\n"
        "v_exp_f32 v85, v85\n"
        "v_exp_f32 v86, v86\n"
        "v_exp_f32 v87, v87\n"
        "v_cvt_pk_bf16_f32 v234, v84, v85\n"
        "v_cvt_pk_bf16_f32 v235, v86, v87\n"
        "v_mfma_f32_32x32x16_bf16 v[144:159], v[220:223], v[228:231], v[144:159]\n"
        "ds_write_b128 v22, v[132:135] offset:44032\n"
        "v_exp_f32 v88, v88\n"
        "v_exp_f32 v89, v89\n"
        "v_exp_f32 v90, v90\n"
        "v_exp_f32 v91, v91\n"
        "v_cvt_pk_bf16_f32 v236, v88, v89\n"
        "v_cvt_pk_bf16_f32 v237, v90, v91\n"
        "v_mfma_f32_32x32x16_bf16 v[96:111], v[216:219], v[232:235], v[96:111]\n"
        "ds_write_b128 v22, v[136:139] offset:45056\n"
        "v_exp_f32 v92, v92\n"
        "v_exp_f32 v93, v93\n"
        "v_exp_f32 v94, v94\n"
        "v_exp_f32 v95, v95\n"
        "v_cvt_pk_bf16_f32 v238, v92, v93\n"
        "v_cvt_pk_bf16_f32 v239, v94, v95\n"
        "ds_write_b128 v22, v[140:143] offset:46080\n"
        ".Latt2:\n"
        "s_nop 0\n"
        "v_mfma_f32_32x32x16_bf16 v[96:111], v[220:223], v[236:239], v[96:111]\n"
        "ds_write_b128 v23, v[144:147] offset:47104\n"
        "ds_write_b128 v23, v[148:151] offset:48128\n"
        "ds_write_b128 v23, v[152:155] offset:49152\n"
        "ds_write_b128 v23, v[156:159] offset:50176\n"
        "v_min3_f32 v17, v124, v140, v156\n"
        "v_add_f32 v18, v124, v140\n"
        "v_add_f32 v18, v18, v156\n"
        "s_nop 4\n"
        "v_add_f32 v18, v18, v108\n"
        "v_min_f32 v17, v17, v108\n"
        "v_cmp_ngt_f32 vcc, s30, v18\n"
        "v_cmp_nlt_f32 s[32:33], s31, v17\n"
        "s_nop 0\n"
        "s_or_b64 vcc, vcc, s[32:33]\n"
        "s_cmp_lg_u32 vcc_hi, 0\n"
        "s_cbranch_scc1 .Lslow\n"
        ".Lcombine:\n"
        "v_mov_b32 v16, 155136\n"
        "s_waitcnt vmcnt(0)\n"
        "s_waitcnt lgkmcnt(0)\n"
        "s_barrier\n"
        "ds_read_b32 v17, v16\n"
        "ds_read_b128 v[124:127], v20 offset:41984\n"
        "ds_read_b128 v[140:143], v20 offset:46080\n"
        "ds_read_b128 v[156:159], v20 offset:50176\n"
        "ds_read_b128 v[112:115], v20 offset:38912\n"
        "ds_read_b128 v[128:131], v20 offset:43008\n"
        "ds_read_b128 v[144:147], v20 offset:47104\n"
        "ds_read_b128 v[116:119], v20 offset:39936\n"
        "ds_read_b128 v[132:135], v20 offset:44032\n"
        "ds_read_b128 v[148:151], v20 offset:48128\n"
        "ds_read_b128 v[120:123], v20 offset:40960\n"
        "ds_read_b128 v[136:139], v20 offset:45056\n"
        "ds_read_b128 v[152:155], v20 offset:49152\n"
        "s_waitcnt lgkmcnt(12)\n"
        "v_cmp_ne_u32 vcc, 0, v17\n"
        "s_cbranch_vccnz .Lrefactor\n"
        ".Lfactored:\n"
        "s_waitcnt lgkmcnt(9)\n"
        "v_mul_f32 v18, v108, v248\n"
        "v_fma_f32 v18, v124, v250, v18\n"
        "v_fma_f32 v18, v140, v252, v18\n"
        "v_fma_f32 v18, v156, v254, v18\n"
        "v_mov_b32 v19, v18\n"
        "s_nop 1\n"
        "v_permlane32_swap_b32 v18, v19\n"
        "v_rcp_f32 v19, v19\n"
        "s_nop 0\n"
        "v_mul_f32 v248, v248, v19\n"
        "v_mul_f32 v250, v250, v19\n"
        "v_mul_f32 v252, v252, v19\n"
        "v_mul_f32 v254, v254, v19\n"
        "s_waitcnt lgkmcnt(3)\n"
        "v_pk_mul_f32 v[96:97], v[96:97], v[248:249] op_sel_hi:[1,0]\n"
        "v_pk_mul_f32 v[98:99], v[98:99], v[248:249] op_sel_hi:[1,0]\n"
        "v_pk_mul_f32 v[100:101], v[100:101], v[248:249] op_sel_hi:[1,0]\n"
        "v_pk_mul_f32 v[102:103], v[102:103], v[248:249] op_sel_hi:[1,0]\n"
        "v_pk_fma_f32 v[96:97], v[112:113], v[250:251], v[96:97] op_sel_hi:[1,0,1]\n"
        "v_pk_fma_f32 v[98:99], v[114:115], v[250:251], v[98:99] op_sel_hi:[1,0,1]\n"
        "v_pk_fma_f32 v[100:101], v[116:117], v[250:251], v[100:101] op_sel_hi:[1,0,1]\n"
        "v_pk_fma_f32 v[102:103], v[118:119], v[250:251], v[102:103] op_sel_hi:[1,0,1]\n"
        "v_pk_fma_f32 v[96:97], v[128:129], v[252:253], v[96:97] op_sel_hi:[1,0,1]\n"
        "v_pk_fma_f32 v[98:99], v[130:131], v[252:253], v[98:99] op_sel_hi:[1,0,1]\n"
        "v_pk_fma_f32 v[100:101], v[132:133], v[252:253], v[100:101] op_sel_hi:[1,0,1]\n"
        "v_pk_fma_f32 v[102:103], v[134:135], v[252:253], v[102:103] op_sel_hi:[1,0,1]\n"
        "v_pk_fma_f32 v[96:97], v[144:145], v[254:255], v[96:97] op_sel_hi:[1,0,1]\n"
        "v_pk_fma_f32 v[98:99], v[146:147], v[254:255], v[98:99] op_sel_hi:[1,0,1]\n"
        "v_pk_fma_f32 v[100:101], v[148:149], v[254:255], v[100:101] op_sel_hi:[1,0,1]\n"
        "v_pk_fma_f32 v[102:103], v[150:151], v[254:255], v[102:103] op_sel_hi:[1,0,1]\n"
        "v_cvt_pk_bf16_f32 v224, v96, v97\n"
        "v_cvt_pk_bf16_f32 v225, v98, v99\n"
        "v_cvt_pk_bf16_f32 v226, v100, v101\n"
        "v_cvt_pk_bf16_f32 v227, v102, v103\n"
        "buffer_store_dwordx4 v[224:227], v1, s[8:11], s36 offen offset:0 sc1\n"
        "s_waitcnt lgkmcnt(0)\n"
        "v_pk_mul_f32 v[104:105], v[104:105], v[248:249] op_sel_hi:[1,0]\n"
        "v_pk_mul_f32 v[106:107], v[106:107], v[248:249] op_sel_hi:[1,0]\n"
        "v_pk_mul_f32 v[108:109], v[108:109], v[248:249] op_sel_hi:[1,0]\n"
        "v_pk_mul_f32 v[110:111], v[110:111], v[248:249] op_sel_hi:[1,0]\n"
        "v_pk_fma_f32 v[104:105], v[120:121], v[250:251], v[104:105] op_sel_hi:[1,0,1]\n"
        "v_pk_fma_f32 v[106:107], v[122:123], v[250:251], v[106:107] op_sel_hi:[1,0,1]\n"
        "v_pk_fma_f32 v[108:109], v[124:125], v[250:251], v[108:109] op_sel_hi:[1,0,1]\n"
        "v_pk_fma_f32 v[110:111], v[126:127], v[250:251], v[110:111] op_sel_hi:[1,0,1]\n"
        "v_pk_fma_f32 v[104:105], v[136:137], v[252:253], v[104:105] op_sel_hi:[1,0,1]\n"
        "v_pk_fma_f32 v[106:107], v[138:139], v[252:253], v[106:107] op_sel_hi:[1,0,1]\n"
        "v_pk_fma_f32 v[108:109], v[140:141], v[252:253], v[108:109] op_sel_hi:[1,0,1]\n"
        "v_pk_fma_f32 v[110:111], v[142:143], v[252:253], v[110:111] op_sel_hi:[1,0,1]\n"
        "v_pk_fma_f32 v[104:105], v[152:153], v[254:255], v[104:105] op_sel_hi:[1,0,1]\n"
        "v_pk_fma_f32 v[106:107], v[154:155], v[254:255], v[106:107] op_sel_hi:[1,0,1]\n"
        "v_pk_fma_f32 v[108:109], v[156:157], v[254:255], v[108:109] op_sel_hi:[1,0,1]\n"
        "v_pk_fma_f32 v[110:111], v[158:159], v[254:255], v[110:111] op_sel_hi:[1,0,1]\n"
        "v_cvt_pk_bf16_f32 v228, v104, v105\n"
        "v_cvt_pk_bf16_f32 v229, v106, v107\n"
        "v_cvt_pk_bf16_f32 v230, v108, v109\n"
        "v_cvt_pk_bf16_f32 v231, v110, v111\n"
        "v_cndmask_b32 v230, v230, 0, s[40:41]\n"
        "v_cndmask_b32 v231, v231, 0, s[40:41]\n"
        "buffer_store_dwordx4 v[228:231], v1, s[8:11], s36 offen offset:1024 sc1\n"
        "v_mov_b32 v16, s96\n"
        "v_mov_b32 v17, s97\n"
        "v_mov_b32 v18, s96\n"
        "v_mov_b32 v19, s97\n"
        "s_waitcnt vmcnt(0)\n"
        "s_mov_b64 exec, 0xff\n"
        "buffer_store_dwordx4 v[16:19], v1, s[8:11], s37 offen sc1\n"
        "s_mov_b64 exec, -1\n"
        "s_barrier\n"
        "s_cmp_ge_u32 s26, 14\n"
        "s_cbranch_scc1 .Lout_done\n"
        "s_mul_i32 s30, s24, 8192\n"
        "s_add_u32 s30, s30, 104448\n"
        "s_mov_b32 m0, s30\n"
        "s_mul_i32 s52, s26, 137216\n"
        "s_add_u32 s52, s52, s30\n"
        "s_add_u32 s52, s52, 3000320\n"
        "s_cmp_lg_u32 s28, 0\n"
        "s_cbranch_scc1 .Lres1\n"
        "s_mul_i32 s31, s27, 28672\n"
        "s_mul_i32 s39, s26, 64\n"
        "s_add_u32 s31, s31, s39\n"
        "v_lshrrev_b32 v16, 6, v1\n"
        "v_and_b32 v16, 8, v16\n"
        "v_sub_u32 v16, v12, v16\n"
        "buffer_load_dwordx2 v[160:161], v16, s[56:59], s31 offen offset:0\n"
        "buffer_load_dwordx2 v[162:163], v16, s[56:59], s31 offen offset:16\n"
        "buffer_load_dwordx2 v[164:165], v16, s[56:59], s31 offen offset:32\n"
        "buffer_load_dwordx2 v[166:167], v16, s[56:59], s31 offen offset:48\n"
        "s_branch .Lres2\n"
        ".Lres1:\n"
        "s_mul_i32 s31, s26, 2048\n"
        "s_add_u32 s31, s31, s53\n"
        "s_add_u32 s31, s31, -13451264\n"
        "buffer_load_dwordx4 v[160:163], v1, s[8:11], s31 offen offset:0 sc1\n"
        "buffer_load_dwordx4 v[164:167], v1, s[8:11], s31 offen offset:1024 sc1\n"
        ".Lres2:\n"
        "s_mul_i32 s30, s26, 2048\n"
        "s_add_u32 s30, s30, s53\n"
        "s_add_u32 s30, s30, 32768\n"
        "s_mul_i32 s33, s26, 256\n"
        "s_add_u32 s33, s33, s53\n"
        "s_add_u32 s33, s33, 61440\n"
        "s_mul_i32 s32, s27, 2048\n"
        "s_add_u32 s32, s32, s29\n"
        "s_add_u32 s32, s32, 133120\n"
        "s_mul_i32 s34, s27, 2048\n"
        "s_mul_i32 s39, s26, 128\n"
        "s_add_u32 s34, s34, s39\n"
        "s_add_u32 s34, s34, s29\n"
        "s_add_u32 s34, s34, 264192\n"
        "s_mov_b32 s31, s53\n"
        "s_mov_b32 s44, 0x20000\n"
        "s_sleep 14\n"
        "buffer_load_dwordx2 v[16:17], v5, s[8:11], s32 offen sc1\n"
        "buffer_load_dwordx4 v1, s[8:11], s52 offen offset:0 sc1 lds\n"
        "buffer_load_dwordx4 v1, s[8:11], s52 offen offset:1024 sc1 lds\n"
        "buffer_load_dwordx4 v1, s[8:11], s52 offen offset:2048 sc1 lds\n"
        "buffer_load_dwordx4 v1, s[8:11], s52 offen offset:3072 sc1 lds\n"
        "s_add_u32 m0, m0, 4096\n"
        "s_add_u32 s52, s52, 4096\n"
        "buffer_load_dwordx4 v1, s[8:11], s52 offen offset:0 sc1 lds\n"
        "buffer_load_dwordx4 v1, s[8:11], s52 offen offset:1024 sc1 lds\n"
        "buffer_load_dwordx4 v1, s[8:11], s52 offen offset:2048 sc1 lds\n"
        "buffer_load_dwordx4 v1, s[8:11], s52 offen offset:3072 sc1 lds\n"
        "s_waitcnt vmcnt(8)\n"
        "v_cmp_ne_u64 vcc, s[96:97], v[16:17]\n"
        "s_cbranch_vccz .Lpolled6\n"
        ".Lpoll5:\n"
        "buffer_load_dwordx2 v[16:17], v5, s[8:11], s32 offen sc1\n"
        "s_waitcnt vmcnt(0)\n"
        "v_cmp_ne_u64 vcc, s[96:97], v[16:17]\n"
        "s_cbranch_vccz .Lpolled6\n"
        "s_sub_u32 s44, s44, 1\n"
        "s_cbranch_scc0 .Lpoll5\n"
        ".Lpolled6:\n"
        "buffer_load_dwordx4 v[32:35], v1, s[8:11], s31 offen offset:0 sc1\n"
        "buffer_load_dwordx4 v[36:39], v1, s[8:11], s31 offen offset:1024 sc1\n"
        "buffer_load_dwordx4 v[40:43], v1, s[8:11], s31 offen offset:2048 sc1\n"
        "buffer_load_dwordx4 v[44:47], v1, s[8:11], s31 offen offset:3072 sc1\n"
        "s_add_u32 s31, s31, 4096\n"
        "buffer_load_dwordx4 v[48:51], v1, s[8:11], s31 offen offset:0 sc1\n"
        "buffer_load_dwordx4 v[52:55], v1, s[8:11], s31 offen offset:1024 sc1\n"
        "buffer_load_dwordx4 v[56:59], v1, s[8:11], s31 offen offset:2048 sc1\n"
        "buffer_load_dwordx4 v[60:63], v1, s[8:11], s31 offen offset:3072 sc1\n"
        "s_add_u32 s31, s31, 4096\n"
        "buffer_load_dwordx4 v[64:67], v1, s[8:11], s31 offen offset:0 sc1\n"
        "buffer_load_dwordx4 v[68:71], v1, s[8:11], s31 offen offset:1024 sc1\n"
        "s_waitcnt vmcnt(10)\n"
        "s_barrier\n"
        "ds_read_b128 v[208:211], v2 offset:38912\n"
        "ds_read_b128 v[212:215], v2 offset:39936\n"
        "ds_read_b128 v[216:219], v2 offset:40960\n"
        "ds_read_b128 v[220:223], v2 offset:41984\n"
        "ds_read_b128 v[224:227], v2 offset:43008\n"
        "ds_read_b128 v[228:231], v2 offset:44032\n"
        "ds_read_b128 v[232:235], v2 offset:45056\n"
        "ds_read_b128 v[236:239], v2 offset:46080\n"
        "v_lshlrev_b32 v176, 16, v160\n"
        "v_and_b32 v177, 0xffff0000, v160\n"
        "v_lshlrev_b32 v178, 16, v161\n"
        "v_and_b32 v179, 0xffff0000, v161\n"
        "v_lshlrev_b32 v180, 16, v162\n"
        "v_and_b32 v181, 0xffff0000, v162\n"
        "v_lshlrev_b32 v182, 16, v163\n"
        "v_and_b32 v183, 0xffff0000, v163\n"
        "v_lshlrev_b32 v184, 16, v164\n"
        "v_and_b32 v185, 0xffff0000, v164\n"
        "v_lshlrev_b32 v186, 16, v165\n"
        "v_and_b32 v187, 0xffff0000, v165\n"
        "v_lshlrev_b32 v188, 16, v166\n"
        "v_and_b32 v189, 0xffff0000, v166\n"
        "v_lshlrev_b32 v190, 16, v167\n"
        "v_and_b32 v191, 0xffff0000, v167\n"
        "s_waitcnt vmcnt(9) lgkmcnt(7)\n"
        "s_nop 0\n"
        "v_mfma_f32_32x32x16_bf16 v[176:191], v[208:211], v[32:35], v[176:191]\n"
        "ds_read_b128 v[240:243], v2 offset:47104\n"
        "buffer_load_dwordx4 v[72:75], v1, s[8:11], s31 offen offset:2048 sc1\n"
        "s_waitcnt vmcnt(9) lgkmcnt(7)\n"
        "v_mfma_f32_32x32x16_bf16 v[176:191], v[212:215], v[36:39], v[176:191]\n"
        "ds_read_b128 v[244:247], v2 offset:48128\n"
        "buffer_load_dwordx4 v[76:79], v1, s[8:11], s31 offen offset:3072 sc1\n"
        "s_waitcnt vmcnt(9) lgkmcnt(7)\n"
        "v_mfma_f32_32x32x16_bf16 v[176:191], v[216:219], v[40:43], v[176:191]\n"
        "ds_read_b128 v[248:251], v2 offset:49152\n"
        "s_add_u32 s31, s31, 4096\n"
        "buffer_load_dwordx4 v[80:83], v1, s[8:11], s31 offen offset:0 sc1\n"
        "s_waitcnt vmcnt(9) lgkmcnt(7)\n"
        "v_mfma_f32_32x32x16_bf16 v[176:191], v[220:223], v[44:47], v[176:191]\n"
        "ds_read_b128 v[252:255], v2 offset:50176\n"
        "buffer_load_dwordx4 v[84:87], v1, s[8:11], s31 offen offset:1024 sc1\n"
        "s_waitcnt vmcnt(9) lgkmcnt(7)\n"
        "v_mfma_f32_32x32x16_bf16 v[176:191], v[224:227], v[48:51], v[176:191]\n"
        "ds_read_b128 v[208:211], v2 offset:51200\n"
        "buffer_load_dwordx4 v[88:91], v1, s[8:11], s31 offen offset:2048 sc1\n"
        "s_waitcnt vmcnt(9) lgkmcnt(7)\n"
        "v_mfma_f32_32x32x16_bf16 v[176:191], v[228:231], v[52:55], v[176:191]\n"
        "ds_read_b128 v[212:215], v2 offset:52224\n"
        "buffer_load_dwordx4 v[92:95], v1, s[8:11], s31 offen offset:3072 sc1\n"
        "s_waitcnt vmcnt(9) lgkmcnt(7)\n"
        "v_mfma_f32_32x32x16_bf16 v[176:191], v[232:235], v[56:59], v[176:191]\n"
        "ds_read_b128 v[216:219], v2 offset:53248\n"
        "s_add_u32 s31, s31, 4096\n"
        "buffer_load_dwordx4 v[96:99], v1, s[8:11], s31 offen offset:0 sc1\n"
        "s_waitcnt vmcnt(9) lgkmcnt(7)\n"
        "v_mfma_f32_32x32x16_bf16 v[176:191], v[236:239], v[60:63], v[176:191]\n"
        "ds_read_b128 v[220:223], v2 offset:54272\n"
        "buffer_load_dwordx4 v[100:103], v1, s[8:11], s31 offen offset:1024 sc1\n"
        "s_waitcnt vmcnt(9) lgkmcnt(7)\n"
        "v_mfma_f32_32x32x16_bf16 v[176:191], v[240:243], v[64:67], v[176:191]\n"
        "ds_read_b128 v[224:227], v2 offset:55296\n"
        "buffer_load_dwordx4 v[104:107], v1, s[8:11], s31 offen offset:2048 sc1\n"
        "s_waitcnt vmcnt(9) lgkmcnt(7)\n"
        "v_mfma_f32_32x32x16_bf16 v[176:191], v[244:247], v[68:71], v[176:191]\n"
        "ds_read_b128 v[228:231], v2 offset:56320\n"
        "buffer_load_dwordx4 v[108:111], v1, s[8:11], s31 offen offset:3072 sc1\n"
        "s_waitcnt vmcnt(9) lgkmcnt(7)\n"
        "v_mfma_f32_32x32x16_bf16 v[176:191], v[248:251], v[72:75], v[176:191]\n"
        "ds_read_b128 v[232:235], v2 offset:57344\n"
        "s_add_u32 s31, s31, 4096\n"
        "buffer_load_dwordx4 v[112:115], v1, s[8:11], s31 offen offset:0 sc1\n"
        "s_waitcnt vmcnt(9) lgkmcnt(7)\n"
        "v_mfma_f32_32x32x16_bf16 v[176:191], v[252:255], v[76:79], v[176:191]\n"
        "ds_read_b128 v[236:239], v2 offset:58368\n"
        "buffer_load_dwordx4 v[116:119], v1, s[8:11], s31 offen offset:1024 sc1\n"
        "s_waitcnt vmcnt(9) lgkmcnt(7)\n"
        "v_mfma_f32_32x32x16_bf16 v[176:191], v[208:211], v[80:83], v[176:191]\n"
        "ds_read_b128 v[240:243], v2 offset:59392\n"
        "buffer_load_dwordx4 v[120:123], v1, s[8:11], s31 offen offset:2048 sc1\n"
        "s_waitcnt vmcnt(9) lgkmcnt(7)\n"
        "v_mfma_f32_32x32x16_bf16 v[176:191], v[212:215], v[84:87], v[176:191]\n"
        "ds_read_b128 v[244:247], v2 offset:60416\n"
        "buffer_load_dwordx4 v[124:127], v1, s[8:11], s31 offen offset:3072 sc1\n"
        "s_waitcnt vmcnt(9) lgkmcnt(7)\n"
        "v_mfma_f32_32x32x16_bf16 v[176:191], v[216:219], v[88:91], v[176:191]\n"
        "ds_read_b128 v[248:251], v2 offset:61440\n"
        "s_add_u32 s31, s31, 4096\n"
        "buffer_load_dwordx4 v[128:131], v1, s[8:11], s31 offen offset:0 sc1\n"
        "s_waitcnt vmcnt(9) lgkmcnt(7)\n"
        "v_mfma_f32_32x32x16_bf16 v[176:191], v[220:223], v[92:95], v[176:191]\n"
        "ds_read_b128 v[252:255], v2 offset:62464\n"
        "buffer_load_dwordx4 v[132:135], v1, s[8:11], s31 offen offset:1024 sc1\n"
        "s_waitcnt vmcnt(9) lgkmcnt(7)\n"
        "v_mfma_f32_32x32x16_bf16 v[176:191], v[224:227], v[96:99], v[176:191]\n"
        "ds_read_b128 v[208:211], v2 offset:63488\n"
        "buffer_load_dwordx4 v[136:139], v1, s[8:11], s31 offen offset:2048 sc1\n"
        "s_waitcnt vmcnt(9) lgkmcnt(7)\n"
        "v_mfma_f32_32x32x16_bf16 v[176:191], v[228:231], v[100:103], v[176:191]\n"
        "ds_read_b128 v[212:215], v2 offset:64512\n"
        "buffer_load_dwordx4 v[140:143], v1, s[8:11], s31 offen offset:3072 sc1\n"
        "s_waitcnt vmcnt(9) lgkmcnt(7)\n"
        "v_mfma_f32_32x32x16_bf16 v[176:191], v[232:235], v[104:107], v[176:191]\n"
        "ds_read_b128 v[216:219], v3 offset:0\n"
        "s_add_u32 s31, s31, 4096\n"
        "buffer_load_dwordx4 v[144:147], v1, s[8:11], s31 offen offset:0 sc1\n"
        "s_waitcnt vmcnt(9) lgkmcnt(7)\n"
        "v_mfma_f32_32x32x16_bf16 v[176:191], v[236:239], v[108:111], v[176:191]\n"
        "ds_read_b128 v[220:223], v3 offset:1024\n"
        "buffer_load_dwordx4 v[148:151], v1, s[8:11], s31 offen offset:1024 sc1\n"
        "s_waitcnt vmcnt(9) lgkmcnt(7)\n"
        "v_mfma_f32_32x32x16_bf16 v[176:191], v[240:243], v[112:115], v[176:191]\n"
        "ds_read_b128 v[224:227], v3 offset:2048\n"
        "buffer_load_dwordx4 v[152:155], v1, s[8:11], s31 offen offset:2048 sc1\n"
        "s_waitcnt vmcnt(9) lgkmcnt(7)\n"
        "v_mfma_f32_32x32x16_bf16 v[176:191], v[244:247], v[116:119], v[176:191]\n"
        "ds_read_b128 v[228:231], v3 offset:3072\n"
        "buffer_load_dwordx4 v[156:159], v1, s[8:11], s31 offen offset:3072 sc1\n"
        "s_waitcnt vmcnt(9) lgkmcnt(7)\n"
        "v_mfma_f32_32x32x16_bf16 v[176:191], v[248:251], v[120:123], v[176:191]\n"
        "ds_read_b128 v[232:235], v3 offset:4096\n"
        "s_waitcnt vmcnt(8) lgkmcnt(7)\n"
        "v_mfma_f32_32x32x16_bf16 v[176:191], v[252:255], v[124:127], v[176:191]\n"
        "ds_read_b128 v[236:239], v3 offset:5120\n"
        "s_waitcnt vmcnt(7) lgkmcnt(7)\n"
        "v_mfma_f32_32x32x16_bf16 v[176:191], v[208:211], v[128:131], v[176:191]\n"
        "s_waitcnt vmcnt(6) lgkmcnt(6)\n"
        "v_mfma_f32_32x32x16_bf16 v[176:191], v[212:215], v[132:135], v[176:191]\n"
        "s_waitcnt vmcnt(5) lgkmcnt(5)\n"
        "v_mfma_f32_32x32x16_bf16 v[176:191], v[216:219], v[136:139], v[176:191]\n"
        "s_waitcnt vmcnt(4) lgkmcnt(4)\n"
        "v_mfma_f32_32x32x16_bf16 v[176:191], v[220:223], v[140:143], v[176:191]\n"
        "s_waitcnt vmcnt(3) lgkmcnt(3)\n"
        "v_mfma_f32_32x32x16_bf16 v[176:191], v[224:227], v[144:147], v[176:191]\n"
        "s_waitcnt vmcnt(2) lgkmcnt(2)\n"
        "v_mfma_f32_32x32x16_bf16 v[176:191], v[228:231], v[148:151], v[176:191]\n"
        "s_waitcnt vmcnt(1) lgkmcnt(1)\n"
        "v_mfma_f32_32x32x16_bf16 v[176:191], v[232:235], v[152:155], v[176:191]\n"
        "s_waitcnt vmcnt(0) lgkmcnt(0)\n"
        "v_mfma_f32_32x32x16_bf16 v[176:191], v[236:239], v[156:159], v[176:191]\n"
        "s_nop 7\n"
        "s_nop 3\n"
        "v_cvt_pk_bf16_f32 v24, v176, v177\n"
        "v_cvt_pk_bf16_f32 v25, v178, v179\n"
        "v_cvt_pk_bf16_f32 v26, v180, v181\n"
        "v_cvt_pk_bf16_f32 v27, v182, v183\n"
        "buffer_store_dwordx4 v[24:27], v1, s[8:11], s30 offen offset:0 sc1\n"
        "v_cvt_pk_bf16_f32 v28, v184, v185\n"
        "v_cvt_pk_bf16_f32 v29, v186, v187\n"
        "v_cvt_pk_bf16_f32 v30, v188, v189\n"
        "v_cvt_pk_bf16_f32 v31, v190, v191\n"
        "buffer_store_dwordx4 v[28:31], v1, s[8:11], s30 offen offset:1024 sc1\n"
        "v_pk_add_f32 v[16:17], v[176:177], v[178:179]\n"
        "v_pk_mul_f32 v[18:19], v[176:177], v[176:177]\n"
        "v_pk_fma_f32 v[18:19], v[178:179], v[178:179], v[18:19]\n"
        "v_pk_add_f32 v[16:17], v[16:17], v[180:181]\n"
        "v_pk_fma_f32 v[18:19], v[180:181], v[180:181], v[18:19]\n"
        "v_pk_add_f32 v[16:17], v[16:17], v[182:183]\n"
        "v_pk_fma_f32 v[18:19], v[182:183], v[182:183], v[18:19]\n"
        "v_pk_add_f32 v[16:17], v[16:17], v[184:185]\n"
        "v_pk_fma_f32 v[18:19], v[184:185], v[184:185], v[18:19]\n"
        "v_pk_add_f32 v[16:17], v[16:17], v[186:187]\n"
        "v_pk_fma_f32 v[18:19], v[186:187], v[186:187], v[18:19]\n"
        "v_pk_add_f32 v[16:17], v[16:17], v[188:189]\n"
        "v_pk_fma_f32 v[18:19], v[188:189], v[188:189], v[18:19]\n"
        "v_pk_add_f32 v[16:17], v[16:17], v[190:191]\n"
        "v_pk_fma_f32 v[18:19], v[190:191], v[190:191], v[18:19]\n"
        "v_add_f32 v16, v16, v17\n"
        "v_add_f32 v17, v18, v19\n"
        "v_mov_b32 v18, v16\n"
        "v_mov_b32 v19, v17\n"
        "s_nop 0\n"
        "v_permlane32_swap_b32 v18, v16\n"
        "v_permlane32_swap_b32 v19, v17\n"
        "v_pk_add_f32 v[16:17], v[16:17], v[18:19]\n"
        "buffer_store_dwordx2 v[16:17], v8, s[8:11], s33 offen sc1\n"
        "v_mov_b32 v16, s96\n"
        "v_mov_b32 v17, s97\n"
        "v_mov_b32 v18, s96\n"
        "v_mov_b32 v19, s97\n"
        "s_waitcnt vmcnt(0)\n"
        "s_mov_b64 exec, 0xff\n"
        "buffer_store_dwordx4 v[16:19], v1, s[8:11], s34 offen sc1\n"
        "s_mov_b64 exec, -1\n"
        "s_branch .Lout_sleep\n"
        ".Lout_done:\n"
        "s_sleep 60\n"
        ".Lout_sleep:\n"
        "s_sleep 12\n"
        ".Lout_end:\n"
        "s_add_u32 s30, s53, 32768\n"
        "s_mul_i32 s33, s26, 7168\n"
        "s_add_u32 s33, s33, s53\n"
        "s_add_u32 s33, s33, 65536\n"
        "s_add_u32 s34, s33, 4096\n"
        "s_mul_i32 s32, s27, 2048\n"
        "s_add_u32 s32, s32, s29\n"
        "s_add_u32 s32, s32, 264192\n"
        "s_mul_i32 s35, s27, 2048\n"
        "s_mul_i32 s39, s26, 128\n"
        "s_add_u32 s35, s35, s39\n"
        "s_add_u32 s35, s35, s29\n"
        "s_add_u32 s35, s35, 395264\n"
        "ds_read_b128 v[208:211], v9 offset:0\n"
        "ds_read_b128 v[212:215], v1 offset:0\n"
        "ds_read_b128 v[216:219], v9 offset:512\n"
        "ds_read_b128 v[220:223], v1 offset:1024\n"
        "ds_read_b128 v[224:227], v9 offset:1024\n"
        "ds_read_b128 v[228:231], v1 offset:2048\n"
        "ds_read_b128 v[232:235], v9 offset:1536\n"
        "ds_read_b128 v[236:239], v1 offset:3072\n"
        "s_mov_b32 s44, 0x20000\n"
        ".Lpoll7:\n"
        "buffer_load_dwordx2 v[16:17], v4, s[8:11], s32 offen sc1\n"
        "s_waitcnt vmcnt(0)\n"
        "v_cmp_ne_u64 vcc, s[96:97], v[16:17]\n"
        "s_cbranch_vccz .Lpolled8\n"
        "s_sub_u32 s44, s44, 1\n"
        "s_cbranch_scc0 .Lpoll7\n"
        ".Lpolled8:\n"
        "buffer_load_dwordx4 v[32:35], v1, s[8:11], s30 offen offset:0 sc1\n"
        "buffer_load_dwordx4 v[36:39], v1, s[8:11], s30 offen offset:1024 sc1\n"
        "buffer_load_dwordx2 v[144:145], v7, s[8:11], s30 offen offset:0 sc1\n"
        "buffer_load_dwordx2 v[146:147], v7, s[8:11], s30 offen offset:256 sc1\n"
        "buffer_load_dwordx2 v[148:149], v7, s[8:11], s30 offen offset:512 sc1\n"
        "buffer_load_dwordx2 v[150:151], v7, s[8:11], s30 offen offset:768 sc1\n"
        "buffer_load_dwordx2 v[152:153], v7, s[8:11], s30 offen offset:1024 sc1\n"
        "buffer_load_dwordx2 v[154:155], v7, s[8:11], s30 offen offset:1280 sc1\n"
        "buffer_load_dwordx2 v[156:157], v7, s[8:11], s30 offen offset:1536 sc1\n"
        "buffer_load_dwordx4 v[40:43], v1, s[8:11], s30 offen offset:2048 sc1\n"
        "buffer_load_dwordx4 v[44:47], v1, s[8:11], s30 offen offset:3072 sc1\n"
        "s_add_u32 s30, s30, 4096\n"
        "buffer_load_dwordx4 v[48:51], v1, s[8:11], s30 offen offset:0 sc1\n"
        "buffer_load_dwordx4 v[52:55], v1, s[8:11], s30 offen offset:1024 sc1\n"
        "buffer_load_dwordx4 v[56:59], v1, s[8:11], s30 offen offset:2048 sc1\n"
        "buffer_load_dwordx4 v[60:63], v1, s[8:11], s30 offen offset:3072 sc1\n"
        "s_add_u32 s30, s30, 4096\n"
        "buffer_load_dwordx4 v[64:67], v1, s[8:11], s30 offen offset:0 sc1\n"
        "buffer_load_dwordx4 v[68:71], v1, s[8:11], s30 offen offset:1024 sc1\n"
        "s_waitcnt vmcnt(16) lgkmcnt(7)\n"
        "v_mfma_f32_32x32x16_bf16 v[160:175], v[208:211], v[32:35], 0\n"
        "ds_read_b128 v[240:243], v9 offset:2048\n"
        "buffer_load_dwordx4 v[72:75], v1, s[8:11], s30 offen offset:2048 sc1\n"
        "s_waitcnt lgkmcnt(7)\n"
        "v_mfma_f32_32x32x16_bf16 v[176:191], v[212:215], v[32:35], 0\n"
        "ds_read_b128 v[244:247], v1 offset:4096\n"
        "s_waitcnt vmcnt(16) lgkmcnt(7)\n"
        "v_mfma_f32_32x32x16_bf16 v[160:175], v[216:219], v[36:39], v[160:175]\n"
        "ds_read_b128 v[248:251], v9 offset:2560\n"
        "buffer_load_dwordx4 v[76:79], v1, s[8:11], s30 offen offset:3072 sc1\n"
        "s_waitcnt lgkmcnt(7)\n"
        "v_mfma_f32_32x32x16_bf16 v[176:191], v[220:223], v[36:39], v[176:191]\n"
        "ds_read_b128 v[252:255], v1 offset:5120\n"
        "s_waitcnt vmcnt(9) lgkmcnt(7)\n"
        "v_mfma_f32_32x32x16_bf16 v[160:175], v[224:227], v[40:43], v[160:175]\n"
        "ds_read_b128 v[208:211], v9 offset:3072\n"
        "s_add_u32 s30, s30, 4096\n"
        "buffer_load_dwordx4 v[80:83], v1, s[8:11], s30 offen offset:0 sc1\n"
        "s_waitcnt lgkmcnt(7)\n"
        "v_mfma_f32_32x32x16_bf16 v[176:191], v[228:231], v[40:43], v[176:191]\n"
        "ds_read_b128 v[212:215], v1 offset:6144\n"
        "s_waitcnt vmcnt(9) lgkmcnt(7)\n"
        "v_mfma_f32_32x32x16_bf16 v[160:175], v[232:235], v[44:47], v[160:175]\n"
        "ds_read_b128 v[216:219], v9 offset:3584\n"
        "buffer_load_dwordx4 v[84:87], v1, s[8:11], s30 offen offset:1024 sc1\n"
        "v_add_f32 v144, v144, v146\n"
        "v_add_f32 v145, v145, v147\n"
        "s_waitcnt lgkmcnt(7)\n"
        "v_mfma_f32_32x32x16_bf16 v[176:191], v[236:239], v[44:47], v[176:191]\n"
        "ds_read_b128 v[220:223], v1 offset:7168\n"
        "v_add_f32 v144, v144, v148\n"
        "v_add_f32 v145, v145, v149\n"
        "s_waitcnt vmcnt(9) lgkmcnt(7)\n"
        "v_mfma_f32_32x32x16_bf16 v[160:175], v[240:243], v[48:51], v[160:175]\n"
        "ds_read_b128 v[224:227], v9 offset:4096\n"
        "buffer_load_dwordx4 v[88:91], v1, s[8:11], s30 offen offset:2048 sc1\n"
        "v_add_f32 v144, v144, v150\n"
        "v_add_f32 v145, v145, v151\n"
        "s_waitcnt lgkmcnt(7)\n"
        "v_mfma_f32_32x32x16_bf16 v[176:191], v[244:247], v[48:51], v[176:191]\n"
        "ds_read_b128 v[228:231], v1 offset:8192\n"
        "v_add_f32 v144, v144, v152\n"
        "v_add_f32 v145, v145, v153\n"
        "s_waitcnt vmcnt(9) lgkmcnt(7)\n"
        "v_mfma_f32_32x32x16_bf16 v[160:175], v[248:251], v[52:55], v[160:175]\n"
        "ds_read_b128 v[232:235], v9 offset:4608\n"
        "buffer_load_dwordx4 v[92:95], v1, s[8:11], s30 offen offset:3072 sc1\n"
        "v_add_f32 v144, v144, v154\n"
        "v_add_f32 v145, v145, v155\n"
        "s_waitcnt lgkmcnt(7)\n"
        "v_mfma_f32_32x32x16_bf16 v[176:191], v[252:255], v[52:55], v[176:191]\n"
        "ds_read_b128 v[236:239], v1 offset:9216\n"
        "v_add_f32 v144, v144, v156\n"
        "v_add_f32 v145, v145, v157\n"
        "s_waitcnt vmcnt(9) lgkmcnt(7)\n"
        "v_mfma_f32_32x32x16_bf16 v[160:175], v[208:211], v[56:59], v[160:175]\n"
        "ds_read_b128 v[240:243], v9 offset:5120\n"
        "s_add_u32 s30, s30, 4096\n"
        "buffer_load_dwordx4 v[96:99], v1, s[8:11], s30 offen offset:0 sc1\n"
        "v_mov_b32 v16, v144\n"
        "v_mov_b32 v17, v145\n"
        "s_waitcnt lgkmcnt(7)\n"
        "v_mfma_f32_32x32x16_bf16 v[176:191], v[212:215], v[56:59], v[176:191]\n"
        "ds_read_b128 v[244:247], v1 offset:10240\n"
        "s_nop 0\n"
        "v_permlane32_swap_b32 v16, v144\n"
        "s_waitcnt vmcnt(9) lgkmcnt(7)\n"
        "v_mfma_f32_32x32x16_bf16 v[160:175], v[216:219], v[60:63], v[160:175]\n"
        "ds_read_b128 v[248:251], v9 offset:5632\n"
        "buffer_load_dwordx4 v[100:103], v1, s[8:11], s30 offen offset:1024 sc1\n"
        "v_permlane32_swap_b32 v17, v145\n"
        "v_add_f32 v144, v144, v16\n"
        "s_waitcnt lgkmcnt(7)\n"
        "v_mfma_f32_32x32x16_bf16 v[176:191], v[220:223], v[60:63], v[176:191]\n"
        "ds_read_b128 v[252:255], v1 offset:11264\n"
        "v_add_f32 v145, v145, v17\n"
        "v_mul_f32 v144, 0xbb124925, v144\n"
        "s_waitcnt vmcnt(9) lgkmcnt(7)\n"
        "v_mfma_f32_32x32x16_bf16 v[160:175], v[224:227], v[64:67], v[160:175]\n"
        "ds_read_b128 v[208:211], v9 offset:6144\n"
        "buffer_load_dwordx4 v[104:107], v1, s[8:11], s30 offen offset:2048 sc1\n"
        "v_mul_f32 v145, 0x3b124925, v145\n"
        "v_fma_f32 v145, -v144, v144, v145\n"
        "s_waitcnt lgkmcnt(7)\n"
        "v_mfma_f32_32x32x16_bf16 v[176:191], v[228:231], v[64:67], v[176:191]\n"
        "ds_read_b128 v[212:215], v1 offset:12288\n"
        "v_max_f32 v145, 0, v145\n"
        "v_add_f32 v145, 0x3727c5ac, v145\n"
        "s_waitcnt vmcnt(9) lgkmcnt(7)\n"
        "v_mfma_f32_32x32x16_bf16 v[160:175], v[232:235], v[68:71], v[160:175]\n"
        "ds_read_b128 v[216:219], v9 offset:6656\n"
        "buffer_load_dwordx4 v[108:111], v1, s[8:11], s30 offen offset:3072 sc1\n"
        "v_rsq_f32 v152, v145\n"
        "s_nop 0\n"
        "s_waitcnt lgkmcnt(7)\n"
        "v_mfma_f32_32x32x16_bf16 v[176:191], v[236:239], v[68:71], v[176:191]\n"
        "ds_read_b128 v[220:223], v1 offset:13312\n"
        "v_mul_f32 v154, v145, v152\n"
        "v_cvt_pk_bf16_f32 v149, v144, v154\n"
        "s_waitcnt vmcnt(9) lgkmcnt(7)\n"
        "v_mfma_f32_32x32x16_bf16 v[160:175], v[240:243], v[72:75], v[160:175]\n"
        "ds_read_b128 v[224:227], v9 offset:7168\n"
        "s_add_u32 s30, s30, 4096\n"
        "buffer_load_dwordx4 v[112:115], v1, s[8:11], s30 offen offset:0 sc1\n"
        "v_lshlrev_b32 v16, 16, v149\n"
        "v_and_b32 v17, 0xffff0000, v149\n"
        "s_waitcnt lgkmcnt(7)\n"
        "v_mfma_f32_32x32x16_bf16 v[176:191], v[244:247], v[72:75], v[176:191]\n"
        "ds_read_b128 v[228:231], v1 offset:14336\n"
        "v_sub_f32 v16, v144, v16\n"
        "v_sub_f32 v17, v154, v17\n"
        "s_waitcnt vmcnt(9) lgkmcnt(7)\n"
        "v_mfma_f32_32x32x16_bf16 v[160:175], v[248:251], v[76:79], v[160:175]\n"
        "ds_read_b128 v[232:235], v9 offset:7680\n"
        "buffer_load_dwordx4 v[116:119], v1, s[8:11], s30 offen offset:1024 sc1\n"
        "v_cvt_pk_bf16_f32 v16, v16, v17\n"
        "v_and_b32 v17, 0xffff, v149\n"
        "s_waitcnt lgkmcnt(7)\n"
        "v_mfma_f32_32x32x16_bf16 v[176:191], v[252:255], v[76:79], v[176:191]\n"
        "ds_read_b128 v[236:239], v1 offset:15360\n"
        "v_lshl_or_b32 v148, v16, 16, v17\n"
        "v_lshrrev_b32 v150, 16, v16\n"
        "s_waitcnt vmcnt(9) lgkmcnt(7)\n"
        "v_mfma_f32_32x32x16_bf16 v[160:175], v[208:211], v[80:83], v[160:175]\n"
        "ds_read_b128 v[240:243], v9 offset:8192\n"
        "buffer_load_dwordx4 v[120:123], v1, s[8:11], s30 offen offset:2048 sc1\n"
        "v_mov_b32 v151, 0\n"
        "v_mul_f32 v156, 0xc0135761, v152\n"
        "s_waitcnt lgkmcnt(7)\n"
        "v_mfma_f32_32x32x16_bf16 v[176:191], v[212:215], v[80:83], v[176:191]\n"
        "ds_read_b128 v[244:247], v1 offset:16384\n"
        "v_mul_f32 v158, v152, v152\n"
        "v_mul_f32 v158, v158, v152\n"
        "s_waitcnt vmcnt(9) lgkmcnt(7)\n"
        "v_mfma_f32_32x32x16_bf16 v[160:175], v[216:219], v[84:87], v[160:175]\n"
        "ds_read_b128 v[248:251], v9 offset:8704\n"
        "buffer_load_dwordx4 v[124:127], v1, s[8:11], s30 offen offset:3072 sc1\n"
        "v_mul_f32 v158, 0xbdd2d3e8, v158\n"
        "s_waitcnt lgkmcnt(7)\n"
        "v_mfma_f32_32x32x16_bf16 v[176:191], v[220:223], v[84:87], v[176:191]\n"
        "ds_read_b128 v[252:255], v1 offset:17408\n"
        "s_waitcnt vmcnt(9) lgkmcnt(7)\n"
        "v_mfma_f32_32x32x16_bf16 v[160:175], v[224:227], v[88:91], v[160:175]\n"
        "ds_read_b128 v[208:211], v9 offset:9216\n"
        "s_add_u32 s30, s30, 4096\n"
        "buffer_load_dwordx4 v[128:131], v1, s[8:11], s30 offen offset:0 sc1\n"
        "s_waitcnt lgkmcnt(7)\n"
        "v_mfma_f32_32x32x16_bf16 v[176:191], v[228:231], v[88:91], v[176:191]\n"
        "ds_read_b128 v[212:215], v1 offset:18432\n"
        "s_waitcnt vmcnt(9) lgkmcnt(7)\n"
        "v_mfma_f32_32x32x16_bf16 v[160:175], v[232:235], v[92:95], v[160:175]\n"
        "ds_read_b128 v[216:219], v9 offset:9728\n"
        "buffer_load_dwordx4 v[132:135], v1, s[8:11], s30 offen offset:1024 sc1\n"
        "s_waitcnt lgkmcnt(7)\n"
        "v_mfma_f32_32x32x16_bf16 v[176:191], v[236:239], v[92:95], v[176:191]\n"
        "ds_read_b128 v[220:223], v1 offset:19456\n"
        "s_waitcnt vmcnt(9) lgkmcnt(7)\n"
        "v_mfma_f32_32x32x16_bf16 v[160:175], v[240:243], v[96:99], v[160:175]\n"
        "ds_read_b128 v[224:227], v9 offset:10240\n"
        "buffer_load_dwordx4 v[136:139], v1, s[8:11], s30 offen offset:2048 sc1\n"
        "s_waitcnt lgkmcnt(7)\n"
        "v_mfma_f32_32x32x16_bf16 v[176:191], v[244:247], v[96:99], v[176:191]\n"
        "ds_read_b128 v[228:231], v1 offset:20480\n"
        "s_waitcnt vmcnt(9) lgkmcnt(7)\n"
        "v_mfma_f32_32x32x16_bf16 v[160:175], v[248:251], v[100:103], v[160:175]\n"
        "ds_read_b128 v[232:235], v9 offset:10752\n"
        "buffer_load_dwordx4 v[140:143], v1, s[8:11], s30 offen offset:3072 sc1\n"
        "s_waitcnt lgkmcnt(7)\n"
        "v_mfma_f32_32x32x16_bf16 v[176:191], v[252:255], v[100:103], v[176:191]\n"
        "ds_read_b128 v[236:239], v1 offset:21504\n"
        "s_waitcnt vmcnt(9) lgkmcnt(7)\n"
        "v_mfma_f32_32x32x16_bf16 v[160:175], v[208:211], v[104:107], v[160:175]\n"
        "ds_read_b128 v[240:243], v9 offset:11264\n"
        "s_waitcnt lgkmcnt(7)\n"
        "v_mfma_f32_32x32x16_bf16 v[176:191], v[212:215], v[104:107], v[176:191]\n"
        "ds_read_b128 v[244:247], v1 offset:22528\n"
        "s_waitcnt vmcnt(8) lgkmcnt(7)\n"
        "v_mfma_f32_32x32x16_bf16 v[160:175], v[216:219], v[108:111], v[160:175]\n"
        "ds_read_b128 v[248:251], v9 offset:11776\n"
        "s_waitcnt lgkmcnt(7)\n"
        "v_mfma_f32_32x32x16_bf16 v[176:191], v[220:223], v[108:111], v[176:191]\n"
        "ds_read_b128 v[252:255], v1 offset:23552\n"
        "s_waitcnt vmcnt(7) lgkmcnt(7)\n"
        "v_mfma_f32_32x32x16_bf16 v[160:175], v[224:227], v[112:115], v[160:175]\n"
        "ds_read_b128 v[208:211], v9 offset:12288\n"
        "s_waitcnt lgkmcnt(7)\n"
        "v_mfma_f32_32x32x16_bf16 v[176:191], v[228:231], v[112:115], v[176:191]\n"
        "ds_read_b128 v[212:215], v1 offset:24576\n"
        "s_waitcnt vmcnt(6) lgkmcnt(7)\n"
        "v_mfma_f32_32x32x16_bf16 v[160:175], v[232:235], v[116:119], v[160:175]\n"
        "ds_read_b128 v[216:219], v9 offset:12800\n"
        "s_waitcnt lgkmcnt(7)\n"
        "v_mfma_f32_32x32x16_bf16 v[176:191], v[236:239], v[116:119], v[176:191]\n"
        "ds_read_b128 v[220:223], v1 offset:25600\n"
        "s_waitcnt vmcnt(5) lgkmcnt(7)\n"
        "v_mfma_f32_32x32x16_bf16 v[160:175], v[240:243], v[120:123], v[160:175]\n"
        "ds_read_b128 v[224:227], v9 offset:13312\n"
        "s_waitcnt lgkmcnt(7)\n"
        "v_mfma_f32_32x32x16_bf16 v[176:191], v[244:247], v[120:123], v[176:191]\n"
        "ds_read_b128 v[228:231], v1 offset:26624\n"
        "s_waitcnt vmcnt(4) lgkmcnt(7)\n"
        "v_mfma_f32_32x32x16_bf16 v[160:175], v[248:251], v[124:127], v[160:175]\n"
        "ds_read_b128 v[232:235], v9 offset:13824\n"
        "s_waitcnt lgkmcnt(7)\n"
        "v_mfma_f32_32x32x16_bf16 v[176:191], v[252:255], v[124:127], v[176:191]\n"
        "ds_read_b128 v[236:239], v1 offset:27648\n"
        "s_waitcnt vmcnt(3) lgkmcnt(7)\n"
        "v_mfma_f32_32x32x16_bf16 v[160:175], v[208:211], v[128:131], v[160:175]\n"
        "ds_read_b128 v[240:243], v9 offset:14336\n"
        "s_waitcnt lgkmcnt(7)\n"
        "v_mfma_f32_32x32x16_bf16 v[176:191], v[212:215], v[128:131], v[176:191]\n"
        "ds_read_b128 v[244:247], v1 offset:28672\n"
        "s_waitcnt vmcnt(2) lgkmcnt(7)\n"
        "v_mfma_f32_32x32x16_bf16 v[160:175], v[216:219], v[132:135], v[160:175]\n"
        "ds_read_b128 v[248:251], v1 offset:29696\n"
        "s_waitcnt lgkmcnt(7)\n"
        "v_mfma_f32_32x32x16_bf16 v[176:191], v[220:223], v[132:135], v[176:191]\n"
        "ds_read_b128 v[252:255], v1 offset:30720\n"
        "s_waitcnt vmcnt(1) lgkmcnt(7)\n"
        "v_mfma_f32_32x32x16_bf16 v[160:175], v[224:227], v[136:139], v[160:175]\n"
        "ds_read_b128 v[208:211], v1 offset:31744\n"
        "s_waitcnt lgkmcnt(7)\n"
        "v_mfma_f32_32x32x16_bf16 v[176:191], v[228:231], v[136:139], v[176:191]\n"
        "ds_read_b128 v[212:215], v1 offset:32768\n"
        "s_waitcnt vmcnt(0) lgkmcnt(7)\n"
        "v_mfma_f32_32x32x16_bf16 v[160:175], v[232:235], v[140:143], v[160:175]\n"
        "ds_read_b128 v[216:219], v1 offset:33792\n"
        "s_waitcnt lgkmcnt(7)\n"
        "v_mfma_f32_32x32x16_bf16 v[176:191], v[236:239], v[140:143], v[176:191]\n"
        "ds_read_b128 v[220:223], v1 offset:34816\n"
        "s_waitcnt lgkmcnt(7)\n"
        "v_mfma_f32_32x32x16_bf16 v[160:175], v[240:243], v[148:151], v[160:175]\n"
        "ds_read_b128 v[224:227], v1 offset:35840\n"
        "s_waitcnt lgkmcnt(7)\n"
        "v_mfma_f32_32x32x16_bf16 v[176:191], v[244:247], v[148:151], v[176:191]\n"
        "ds_read_b128 v[228:231], v1 offset:36864\n"
        "s_waitcnt lgkmcnt(7)\n"
        "v_mfma_f32_32x32x16_bf16 v[192:207], v[248:251], v[32:35], 0\n"
        "ds_read_b128 v[232:235], v1 offset:37888\n"
        "s_waitcnt lgkmcnt(7)\n"
        "v_mfma_f32_32x32x16_bf16 v[192:207], v[252:255], v[36:39], v[192:207]\n"
        "ds_read_b128 v[236:239], v1 offset:38912\n"
        "s_nop 1\n"
        "v_mul_f32 v16, v160, v160\n"
        "v_mul_f32 v17, v161, v161\n"
        "v_fma_f32 v16, v16, v158, v156\n"
        "v_fma_f32 v17, v17, v158, v156\n"
        "v_mul_f32 v16, v160, v16\n"
        "v_mul_f32 v17, v161, v17\n"
        "s_nop 0\n"
        "s_waitcnt lgkmcnt(7)\n"
        "v_mfma_f32_32x32x16_bf16 v[192:207], v[208:211], v[40:43], v[192:207]\n"
        "ds_read_b128 v[240:243], v1 offset:39936\n"
        "v_exp_f32 v16, v16\n"
        "v_exp_f32 v17, v17\n"
        "v_fma_f32 v16, v16, v154, v154\n"
        "v_fma_f32 v17, v17, v154, v154\n"
        "v_rcp_f32 v16, v16\n"
        "s_nop 0\n"
        "v_rcp_f32 v17, v17\n"
        "v_mul_f32 v160, v160, v16\n"
        "s_waitcnt lgkmcnt(7)\n"
        "v_mfma_f32_32x32x16_bf16 v[192:207], v[212:215], v[44:47], v[192:207]\n"
        "ds_read_b128 v[244:247], v1 offset:40960\n"
        "v_mul_f32 v161, v161, v17\n"
        "v_cvt_pk_bf16_f32 v24, v160, v161\n"
        "v_mul_f32 v16, v162, v162\n"
        "v_mul_f32 v17, v163, v163\n"
        "s_nop 0\n"
        "v_fma_f32 v16, v16, v158, v156\n"
        "v_fma_f32 v17, v17, v158, v156\n"
        "v_mul_f32 v16, v162, v16\n"
        "s_waitcnt lgkmcnt(7)\n"
        "v_mfma_f32_32x32x16_bf16 v[192:207], v[216:219], v[48:51], v[192:207]\n"
        "ds_read_b128 v[248:251], v1 offset:41984\n"
        "v_mul_f32 v17, v163, v17\n"
        "v_exp_f32 v16, v16\n"
        "s_nop 0\n"
        "v_exp_f32 v17, v17\n"
        "v_fma_f32 v16, v16, v154, v154\n"
        "v_fma_f32 v17, v17, v154, v154\n"
        "v_rcp_f32 v16, v16\n"
        "s_waitcnt lgkmcnt(7)\n"
        "v_mfma_f32_32x32x16_bf16 v[192:207], v[220:223], v[52:55], v[192:207]\n"
        "ds_read_b128 v[252:255], v1 offset:43008\n"
        "v_rcp_f32 v17, v17\n"
        "s_nop 0\n"
        "v_mul_f32 v162, v162, v16\n"
        "v_mul_f32 v163, v163, v17\n"
        "v_cvt_pk_bf16_f32 v25, v162, v163\n"
        "v_mul_f32 v16, v164, v164\n"
        "v_mul_f32 v17, v165, v165\n"
        "v_fma_f32 v16, v16, v158, v156\n"
        "s_waitcnt lgkmcnt(7)\n"
        "v_mfma_f32_32x32x16_bf16 v[192:207], v[224:227], v[56:59], v[192:207]\n"
        "ds_read_b128 v[208:211], v1 offset:44032\n"
        "s_nop 0\n"
        "v_fma_f32 v17, v17, v158, v156\n"
        "v_mul_f32 v16, v164, v16\n"
        "v_mul_f32 v17, v165, v17\n"
        "v_exp_f32 v16, v16\n"
        "v_exp_f32 v17, v17\n"
        "s_nop 0\n"
        "v_fma_f32 v16, v16, v154, v154\n"
        "s_waitcnt lgkmcnt(7)\n"
        "v_mfma_f32_32x32x16_bf16 v[192:207], v[228:231], v[60:63], v[192:207]\n"
        "ds_read_b128 v[212:215], v1 offset:45056\n"
        "v_fma_f32 v17, v17, v154, v154\n"
        "v_rcp_f32 v16, v16\n"
        "v_rcp_f32 v17, v17\n"
        "v_mul_f32 v164, v164, v16\n"
        "v_mul_f32 v165, v165, v17\n"
        "s_nop 0\n"
        "v_cvt_pk_bf16_f32 v26, v164, v165\n"
        "v_mul_f32 v16, v166, v166\n"
        "s_waitcnt lgkmcnt(7)\n"
        "v_mfma_f32_32x32x16_bf16 v[192:207], v[232:235], v[64:67], v[192:207]\n"
        "ds_read_b128 v[216:219], v1 offset:46080\n"
        "v_mul_f32 v17, v167, v167\n"
        "v_fma_f32 v16, v16, v158, v156\n"
        "v_fma_f32 v17, v17, v158, v156\n"
        "s_nop 0\n"
        "v_mul_f32 v16, v166, v16\n"
        "v_mul_f32 v17, v167, v17\n"
        "v_exp_f32 v16, v16\n"
        "s_waitcnt lgkmcnt(7)\n"
        "v_mfma_f32_32x32x16_bf16 v[192:207], v[236:239], v[68:71], v[192:207]\n"
        "ds_read_b128 v[220:223], v1 offset:47104\n"
        "v_exp_f32 v17, v17\n"
        "v_fma_f32 v16, v16, v154, v154\n"
        "s_nop 0\n"
        "v_fma_f32 v17, v17, v154, v154\n"
        "v_rcp_f32 v16, v16\n"
        "v_rcp_f32 v17, v17\n"
        "v_mul_f32 v166, v166, v16\n"
        "v_mul_f32 v167, v167, v17\n"
        "s_waitcnt lgkmcnt(7)\n"
        "v_mfma_f32_32x32x16_bf16 v[192:207], v[240:243], v[72:75], v[192:207]\n"
        "ds_read_b128 v[224:227], v1 offset:48128\n"
        "v_cvt_pk_bf16_f32 v27, v166, v167\n"
        "s_nop 0\n"
        "buffer_store_dwordx4 v[24:27], v1, s[8:11], s34 offen offset:2048 sc1\n"
        "v_mul_f32 v16, v176, v176\n"
        "v_mul_f32 v17, v177, v177\n"
        "v_fma_f32 v16, v16, v158, v156\n"
        "v_fma_f32 v17, v17, v158, v156\n"
        "s_nop 0\n"
        "s_waitcnt lgkmcnt(7)\n"
        "v_mfma_f32_32x32x16_bf16 v[192:207], v[244:247], v[76:79], v[192:207]\n"
        "ds_read_b128 v[228:231], v1 offset:49152\n"
        "v_mul_f32 v16, v176, v16\n"
        "v_mul_f32 v17, v177, v17\n"
        "v_exp_f32 v16, v16\n"
        "v_exp_f32 v17, v17\n"
        "v_fma_f32 v16, v16, v154, v154\n"
        "s_nop 0\n"
        "v_fma_f32 v17, v17, v154, v154\n"
        "s_waitcnt lgkmcnt(7)\n"
        "v_mfma_f32_32x32x16_bf16 v[192:207], v[248:251], v[80:83], v[192:207]\n"
        "ds_read_b128 v[232:235], v1 offset:50176\n"
        "v_rcp_f32 v16, v16\n"
        "v_rcp_f32 v17, v17\n"
        "v_mul_f32 v176, v176, v16\n"
        "v_mul_f32 v177, v177, v17\n"
        "v_cvt_pk_bf16_f32 v24, v176, v177\n"
        "s_nop 0\n"
        "v_mul_f32 v16, v178, v178\n"
        "v_mul_f32 v17, v179, v179\n"
        "s_waitcnt lgkmcnt(7)\n"
        "v_mfma_f32_32x32x16_bf16 v[192:207], v[252:255], v[84:87], v[192:207]\n"
        "ds_read_b128 v[236:239], v1 offset:51200\n"
        "v_fma_f32 v16, v16, v158, v156\n"
        "v_fma_f32 v17, v17, v158, v156\n"
        "v_mul_f32 v16, v178, v16\n"
        "s_nop 0\n"
        "v_mul_f32 v17, v179, v17\n"
        "v_exp_f32 v16, v16\n"
        "v_exp_f32 v17, v17\n"
        "v_fma_f32 v16, v16, v154, v154\n"
        "s_waitcnt lgkmcnt(7)\n"
        "v_mfma_f32_32x32x16_bf16 v[192:207], v[208:211], v[88:91], v[192:207]\n"
        "ds_read_b128 v[240:243], v1 offset:52224\n"
        "v_fma_f32 v17, v17, v154, v154\n"
        "v_rcp_f32 v16, v16\n"
        "s_nop 0\n"
        "v_rcp_f32 v17, v17\n"
        "v_mul_f32 v178, v178, v16\n"
        "v_mul_f32 v179, v179, v17\n"
        "v_cvt_pk_bf16_f32 v25, v178, v179\n"
        "v_mul_f32 v16, v180, v180\n"
        "s_waitcnt lgkmcnt(7)\n"
        "v_mfma_f32_32x32x16_bf16 v[192:207], v[212:215], v[92:95], v[192:207]\n"
        "ds_read_b128 v[244:247], v1 offset:53248\n"
        "s_nop 0\n"
        "v_mul_f32 v17, v181, v181\n"
        "v_fma_f32 v16, v16, v158, v156\n"
        "v_fma_f32 v17, v17, v158, v156\n"
        "v_mul_f32 v16, v180, v16\n"
        "v_mul_f32 v17, v181, v17\n"
        "s_nop 0\n"
        "s_waitcnt lgkmcnt(7)\n"
        "v_mfma_f32_32x32x16_bf16 v[192:207], v[216:219], v[96:99], v[192:207]\n"
        "ds_read_b128 v[248:251], v1 offset:54272\n"
        "v_exp_f32 v16, v16\n"
        "v_exp_f32 v17, v17\n"
        "v_fma_f32 v16, v16, v154, v154\n"
        "v_fma_f32 v17, v17, v154, v154\n"
        "v_rcp_f32 v16, v16\n"
        "v_rcp_f32 v17, v17\n"
        "s_nop 0\n"
        "v_mul_f32 v180, v180, v16\n"
        "s_waitcnt lgkmcnt(7)\n"
        "v_mfma_f32_32x32x16_bf16 v[192:207], v[220:223], v[100:103], v[192:207]\n"
        "ds_read_b128 v[252:255], v1 offset:55296\n"
        "v_mul_f32 v181, v181, v17\n"
        "v_cvt_pk_bf16_f32 v26, v180, v181\n"
        "v_mul_f32 v16, v182, v182\n"
        "v_mul_f32 v17, v183, v183\n"
        "s_nop 0\n"
        "v_fma_f32 v16, v16, v158, v156\n"
        "v_fma_f32 v17, v17, v158, v156\n"
        "v_mul_f32 v16, v182, v16\n"
        "s_waitcnt lgkmcnt(7)\n"
        "v_mfma_f32_32x32x16_bf16 v[192:207], v[224:227], v[104:107], v[192:207]\n"
        "ds_read_b128 v[208:211], v1 offset:56320\n"
        "v_mul_f32 v17, v183, v17\n"
        "v_exp_f32 v16, v16\n"
        "v_exp_f32 v17, v17\n"
        "s_nop 0\n"
        "v_fma_f32 v16, v16, v154, v154\n"
        "v_fma_f32 v17, v17, v154, v154\n"
        "v_rcp_f32 v16, v16\n"
        "s_waitcnt lgkmcnt(7)\n"
        "v_mfma_f32_32x32x16_bf16 v[192:207], v[228:231], v[108:111], v[192:207]\n"
        "ds_read_b128 v[212:215], v1 offset:57344\n"
        "v_rcp_f32 v17, v17\n"
        "v_mul_f32 v182, v182, v16\n"
        "s_nop 0\n"
        "v_mul_f32 v183, v183, v17\n"
        "v_cvt_pk_bf16_f32 v27, v182, v183\n"
        "buffer_store_dwordx4 v[24:27], v1, s[8:11], s33 offen offset:0 sc1\n"
        "v_mul_f32 v16, v184, v184\n"
        "v_mul_f32 v17, v185, v185\n"
        "s_waitcnt lgkmcnt(7)\n"
        "v_mfma_f32_32x32x16_bf16 v[192:207], v[232:235], v[112:115], v[192:207]\n"
        "ds_read_b128 v[216:219], v1 offset:58368\n"
        "s_nop 0\n"
        "v_fma_f32 v16, v16, v158, v156\n"
        "v_fma_f32 v17, v17, v158, v156\n"
        "v_mul_f32 v16, v184, v16\n"
        "v_mul_f32 v17, v185, v17\n"
        "v_exp_f32 v16, v16\n"
        "v_exp_f32 v17, v17\n"
        "s_nop 0\n"
        "s_waitcnt lgkmcnt(7)\n"
        "v_mfma_f32_32x32x16_bf16 v[192:207], v[236:239], v[116:119], v[192:207]\n"
        "ds_read_b128 v[220:223], v1 offset:59392\n"
        "v_fma_f32 v16, v16, v154, v154\n"
        "v_fma_f32 v17, v17, v154, v154\n"
        "v_rcp_f32 v16, v16\n"
        "v_rcp_f32 v17, v17\n"
        "v_mul_f32 v184, v184, v16\n"
        "s_nop 0\n"
        "v_mul_f32 v185, v185, v17\n"
        "v_cvt_pk_bf16_f32 v28, v184, v185\n"
        "s_waitcnt lgkmcnt(7)\n"
        "v_mfma_f32_32x32x16_bf16 v[192:207], v[240:243], v[120:123], v[192:207]\n"
        "ds_read_b128 v[224:227], v1 offset:60416\n"
        "v_mul_f32 v16, v186, v186\n"
        "v_mul_f32 v17, v187, v187\n"
        "v_fma_f32 v16, v16, v158, v156\n"
        "s_nop 0\n"
        "v_fma_f32 v17, v17, v158, v156\n"
        "v_mul_f32 v16, v186, v16\n"
        "v_mul_f32 v17, v187, v17\n"
        "s_waitcnt lgkmcnt(7)\n"
        "v_mfma_f32_32x32x16_bf16 v[192:207], v[244:247], v[124:127], v[192:207]\n"
        "ds_read_b128 v[228:231], v1 offset:61440\n"
        "v_exp_f32 v16, v16\n"
        "v_exp_f32 v17, v17\n"
        "v_fma_f32 v16, v16, v154, v154\n"
        "s_nop 0\n"
        "v_fma_f32 v17, v17, v154, v154\n"
        "v_rcp_f32 v16, v16\n"
        "v_rcp_f32 v17, v17\n"
        "v_mul_f32 v186, v186, v16\n"
        "s_waitcnt lgkmcnt(7)\n"
        "v_mfma_f32_32x32x16_bf16 v[192:207], v[248:251], v[128:131], v[192:207]\n"
        "ds_read_b128 v[232:235], v1 offset:62464\n"
        "v_mul_f32 v187, v187, v17\n"
        "s_nop 0\n"
        "v_cvt_pk_bf16_f32 v29, v186, v187\n"
        "v_mul_f32 v16, v188, v188\n"
        "v_mul_f32 v17, v189, v189\n"
        "v_fma_f32 v16, v16, v158, v156\n"
        "v_fma_f32 v17, v17, v158, v156\n"
        "v_mul_f32 v16, v188, v16\n"
        "s_waitcnt lgkmcnt(7)\n"
        "v_mfma_f32_32x32x16_bf16 v[192:207], v[252:255], v[132:135], v[192:207]\n"
        "ds_read_b128 v[236:239], v1 offset:63488\n"
        "s_nop 0\n"
        "v_mul_f32 v17, v189, v17\n"
        "v_exp_f32 v16, v16\n"
        "v_exp_f32 v17, v17\n"
        "v_fma_f32 v16, v16, v154, v154\n"
        "v_fma_f32 v17, v17, v154, v154\n"
        "s_nop 0\n"
        "s_waitcnt lgkmcnt(7)\n"
        "v_mfma_f32_32x32x16_bf16 v[192:207], v[208:211], v[136:139], v[192:207]\n"
        "ds_read_b128 v[240:243], v1 offset:64512\n"
        "v_rcp_f32 v16, v16\n"
        "v_rcp_f32 v17, v17\n"
        "v_mul_f32 v188, v188, v16\n"
        "v_mul_f32 v189, v189, v17\n"
        "v_cvt_pk_bf16_f32 v30, v188, v189\n"
        "s_nop 0\n"
        "v_mul_f32 v16, v190, v190\n"
        "v_mul_f32 v17, v191, v191\n"
        "s_waitcnt lgkmcnt(7)\n"
        "v_mfma_f32_32x32x16_bf16 v[192:207], v[212:215], v[140:143], v[192:207]\n"
        "ds_read_b128 v[244:247], v2 offset:0\n"
        "v_fma_f32 v16, v16, v158, v156\n"
        "v_fma_f32 v17, v17, v158, v156\n"
        "v_mul_f32 v16, v190, v16\n"
        "v_mul_f32 v17, v191, v17\n"
        "s_nop 0\n"
        "v_exp_f32 v16, v16\n"
        "v_exp_f32 v17, v17\n"
        "v_fma_f32 v16, v16, v154, v154\n"
        "s_waitcnt lgkmcnt(7)\n"
        "v_mfma_f32_32x32x16_bf16 v[192:207], v[216:219], v[148:151], v[192:207]\n"
        "ds_read_b128 v[248:251], v2 offset:1024\n"
        "v_fma_f32 v17, v17, v154, v154\n"
        "v_rcp_f32 v16, v16\n"
        "s_nop 0\n"
        "v_rcp_f32 v17, v17\n"
        "v_mul_f32 v190, v190, v16\n"
        "v_mul_f32 v191, v191, v17\n"
        "v_cvt_pk_bf16_f32 v31, v190, v191\n"
        "buffer_store_dwordx4 v[28:31], v1, s[8:11], s33 offen offset:1024 sc1\n"
        "s_waitcnt lgkmcnt(7)\n"
        "v_mfma_f32_32x32x16_bf16 v[160:175], v[220:223], v[32:35], 0\n"
        "ds_read_b128 v[252:255], v2 offset:2048\n"
        "s_waitcnt lgkmcnt(7)\n"
        "v_mfma_f32_32x32x16_bf16 v[160:175], v[224:227], v[36:39], v[160:175]\n"
        "ds_read_b128 v[208:211], v2 offset:3072\n"
        "s_waitcnt lgkmcnt(7)\n"
        "v_mfma_f32_32x32x16_bf16 v[160:175], v[228:231], v[40:43], v[160:175]\n"
        "ds_read_b128 v[212:215], v2 offset:4096\n"
        "v_mul_f32 v16, v192, v192\n"
        "v_mul_f32 v17, v193, v193\n"
        "v_fma_f32 v16, v16, v158, v156\n"
        "v_fma_f32 v17, v17, v158, v156\n"
        "v_mul_f32 v16, v192, v16\n"
        "s_waitcnt lgkmcnt(7)\n"
        "v_mfma_f32_32x32x16_bf16 v[160:175], v[232:235], v[44:47], v[160:175]\n"
        "ds_read_b128 v[216:219], v2 offset:5120\n"
        "s_nop 0\n"
        "v_mul_f32 v17, v193, v17\n"
        "v_exp_f32 v16, v16\n"
        "v_exp_f32 v17, v17\n"
        "v_fma_f32 v16, v16, v154, v154\n"
        "s_waitcnt lgkmcnt(7)\n"
        "v_mfma_f32_32x32x16_bf16 v[160:175], v[236:239], v[48:51], v[160:175]\n"
        "ds_read_b128 v[220:223], v2 offset:6144\n"
        "v_fma_f32 v17, v17, v154, v154\n"
        "s_nop 0\n"
        "v_rcp_f32 v16, v16\n"
        "v_rcp_f32 v17, v17\n"
        "v_mul_f32 v192, v192, v16\n"
        "v_mul_f32 v193, v193, v17\n"
        "s_waitcnt lgkmcnt(7)\n"
        "v_mfma_f32_32x32x16_bf16 v[160:175], v[240:243], v[52:55], v[160:175]\n"
        "ds_read_b128 v[224:227], v2 offset:7168\n"
        "v_cvt_pk_bf16_f32 v24, v192, v193\n"
        "s_nop 0\n"
        "v_mul_f32 v16, v194, v194\n"
        "v_mul_f32 v17, v195, v195\n"
        "v_fma_f32 v16, v16, v158, v156\n"
        "s_waitcnt lgkmcnt(7)\n"
        "v_mfma_f32_32x32x16_bf16 v[160:175], v[244:247], v[56:59], v[160:175]\n"
        "ds_read_b128 v[228:231], v2 offset:8192\n"
        "v_fma_f32 v17, v17, v158, v156\n"
        "s_nop 0\n"
        "v_mul_f32 v16, v194, v16\n"
        "v_mul_f32 v17, v195, v17\n"
        "v_exp_f32 v16, v16\n"
        "v_exp_f32 v17, v17\n"
        "s_waitcnt lgkmcnt(7)\n"
        "v_mfma_f32_32x32x16_bf16 v[160:175], v[248:251], v[60:63], v[160:175]\n"
        "ds_read_b128 v[232:235], v2 offset:9216\n"
        "v_fma_f32 v16, v16, v154, v154\n"
        "s_nop 0\n"
        "v_fma_f32 v17, v17, v154, v154\n"
        "v_rcp_f32 v16, v16\n"
        "v_rcp_f32 v17, v17\n"
        "s_waitcnt lgkmcnt(7)\n"
        "v_mfma_f32_32x32x16_bf16 v[160:175], v[252:255], v[64:67], v[160:175]\n"
        "ds_read_b128 v[236:239], v2 offset:10240\n"
        "v_mul_f32 v194, v194, v16\n"
        "v_mul_f32 v195, v195, v17\n"
        "s_nop 0\n"
        "v_cvt_pk_bf16_f32 v25, v194, v195\n"
        "v_mul_f32 v16, v196, v196\n"
        "v_mul_f32 v17, v197, v197\n"
        "s_waitcnt lgkmcnt(7)\n"
        "v_mfma_f32_32x32x16_bf16 v[160:175], v[208:211], v[68:71], v[160:175]\n"
        "ds_read_b128 v[240:243], v2 offset:11264\n"
        "v_fma_f32 v16, v16, v158, v156\n"
        "s_nop 0\n"
        "v_fma_f32 v17, v17, v158, v156\n"
        "v_mul_f32 v16, v196, v16\n"
        "v_mul_f32 v17, v197, v17\n"
        "s_waitcnt lgkmcnt(7)\n"
        "v_mfma_f32_32x32x16_bf16 v[160:175], v[212:215], v[72:75], v[160:175]\n"
        "ds_read_b128 v[244:247], v2 offset:12288\n"
        "v_exp_f32 v16, v16\n"
        "v_exp_f32 v17, v17\n"
        "s_nop 0\n"
        "v_fma_f32 v16, v16, v154, v154\n"
        "v_fma_f32 v17, v17, v154, v154\n"
        "v_rcp_f32 v16, v16\n"
        "s_waitcnt lgkmcnt(7)\n"
        "v_mfma_f32_32x32x16_bf16 v[160:175], v[216:219], v[76:79], v[160:175]\n"
        "ds_read_b128 v[248:251], v2 offset:13312\n"
        "v_rcp_f32 v17, v17\n"
        "v_mul_f32 v196, v196, v16\n"
        "s_nop 0\n"
        "v_mul_f32 v197, v197, v17\n"
        "v_cvt_pk_bf16_f32 v26, v196, v197\n"
        "s_waitcnt lgkmcnt(7)\n"
        "v_mfma_f32_32x32x16_bf16 v[160:175], v[220:223], v[80:83], v[160:175]\n"
        "ds_read_b128 v[252:255], v2 offset:14336\n"
        "v_mul_f32 v16, v198, v198\n"
        "v_mul_f32 v17, v199, v199\n"
        "s_nop 0\n"
        "v_fma_f32 v16, v16, v158, v156\n"
        "v_fma_f32 v17, v17, v158, v156\n"
        "s_waitcnt lgkmcnt(7)\n"
        "v_mfma_f32_32x32x16_bf16 v[160:175], v[224:227], v[84:87], v[160:175]\n"
        "ds_read_b128 v[208:211], v2 offset:15360\n"
        "v_mul_f32 v16, v198, v16\n"
        "v_mul_f32 v17, v199, v17\n"
        "v_exp_f32 v16, v16\n"
        "s_nop 0\n"
        "v_exp_f32 v17, v17\n"
        "v_fma_f32 v16, v16, v154, v154\n"
        "s_waitcnt lgkmcnt(7)\n"
        "v_mfma_f32_32x32x16_bf16 v[160:175], v[228:231], v[88:91], v[160:175]\n"
        "ds_read_b128 v[212:215], v2 offset:16384\n"
        "v_fma_f32 v17, v17, v154, v154\n"
        "v_rcp_f32 v16, v16\n"
        "v_rcp_f32 v17, v17\n"
        "s_nop 0\n"
        "v_mul_f32 v198, v198, v16\n"
        "s_waitcnt lgkmcnt(7)\n"
        "v_mfma_f32_32x32x16_bf16 v[160:175], v[232:235], v[92:95], v[160:175]\n"
        "ds_read_b128 v[216:219], v2 offset:17408\n"
        "v_mul_f32 v199, v199, v17\n"
        "v_cvt_pk_bf16_f32 v27, v198, v199\n"
        "buffer_store_dwordx4 v[24:27], v1, s[8:11], s33 offen offset:2048 sc1\n"
        "s_nop 0\n"
        "v_mul_f32 v16, v200, v200\n"
        "v_mul_f32 v17, v201, v201\n"
        "s_waitcnt lgkmcnt(7)\n"
        "v_mfma_f32_32x32x16_bf16 v[160:175], v[236:239], v[96:99], v[160:175]\n"
        "ds_read_b128 v[220:223], v2 offset:18432\n"
        "v_fma_f32 v16, v16, v158, v156\n"
        "v_fma_f32 v17, v17, v158, v156\n"
        "v_mul_f32 v16, v200, v16\n"
        "s_nop 0\n"
        "v_mul_f32 v17, v201, v17\n"
        "s_waitcnt lgkmcnt(7)\n"
        "v_mfma_f32_32x32x16_bf16 v[160:175], v[240:243], v[100:103], v[160:175]\n"
        "ds_read_b128 v[224:227], v2 offset:19456\n"
        "v_exp_f32 v16, v16\n"
        "v_exp_f32 v17, v17\n"
        "v_fma_f32 v16, v16, v154, v154\n"
        "v_fma_f32 v17, v17, v154, v154\n"
        "s_nop 0\n"
        "v_rcp_f32 v16, v16\n"
        "s_waitcnt lgkmcnt(7)\n"
        "v_mfma_f32_32x32x16_bf16 v[160:175], v[244:247], v[104:107], v[160:175]\n"
        "ds_read_b128 v[228:231], v2 offset:20480\n"
        "v_rcp_f32 v17, v17\n"
        "v_mul_f32 v200, v200, v16\n"
        "v_mul_f32 v201, v201, v17\n"
        "v_cvt_pk_bf16_f32 v28, v200, v201\n"
        "s_nop 0\n"
        "s_waitcnt lgkmcnt(7)\n"
        "v_mfma_f32_32x32x16_bf16 v[160:175], v[248:251], v[108:111], v[160:175]\n"
        "ds_read_b128 v[232:235], v2 offset:21504\n"
        "v_mul_f32 v16, v202, v202\n"
        "v_mul_f32 v17, v203, v203\n"
        "v_fma_f32 v16, v16, v158, v156\n"
        "v_fma_f32 v17, v17, v158, v156\n"
        "s_nop 0\n"
        "v_mul_f32 v16, v202, v16\n"
        "s_waitcnt lgkmcnt(7)\n"
        "v_mfma_f32_32x32x16_bf16 v[160:175], v[252:255], v[112:115], v[160:175]\n"
        "ds_read_b128 v[236:239], v2 offset:22528\n"
        "v_mul_f32 v17, v203, v17\n"
        "v_exp_f32 v16, v16\n"
        "v_exp_f32 v17, v17\n"
        "v_fma_f32 v16, v16, v154, v154\n"
        "s_nop 0\n"
        "s_waitcnt lgkmcnt(7)\n"
        "v_mfma_f32_32x32x16_bf16 v[160:175], v[208:211], v[116:119], v[160:175]\n"
        "v_fma_f32 v17, v17, v154, v154\n"
        "v_rcp_f32 v16, v16\n"
        "v_rcp_f32 v17, v17\n"
        "v_mul_f32 v202, v202, v16\n"
        "v_mul_f32 v203, v203, v17\n"
        "s_waitcnt lgkmcnt(6)\n"
        "v_mfma_f32_32x32x16_bf16 v[160:175], v[212:215], v[120:123], v[160:175]\n"
        "s_nop 0\n"
        "v_cvt_pk_bf16_f32 v29, v202, v203\n"
        "v_mul_f32 v16, v204, v204\n"
        "v_mul_f32 v17, v205, v205\n"
        "v_fma_f32 v16, v16, v158, v156\n"
        "s_nop 0\n"
        "s_waitcnt lgkmcnt(5)\n"
        "v_mfma_f32_32x32x16_bf16 v[160:175], v[216:219], v[124:127], v[160:175]\n"
        "v_fma_f32 v17, v17, v158, v156\n"
        "v_mul_f32 v16, v204, v16\n"
        "v_mul_f32 v17, v205, v17\n"
        "v_exp_f32 v16, v16\n"
        "v_exp_f32 v17, v17\n"
        "s_waitcnt lgkmcnt(4)\n"
        "v_mfma_f32_32x32x16_bf16 v[160:175], v[220:223], v[128:131], v[160:175]\n"
        "s_nop 0\n"
        "v_fma_f32 v16, v16, v154, v154\n"
        "v_fma_f32 v17, v17, v154, v154\n"
        "v_rcp_f32 v16, v16\n"
        "v_rcp_f32 v17, v17\n"
        "v_mul_f32 v204, v204, v16\n"
        "s_waitcnt lgkmcnt(3)\n"
        "v_mfma_f32_32x32x16_bf16 v[160:175], v[224:227], v[132:135], v[160:175]\n"
        "s_nop 0\n"
        "v_mul_f32 v205, v205, v17\n"
        "v_cvt_pk_bf16_f32 v30, v204, v205\n"
        "v_mul_f32 v16, v206, v206\n"
        "v_mul_f32 v17, v207, v207\n"
        "s_waitcnt lgkmcnt(2)\n"
        "v_mfma_f32_32x32x16_bf16 v[160:175], v[228:231], v[136:139], v[160:175]\n"
        "s_nop 0\n"
        "v_fma_f32 v16, v16, v158, v156\n"
        "v_fma_f32 v17, v17, v158, v156\n"
        "v_mul_f32 v16, v206, v16\n"
        "v_mul_f32 v17, v207, v17\n"
        "v_exp_f32 v16, v16\n"
        "s_waitcnt lgkmcnt(1)\n"
        "v_mfma_f32_32x32x16_bf16 v[160:175], v[232:235], v[140:143], v[160:175]\n"
        "s_nop 0\n"
        "v_exp_f32 v17, v17\n"
        "v_fma_f32 v16, v16, v154, v154\n"
        "v_fma_f32 v17, v17, v154, v154\n"
        "v_rcp_f32 v16, v16\n"
        "s_waitcnt lgkmcnt(0)\n"
        "v_mfma_f32_32x32x16_bf16 v[160:175], v[236:239], v[148:151], v[160:175]\n"
        "v_rcp_f32 v17, v17\n"
        "s_nop 0\n"
        "v_mul_f32 v206, v206, v16\n"
        "v_mul_f32 v207, v207, v17\n"
        "v_cvt_pk_bf16_f32 v31, v206, v207\n"
        "buffer_store_dwordx4 v[28:31], v1, s[8:11], s33 offen offset:3072 sc1\n"
        "v_mov_b32 v20, s96\n"
        "v_mov_b32 v21, s97\n"
        "v_mov_b32 v22, s96\n"
        "v_mov_b32 v23, s97\n"
        "s_waitcnt vmcnt(1)\n"
        "s_mov_b64 exec, 0xff\n"
        "buffer_store_dwordx4 v[20:23], v1, s[8:11], s35 offen sc1\n"
        "s_mov_b64 exec, -1\n"
        "v_pk_mul_f32 v[16:17], v[160:161], v[160:161]\n"
        "v_pk_mul_f32 v[18:19], v[162:163], v[162:163]\n"
        "v_pk_fma_f32 v[16:17], v[16:17], v[158:159], v[156:157] op_sel_hi:[1,0,0]\n"
        "v_pk_fma_f32 v[18:19], v[18:19], v[158:159], v[156:157] op_sel_hi:[1,0,0]\n"
        "v_pk_mul_f32 v[16:17], v[160:161], v[16:17]\n"
        "v_pk_mul_f32 v[18:19], v[162:163], v[18:19]\n"
        "v_exp_f32 v16, v16\n"
        "v_exp_f32 v17, v17\n"
        "v_exp_f32 v18, v18\n"
        "v_exp_f32 v19, v19\n"
        "v_pk_fma_f32 v[16:17], v[16:17], v[154:155], v[154:155] op_sel_hi:[1,0,0]\n"
        "v_pk_fma_f32 v[18:19], v[18:19], v[154:155], v[154:155] op_sel_hi:[1,0,0]\n"
        "v_rcp_f32 v16, v16\n"
        "v_rcp_f32 v17, v17\n"
        "v_rcp_f32 v18, v18\n"
        "v_rcp_f32 v19, v19\n"
        "v_pk_mul_f32 v[160:161], v[160:161], v[16:17]\n"
        "v_pk_mul_f32 v[162:163], v[162:163], v[18:19]\n"
        "v_cvt_pk_bf16_f32 v24, v160, v161\n"
        "v_cvt_pk_bf16_f32 v25, v162, v163\n"
        "v_pk_mul_f32 v[16:17], v[164:165], v[164:165]\n"
        "v_pk_mul_f32 v[18:19], v[166:167], v[166:167]\n"
        "v_pk_fma_f32 v[16:17], v[16:17], v[158:159], v[156:157] op_sel_hi:[1,0,0]\n"
        "v_pk_fma_f32 v[18:19], v[18:19], v[158:159], v[156:157] op_sel_hi:[1,0,0]\n"
        "v_pk_mul_f32 v[16:17], v[164:165], v[16:17]\n"
        "v_pk_mul_f32 v[18:19], v[166:167], v[18:19]\n"
        "v_exp_f32 v16, v16\n"
        "v_exp_f32 v17, v17\n"
        "v_exp_f32 v18, v18\n"
        "v_exp_f32 v19, v19\n"
        "v_pk_fma_f32 v[16:17], v[16:17], v[154:155], v[154:155] op_sel_hi:[1,0,0]\n"
        "v_pk_fma_f32 v[18:19], v[18:19], v[154:155], v[154:155] op_sel_hi:[1,0,0]\n"
        "v_rcp_f32 v16, v16\n"
        "v_rcp_f32 v17, v17\n"
        "v_rcp_f32 v18, v18\n"
        "v_rcp_f32 v19, v19\n"
        "v_pk_mul_f32 v[164:165], v[164:165], v[16:17]\n"
        "v_pk_mul_f32 v[166:167], v[166:167], v[18:19]\n"
        "v_cvt_pk_bf16_f32 v26, v164, v165\n"
        "v_cvt_pk_bf16_f32 v27, v166, v167\n"
        "buffer_store_dwordx4 v[24:27], v1, s[8:11], s34 offen offset:0 sc1\n"
        "v_pk_mul_f32 v[16:17], v[168:169], v[168:169]\n"
        "v_pk_mul_f32 v[18:19], v[170:171], v[170:171]\n"
        "v_pk_fma_f32 v[16:17], v[16:17], v[158:159], v[156:157] op_sel_hi:[1,0,0]\n"
        "v_pk_fma_f32 v[18:19], v[18:19], v[158:159], v[156:157] op_sel_hi:[1,0,0]\n"
        "v_pk_mul_f32 v[16:17], v[168:169], v[16:17]\n"
        "v_pk_mul_f32 v[18:19], v[170:171], v[18:19]\n"
        "v_exp_f32 v16, v16\n"
        "v_exp_f32 v17, v17\n"
        "v_exp_f32 v18, v18\n"
        "v_exp_f32 v19, v19\n"
        "v_pk_fma_f32 v[16:17], v[16:17], v[154:155], v[154:155] op_sel_hi:[1,0,0]\n"
        "v_pk_fma_f32 v[18:19], v[18:19], v[154:155], v[154:155] op_sel_hi:[1,0,0]\n"
        "v_rcp_f32 v16, v16\n"
        "v_rcp_f32 v17, v17\n"
        "v_rcp_f32 v18, v18\n"
        "v_rcp_f32 v19, v19\n"
        "v_pk_mul_f32 v[168:169], v[168:169], v[16:17]\n"
        "v_pk_mul_f32 v[170:171], v[170:171], v[18:19]\n"
        "v_cvt_pk_bf16_f32 v28, v168, v169\n"
        "v_cvt_pk_bf16_f32 v29, v170, v171\n"
        "v_pk_mul_f32 v[16:17], v[172:173], v[172:173]\n"
        "v_pk_mul_f32 v[18:19], v[174:175], v[174:175]\n"
        "v_pk_fma_f32 v[16:17], v[16:17], v[158:159], v[156:157] op_sel_hi:[1,0,0]\n"
        "v_pk_fma_f32 v[18:19], v[18:19], v[158:159], v[156:157] op_sel_hi:[1,0,0]\n"
        "v_pk_mul_f32 v[16:17], v[172:173], v[16:17]\n"
        "v_pk_mul_f32 v[18:19], v[174:175], v[18:19]\n"
        "v_exp_f32 v16, v16\n"
        "v_exp_f32 v17, v17\n"
        "v_exp_f32 v18, v18\n"
        "v_exp_f32 v19, v19\n"
        "v_pk_fma_f32 v[16:17], v[16:17], v[154:155], v[154:155] op_sel_hi:[1,0,0]\n"
        "v_pk_fma_f32 v[18:19], v[18:19], v[154:155], v[154:155] op_sel_hi:[1,0,0]\n"
        "v_rcp_f32 v16, v16\n"
        "v_rcp_f32 v17, v17\n"
        "v_rcp_f32 v18, v18\n"
        "v_rcp_f32 v19, v19\n"
        "v_pk_mul_f32 v[172:173], v[172:173], v[16:17]\n"
        "v_pk_mul_f32 v[174:175], v[174:175], v[18:19]\n"
        "v_cvt_pk_bf16_f32 v30, v172, v173\n"
        "v_cvt_pk_bf16_f32 v31, v174, v175\n"
        "buffer_store_dwordx4 v[28:31], v1, s[8:11], s34 offen offset:1024 sc1\n"
        "s_add_u32 s51, s35, 131072\n"
        "s_barrier\n"
        "s_cmp_ge_u32 s26, 14\n"
        "s_cbranch_scc1 .Lffn2_idle\n"
        "s_mul_i32 s30, s45, 2048\n"
        "s_mul_i32 s39, s24, 512\n"
        "s_add_u32 s30, s30, s39\n"
        "s_add_u32 s30, s30, s29\n"
        "s_add_u32 s30, s30, 395264\n"
        "s_mul_i32 s31, s24, 28672\n"
        "s_add_u32 s31, s31, s54\n"
        "s_add_u32 s31, s31, 65536\n"
        "s_bitcmp1_b32 s48, 0\n"
        "s_cselect_b32 s32, -425984, 0\n"
        "s_add_u32 s32, s32, 212992\n"
        "s_add_u32 s32, s32, s31\n"
        "s_lshl_b32 s33, s49, 6\n"
        "v_lshrrev_b32 v18, 6, v1\n"
        "v_and_b32 v18, 8, v18\n"
        "buffer_load_dwordx2 v[216:217], v18, s[16:19], s33 offen offset:0\n"
        "buffer_load_dwordx2 v[218:219], v18, s[16:19], s33 offen offset:16\n"
        "buffer_load_dwordx2 v[220:221], v18, s[16:19], s33 offen offset:32\n"
        "buffer_load_dwordx2 v[222:223], v18, s[16:19], s33 offen offset:48\n"
        "s_xor_b32 s35, s24, 1\n"
        "s_mul_i32 s35, s35, 12288\n"
        "v_add_u32 v25, s35, v2\n"
        "s_xor_b32 s35, s24, 2\n"
        "s_mul_i32 s35, s35, 12288\n"
        "v_add_u32 v26, s35, v2\n"
        "s_xor_b32 s35, s24, 3\n"
        "s_mul_i32 s35, s35, 12288\n"
        "v_add_u32 v27, s35, v2\n"
        "s_mul_i32 s35, s24, 12288\n"
        "v_add_u32 v24, s35, v2\n"
        "s_mul_i32 s34, s49, 2048\n"
        "s_add_u32 s34, s34, s54\n"
        "s_add_u32 s34, s34, 32768\n"
        "s_cmp_lg_u32 s28, 0\n"
        "s_cbranch_scc1 .Lh1\n"
        "s_mov_b32 s44, 0x20000\n"
        "s_sleep 7\n"
        "buffer_load_dwordx2 v[16:17], v6, s[8:11], s30 offen sc1\n"
        "s_mul_i32 s38, s24, 22528\n"
        "s_min_u32 s38, s38, 66560\n"
        "s_add_u32 m0, s38, 0\n"
        "s_mul_i32 s52, s26, 89088\n"
        "s_add_u32 s52, s52, s38\n"
        "s_add_u32 s52, s52, 1574912\n"
        "buffer_load_dwordx4 v1, s[8:11], s52 offen offset:0 sc1 lds\n"
        "buffer_load_dwordx4 v1, s[8:11], s52 offen offset:1024 sc1 lds\n"
        "buffer_load_dwordx4 v1, s[8:11], s52 offen offset:2048 sc1 lds\n"
        "buffer_load_dwordx4 v1, s[8:11], s52 offen offset:3072 sc1 lds\n"
        "s_add_u32 m0, m0, 4096\n"
        "s_add_u32 s52, s52, 4096\n"
        "buffer_load_dwordx4 v1, s[8:11], s52 offen offset:0 sc1 lds\n"
        "buffer_load_dwordx4 v1, s[8:11], s52 offen offset:1024 sc1 lds\n"
        "buffer_load_dwordx4 v1, s[8:11], s52 offen offset:2048 sc1 lds\n"
        "buffer_load_dwordx4 v1, s[8:11], s52 offen offset:3072 sc1 lds\n"
        "s_add_u32 m0, m0, 4096\n"
        "s_add_u32 s52, s52, 4096\n"
        "buffer_load_dwordx4 v1, s[8:11], s52 offen offset:0 sc1 lds\n"
        "buffer_load_dwordx4 v1, s[8:11], s52 offen offset:1024 sc1 lds\n"
        "buffer_load_dwordx4 v1, s[8:11], s52 offen offset:2048 sc1 lds\n"
        "s_waitcnt vmcnt(11)\n"
        "v_cmp_ne_u64 vcc, s[96:97], v[16:17]\n"
        "s_cbranch_vccz .Lpolled10\n"
        ".Lpoll9:\n"
        "buffer_load_dwordx2 v[16:17], v6, s[8:11], s30 offen sc1\n"
        "s_waitcnt vmcnt(0)\n"
        "v_cmp_ne_u64 vcc, s[96:97], v[16:17]\n"
        "s_cbranch_vccz .Lpolled10\n"
        "s_sub_u32 s44, s44, 1\n"
        "s_cbranch_scc0 .Lpoll9\n"
        ".Lpolled10:\n"
        "s_branch .Lh2\n"
        ".Lh1:\n"
        "s_mov_b32 s44, 0x20000\n"
        "s_sleep 7\n"
        ".Lpoll11:\n"
        "buffer_load_dwordx2 v[16:17], v6, s[8:11], s30 offen sc1\n"
        "s_waitcnt vmcnt(0)\n"
        "v_cmp_ne_u64 vcc, s[96:97], v[16:17]\n"
        "s_cbranch_vccz .Lpolled12\n"
        "s_sub_u32 s44, s44, 1\n"
        "s_cbranch_scc0 .Lpoll11\n"
        ".Lpolled12:\n"
        ".Lh2:\n"
        "v_mov_b32 v16, s96\n"
        "v_mov_b32 v17, s97\n"
        "v_mov_b32 v18, s96\n"
        "v_mov_b32 v19, s97\n"
        "s_mov_b64 exec, 0xff\n"
        "buffer_store_dwordx4 v[16:19], v1, s[8:11], s51 offen sc1\n"
        "s_mov_b64 exec, -1\n"
        "s_add_u32 s31, s31, 4096\n"
        "buffer_load_dwordx4 v[32:35], v1, s[8:11], s31 offen offset:2048 sc1\n"
        "s_add_u32 s32, s32, 4096\n"
        "buffer_load_dwordx4 v[36:39], v1, s[8:11], s32 offen offset:2048 sc1\n"
        "buffer_load_dwordx4 v[208:211], v1, s[8:11], s34 offen offset:0 sc1\n"
        "buffer_load_dwordx4 v[212:215], v1, s[8:11], s34 offen offset:1024 sc1\n"
        "s_add_u32 s31, s31, -4096\n"
        "buffer_load_dwordx4 v[40:43], v1, s[8:11], s31 offen offset:0 sc1\n"
        "s_add_u32 s32, s32, -4096\n"
        "buffer_load_dwordx4 v[44:47], v1, s[8:11], s32 offen offset:0 sc1\n"
        "buffer_load_dwordx4 v[48:51], v1, s[8:11], s31 offen offset:1024 sc1\n"
        "buffer_load_dwordx4 v[52:55], v1, s[8:11], s32 offen offset:1024 sc1\n"
        "buffer_load_dwordx4 v[56:59], v1, s[8:11], s31 offen offset:2048 sc1\n"
        "buffer_load_dwordx4 v[60:63], v1, s[8:11], s32 offen offset:2048 sc1\n"
        "s_add_u32 s31, s31, 12288\n"
        "buffer_load_dwordx4 v[64:67], v1, s[8:11], s31 offen offset:1024 sc1\n"
        "s_add_u32 s32, s32, 12288\n"
        "buffer_load_dwordx4 v[68:71], v1, s[8:11], s32 offen offset:1024 sc1\n"
        "s_add_u32 s31, s31, -8192\n"
        "buffer_load_dwordx4 v[72:75], v1, s[8:11], s31 offen offset:3072 sc1\n"
        "s_add_u32 s32, s32, -8192\n"
        "buffer_load_dwordx4 v[76:79], v1, s[8:11], s32 offen offset:3072 sc1\n"
        "v_lshlrev_b32 v160, 16, v216\n"
        "v_and_b32 v161, 0xffff0000, v216\n"
        "v_lshlrev_b32 v162, 16, v217\n"
        "v_and_b32 v163, 0xffff0000, v217\n"
        "v_lshlrev_b32 v164, 16, v218\n"
        "v_and_b32 v165, 0xffff0000, v218\n"
        "v_lshlrev_b32 v166, 16, v219\n"
        "v_and_b32 v167, 0xffff0000, v219\n"
        "v_lshlrev_b32 v168, 16, v220\n"
        "v_and_b32 v169, 0xffff0000, v220\n"
        "v_lshlrev_b32 v170, 16, v221\n"
        "v_and_b32 v171, 0xffff0000, v221\n"
        "v_lshlrev_b32 v172, 16, v222\n"
        "v_and_b32 v173, 0xffff0000, v222\n"
        "v_lshlrev_b32 v174, 16, v223\n"
        "v_and_b32 v175, 0xffff0000, v223\n"
        "s_waitcnt vmcnt(10)\n"
        "v_lshlrev_b32 v144, 16, v208\n"
        "v_and_b32 v145, 0xffff0000, v208\n"
        "v_lshlrev_b32 v146, 16, v209\n"
        "v_and_b32 v147, 0xffff0000, v209\n"
        "v_lshlrev_b32 v148, 16, v210\n"
        "v_and_b32 v149, 0xffff0000, v210\n"
        "v_lshlrev_b32 v150, 16, v211\n"
        "v_and_b32 v151, 0xffff0000, v211\n"
        "v_lshlrev_b32 v152, 16, v212\n"
        "v_and_b32 v153, 0xffff0000, v212\n"
        "v_lshlrev_b32 v154, 16, v213\n"
        "v_and_b32 v155, 0xffff0000, v213\n"
        "v_lshlrev_b32 v156, 16, v214\n"
        "v_and_b32 v157, 0xffff0000, v214\n"
        "v_lshlrev_b32 v158, 16, v215\n"
        "v_and_b32 v159, 0xffff0000, v215\n"
        "v_pk_add_f32 v[144:145], v[144:145], v[160:161]\n"
        "v_pk_add_f32 v[146:147], v[146:147], v[162:163]\n"
        "v_pk_add_f32 v[148:149], v[148:149], v[164:165]\n"
        "v_pk_add_f32 v[150:151], v[150:151], v[166:167]\n"
        "v_pk_add_f32 v[152:153], v[152:153], v[168:169]\n"
        "v_pk_add_f32 v[154:155], v[154:155], v[170:171]\n"
        "v_pk_add_f32 v[156:157], v[156:157], v[172:173]\n"
        "v_pk_add_f32 v[158:159], v[158:159], v[174:175]\n"
        "s_add_u32 s30, s30, 131072\n"
        "s_nop 0\n"
        "v_mfma_f32_32x32x16_bf16 v[144:159], a[24:27], v[32:35], v[144:159]\n"
        "s_add_u32 s31, s31, 4096\n"
        "buffer_load_dwordx4 v[80:83], v1, s[8:11], s31 offen offset:0 sc1\n"
        "v_mfma_f32_32x32x16_bf16 v[160:175], a[136:139], v[32:35], 0\n"
        "v_mfma_f32_32x32x16_bf16 v[176:191], a[24:27], v[36:39], 0\n"
        "s_add_u32 s32, s32, 4096\n"
        "buffer_load_dwordx4 v[84:87], v1, s[8:11], s32 offen offset:0 sc1\n"
        "v_mfma_f32_32x32x16_bf16 v[192:207], a[136:139], v[36:39], 0\n"
        "s_waitcnt vmcnt(10)\n"
        "v_mfma_f32_32x32x16_bf16 v[144:159], a[0:3], v[40:43], v[144:159]\n"
        "buffer_load_dwordx4 v[88:91], v1, s[8:11], s31 offen offset:1024 sc1\n"
        "v_mfma_f32_32x32x16_bf16 v[160:175], a[112:115], v[40:43], v[160:175]\n"
        "v_mfma_f32_32x32x16_bf16 v[176:191], a[0:3], v[44:47], v[176:191]\n"
        "buffer_load_dwordx4 v[92:95], v1, s[8:11], s32 offen offset:1024 sc1\n"
        "v_mfma_f32_32x32x16_bf16 v[192:207], a[112:115], v[44:47], v[192:207]\n"
        "s_waitcnt vmcnt(10)\n"
        "v_mfma_f32_32x32x16_bf16 v[144:159], a[4:7], v[48:51], v[144:159]\n"
        "s_add_u32 s31, s31, 12288\n"
        "buffer_load_dwordx4 v[96:99], v1, s[8:11], s31 offen offset:0 sc1\n"
        "v_mfma_f32_32x32x16_bf16 v[160:175], a[116:119], v[48:51], v[160:175]\n"
        "v_mfma_f32_32x32x16_bf16 v[176:191], a[4:7], v[52:55], v[176:191]\n"
        "s_add_u32 s32, s32, 12288\n"
        "buffer_load_dwordx4 v[100:103], v1, s[8:11], s32 offen offset:0 sc1\n"
        "v_mfma_f32_32x32x16_bf16 v[192:207], a[116:119], v[52:55], v[192:207]\n"
        "s_waitcnt vmcnt(10)\n"
        "v_mfma_f32_32x32x16_bf16 v[144:159], a[8:11], v[56:59], v[144:159]\n"
        "s_add_u32 s31, s31, -8192\n"
        "buffer_load_dwordx4 v[104:107], v1, s[8:11], s31 offen offset:2048 sc1\n"
        "v_mfma_f32_32x32x16_bf16 v[160:175], a[120:123], v[56:59], v[160:175]\n"
        "buffer_load_dwordx2 v[224:225], v6, s[8:11], s30 offen sc1\n"
        "v_mfma_f32_32x32x16_bf16 v[176:191], a[8:11], v[60:63], v[176:191]\n"
        "s_add_u32 s32, s32, -8192\n"
        "buffer_load_dwordx4 v[108:111], v1, s[8:11], s32 offen offset:2048 sc1\n"
        "v_mfma_f32_32x32x16_bf16 v[192:207], a[120:123], v[60:63], v[192:207]\n"
        "s_waitcnt vmcnt(11)\n"
        "v_mfma_f32_32x32x16_bf16 v[144:159], a[52:55], v[64:67], v[144:159]\n"
        "buffer_load_dwordx4 v[112:115], v1, s[8:11], s31 offen offset:3072 sc1\n"
        "v_mfma_f32_32x32x16_bf16 v[160:175], a[164:167], v[64:67], v[160:175]\n"
        "v_mfma_f32_32x32x16_bf16 v[176:191], a[52:55], v[68:71], v[176:191]\n"
        "buffer_load_dwordx4 v[116:119], v1, s[8:11], s32 offen offset:3072 sc1\n"
        "v_mfma_f32_32x32x16_bf16 v[192:207], a[164:167], v[68:71], v[192:207]\n"
        "s_waitcnt vmcnt(11)\n"
        "v_mfma_f32_32x32x16_bf16 v[144:159], a[28:31], v[72:75], v[144:159]\n"
        "s_add_u32 s31, s31, 4096\n"
        "buffer_load_dwordx4 v[120:123], v1, s[8:11], s31 offen offset:0 sc1\n"
        "v_mfma_f32_32x32x16_bf16 v[160:175], a[140:143], v[72:75], v[160:175]\n"
        "v_mfma_f32_32x32x16_bf16 v[176:191], a[28:31], v[76:79], v[176:191]\n"
        "s_add_u32 s32, s32, 4096\n"
        "buffer_load_dwordx4 v[124:127], v1, s[8:11], s32 offen offset:0 sc1\n"
        "v_mfma_f32_32x32x16_bf16 v[192:207], a[140:143], v[76:79], v[192:207]\n"
        "s_waitcnt vmcnt(11)\n"
        "v_mfma_f32_32x32x16_bf16 v[144:159], a[32:35], v[80:83], v[144:159]\n"
        "s_add_u32 s31, s31, 8192\n"
        "buffer_load_dwordx4 v[128:131], v1, s[8:11], s31 offen offset:3072 sc1\n"
        "v_mfma_f32_32x32x16_bf16 v[160:175], a[144:147], v[80:83], v[160:175]\n"
        "v_mfma_f32_32x32x16_bf16 v[176:191], a[32:35], v[84:87], v[176:191]\n"
        "s_add_u32 s32, s32, 8192\n"
        "buffer_load_dwordx4 v[132:135], v1, s[8:11], s32 offen offset:3072 sc1\n"
        "v_mfma_f32_32x32x16_bf16 v[192:207], a[144:147], v[84:87], v[192:207]\n"
        "s_waitcnt vmcnt(11)\n"
        "v_mfma_f32_32x32x16_bf16 v[144:159], a[36:39], v[88:91], v[144:159]\n"
        "s_add_u32 s31, s31, -4096\n"
        "buffer_load_dwordx4 v[136:139], v1, s[8:11], s31 offen offset:1024 sc1\n"
        "v_mfma_f32_32x32x16_bf16 v[160:175], a[148:151], v[88:91], v[160:175]\n"
        "v_mfma_f32_32x32x16_bf16 v[176:191], a[36:39], v[92:95], v[176:191]\n"
        "s_add_u32 s32, s32, -4096\n"
        "buffer_load_dwordx4 v[140:143], v1, s[8:11], s32 offen offset:1024 sc1\n"
        "v_mfma_f32_32x32x16_bf16 v[192:207], a[148:151], v[92:95], v[192:207]\n"
        "s_waitcnt vmcnt(11)\n"
        "v_mfma_f32_32x32x16_bf16 v[144:159], a[80:83], v[96:99], v[144:159]\n"
        "buffer_load_dwordx4 v[32:35], v1, s[8:11], s31 offen offset:2048 sc1\n"
        "v_mfma_f32_32x32x16_bf16 v[160:175], a[192:195], v[96:99], v[160:175]\n"
        "v_mfma_f32_32x32x16_bf16 v[176:191], a[80:83], v[100:103], v[176:191]\n"
        "buffer_load_dwordx4 v[36:39], v1, s[8:11], s32 offen offset:2048 sc1\n"
        "v_mfma_f32_32x32x16_bf16 v[192:207], a[192:195], v[100:103], v[192:207]\n"
        "s_waitcnt vmcnt(10)\n"
        "v_mfma_f32_32x32x16_bf16 v[144:159], a[56:59], v[104:107], v[144:159]\n"
        "buffer_load_dwordx4 v[40:43], v1, s[8:11], s31 offen offset:3072 sc1\n"
        "v_mfma_f32_32x32x16_bf16 v[160:175], a[168:171], v[104:107], v[160:175]\n"
        "v_mfma_f32_32x32x16_bf16 v[176:191], a[56:59], v[108:111], v[176:191]\n"
        "buffer_load_dwordx4 v[44:47], v1, s[8:11], s32 offen offset:3072 sc1\n"
        "v_mfma_f32_32x32x16_bf16 v[192:207], a[168:171], v[108:111], v[192:207]\n"
        "s_waitcnt vmcnt(10)\n"
        "v_cmp_ne_u64 vcc, s[96:97], v[224:225]\n"
        "s_cbranch_vccnz .Lh2poll\n"
        ".Lh2ok:\n"
        "v_mfma_f32_32x32x16_bf16 v[144:159], a[60:63], v[112:115], v[144:159]\n"
        "s_add_u32 s31, s31, -20480\n"
        "buffer_load_dwordx4 v[48:51], v1, s[8:11], s31 offen offset:3072 sc1\n"
        "v_mfma_f32_32x32x16_bf16 v[160:175], a[172:175], v[112:115], v[160:175]\n"
        "v_mfma_f32_32x32x16_bf16 v[176:191], a[60:63], v[116:119], v[176:191]\n"
        "s_add_u32 s32, s32, -20480\n"
        "buffer_load_dwordx4 v[52:55], v1, s[8:11], s32 offen offset:3072 sc1\n"
        "v_mfma_f32_32x32x16_bf16 v[192:207], a[172:175], v[116:119], v[192:207]\n"
        "s_waitcnt vmcnt(10)\n"
        "v_mfma_f32_32x32x16_bf16 v[144:159], a[64:67], v[120:123], v[144:159]\n"
        "s_add_u32 s31, s31, 4096\n"
        "buffer_load_dwordx4 v[56:59], v1, s[8:11], s31 offen offset:0 sc1\n"
        "v_mfma_f32_32x32x16_bf16 v[160:175], a[176:179], v[120:123], v[160:175]\n"
        "v_mfma_f32_32x32x16_bf16 v[176:191], a[64:67], v[124:127], v[176:191]\n"
        "s_add_u32 s32, s32, 4096\n"
        "buffer_load_dwordx4 v[60:63], v1, s[8:11], s32 offen offset:0 sc1\n"
        "v_mfma_f32_32x32x16_bf16 v[192:207], a[176:179], v[124:127], v[192:207]\n"
        "s_waitcnt vmcnt(10)\n"
        "v_mfma_f32_32x32x16_bf16 v[144:159], a[108:111], v[128:131], v[144:159]\n"
        "buffer_load_dwordx4 v[64:67], v1, s[8:11], s31 offen offset:1024 sc1\n"
        "v_mfma_f32_32x32x16_bf16 v[160:175], a[220:223], v[128:131], v[160:175]\n"
        "v_mfma_f32_32x32x16_bf16 v[176:191], a[108:111], v[132:135], v[176:191]\n"
        "buffer_load_dwordx4 v[68:71], v1, s[8:11], s32 offen offset:1024 sc1\n"
        "v_mfma_f32_32x32x16_bf16 v[192:207], a[220:223], v[132:135], v[192:207]\n"
        "s_waitcnt vmcnt(10)\n"
        "v_mfma_f32_32x32x16_bf16 v[144:159], a[84:87], v[136:139], v[144:159]\n"
        "s_add_u32 s31, s31, 4096\n"
        "buffer_load_dwordx4 v[72:75], v1, s[8:11], s31 offen offset:2048 sc1\n"
        "v_mfma_f32_32x32x16_bf16 v[160:175], a[196:199], v[136:139], v[160:175]\n"
        "v_mfma_f32_32x32x16_bf16 v[176:191], a[84:87], v[140:143], v[176:191]\n"
        "s_add_u32 s32, s32, 4096\n"
        "buffer_load_dwordx4 v[76:79], v1, s[8:11], s32 offen offset:2048 sc1\n"
        "v_mfma_f32_32x32x16_bf16 v[192:207], a[196:199], v[140:143], v[192:207]\n"
        "s_waitcnt vmcnt(10)\n"
        "v_mfma_f32_32x32x16_bf16 v[144:159], a[88:91], v[32:35], v[144:159]\n"
        "buffer_load_dwordx4 v[80:83], v1, s[8:11], s31 offen offset:3072 sc1\n"
        "v_mfma_f32_32x32x16_bf16 v[160:175], a[200:203], v[32:35], v[160:175]\n"
        "v_mfma_f32_32x32x16_bf16 v[176:191], a[88:91], v[36:39], v[176:191]\n"
        "buffer_load_dwordx4 v[84:87], v1, s[8:11], s32 offen offset:3072 sc1\n"
        "v_mfma_f32_32x32x16_bf16 v[192:207], a[200:203], v[36:39], v[192:207]\n"
        "s_waitcnt vmcnt(10)\n"
        "v_mfma_f32_32x32x16_bf16 v[144:159], a[92:95], v[40:43], v[144:159]\n"
        "s_add_u32 s31, s31, 4096\n"
        "buffer_load_dwordx4 v[88:91], v1, s[8:11], s31 offen offset:0 sc1\n"
        "v_mfma_f32_32x32x16_bf16 v[160:175], a[204:207], v[40:43], v[160:175]\n"
        "v_mfma_f32_32x32x16_bf16 v[176:191], a[92:95], v[44:47], v[176:191]\n"
        "s_add_u32 s32, s32, 4096\n"
        "buffer_load_dwordx4 v[92:95], v1, s[8:11], s32 offen offset:0 sc1\n"
        "v_mfma_f32_32x32x16_bf16 v[192:207], a[204:207], v[44:47], v[192:207]\n"
        "s_waitcnt vmcnt(10)\n"
        "v_mfma_f32_32x32x16_bf16 v[144:159], a[12:15], v[48:51], v[144:159]\n"
        "s_add_u32 s31, s31, 4096\n"
        "buffer_load_dwordx4 v[96:99], v1, s[8:11], s31 offen offset:1024 sc1\n"
        "v_mfma_f32_32x32x16_bf16 v[160:175], a[124:127], v[48:51], v[160:175]\n"
        "v_mfma_f32_32x32x16_bf16 v[176:191], a[12:15], v[52:55], v[176:191]\n"
        "s_add_u32 s32, s32, 4096\n"
        "buffer_load_dwordx4 v[100:103], v1, s[8:11], s32 offen offset:1024 sc1\n"
        "v_mfma_f32_32x32x16_bf16 v[192:207], a[124:127], v[52:55], v[192:207]\n"
        "s_waitcnt vmcnt(10)\n"
        "v_mfma_f32_32x32x16_bf16 v[144:159], a[16:19], v[56:59], v[144:159]\n"
        "buffer_load_dwordx4 v[104:107], v1, s[8:11], s31 offen offset:2048 sc1\n"
        "v_mfma_f32_32x32x16_bf16 v[160:175], a[128:131], v[56:59], v[160:175]\n"
        "v_mfma_f32_32x32x16_bf16 v[176:191], a[16:19], v[60:63], v[176:191]\n"
        "buffer_load_dwordx4 v[108:111], v1, s[8:11], s32 offen offset:2048 sc1\n"
        "v_mfma_f32_32x32x16_bf16 v[192:207], a[128:131], v[60:63], v[192:207]\n"
        "s_waitcnt vmcnt(10)\n"
        "v_mfma_f32_32x32x16_bf16 v[144:159], a[20:23], v[64:67], v[144:159]\n"
        "buffer_load_dwordx4 v[112:115], v1, s[8:11], s31 offen offset:3072 sc1\n"
        "v_mfma_f32_32x32x16_bf16 v[160:175], a[132:135], v[64:67], v[160:175]\n"
        "v_mfma_f32_32x32x16_bf16 v[176:191], a[20:23], v[68:71], v[176:191]\n"
        "buffer_load_dwordx4 v[116:119], v1, s[8:11], s32 offen offset:3072 sc1\n"
        "v_mfma_f32_32x32x16_bf16 v[192:207], a[132:135], v[68:71], v[192:207]\n"
        "s_waitcnt vmcnt(10)\n"
        "v_mfma_f32_32x32x16_bf16 v[144:159], a[40:43], v[72:75], v[144:159]\n"
        "s_add_u32 s31, s31, 8192\n"
        "buffer_load_dwordx4 v[120:123], v1, s[8:11], s31 offen offset:0 sc1\n"
        "v_mfma_f32_32x32x16_bf16 v[160:175], a[152:155], v[72:75], v[160:175]\n"
        "v_mfma_f32_32x32x16_bf16 v[176:191], a[40:43], v[76:79], v[176:191]\n"
        "s_add_u32 s32, s32, 8192\n"
        "buffer_load_dwordx4 v[124:127], v1, s[8:11], s32 offen offset:0 sc1\n"
        "v_mfma_f32_32x32x16_bf16 v[192:207], a[152:155], v[76:79], v[192:207]\n"
        "s_waitcnt vmcnt(10)\n"
        "v_mfma_f32_32x32x16_bf16 v[144:159], a[44:47], v[80:83], v[144:159]\n"
        "buffer_load_dwordx4 v[128:131], v1, s[8:11], s31 offen offset:1024 sc1\n"
        "v_mfma_f32_32x32x16_bf16 v[160:175], a[156:159], v[80:83], v[160:175]\n"
        "v_mfma_f32_32x32x16_bf16 v[176:191], a[44:47], v[84:87], v[176:191]\n"
        "buffer_load_dwordx4 v[132:135], v1, s[8:11], s32 offen offset:1024 sc1\n"
        "v_mfma_f32_32x32x16_bf16 v[192:207], a[156:159], v[84:87], v[192:207]\n"
        "s_waitcnt vmcnt(10)\n"
        "v_mfma_f32_32x32x16_bf16 v[144:159], a[48:51], v[88:91], v[144:159]\n"
        "buffer_load_dwordx4 v[136:139], v1, s[8:11], s31 offen offset:2048 sc1\n"
        "v_mfma_f32_32x32x16_bf16 v[160:175], a[160:163], v[88:91], v[160:175]\n"
        "v_mfma_f32_32x32x16_bf16 v[176:191], a[48:51], v[92:95], v[176:191]\n"
        "buffer_load_dwordx4 v[140:143], v1, s[8:11], s32 offen offset:2048 sc1\n"
        "v_mfma_f32_32x32x16_bf16 v[192:207], a[160:163], v[92:95], v[192:207]\n"
        "s_waitcnt vmcnt(10)\n"
        "v_mfma_f32_32x32x16_bf16 v[144:159], a[68:71], v[96:99], v[144:159]\n"
        "v_mfma_f32_32x32x16_bf16 v[160:175], a[180:183], v[96:99], v[160:175]\n"
        "v_mfma_f32_32x32x16_bf16 v[176:191], a[68:71], v[100:103], v[176:191]\n"
        "v_mfma_f32_32x32x16_bf16 v[192:207], a[180:183], v[100:103], v[192:207]\n"
        "s_waitcnt vmcnt(8)\n"
        "v_mfma_f32_32x32x16_bf16 v[192:207], a[184:187], v[108:111], v[192:207]\n"
        "s_waitcnt vmcnt(6)\n"
        "v_mfma_f32_32x32x16_bf16 v[192:207], a[188:191], v[116:119], v[192:207]\n"
        "s_waitcnt vmcnt(4)\n"
        "v_mfma_f32_32x32x16_bf16 v[192:207], a[208:211], v[124:127], v[192:207]\n"
        "s_waitcnt vmcnt(2)\n"
        "v_mfma_f32_32x32x16_bf16 v[192:207], a[212:215], v[132:135], v[192:207]\n"
        "s_waitcnt vmcnt(0)\n"
        "v_mfma_f32_32x32x16_bf16 v[192:207], a[216:219], v[140:143], v[192:207]\n"
        "v_mfma_f32_32x32x16_bf16 v[176:191], a[72:75], v[108:111], v[176:191]\n"
        "v_mfma_f32_32x32x16_bf16 v[176:191], a[76:79], v[116:119], v[176:191]\n"
        "v_mfma_f32_32x32x16_bf16 v[176:191], a[96:99], v[124:127], v[176:191]\n"
        "s_nop 7\n"
        "s_nop 0\n"
        "ds_write_b128 v27, v[192:195] offset:31744\n"
        "ds_write_b128 v27, v[196:199] offset:32768\n"
        "v_mfma_f32_32x32x16_bf16 v[176:191], a[100:103], v[132:135], v[176:191]\n"
        "ds_write_b128 v27, v[200:203] offset:33792\n"
        "ds_write_b128 v27, v[204:207] offset:34816\n"
        "v_mfma_f32_32x32x16_bf16 v[176:191], a[104:107], v[140:143], v[176:191]\n"
        "v_mfma_f32_32x32x16_bf16 v[160:175], a[184:187], v[104:107], v[160:175]\n"
        "v_mfma_f32_32x32x16_bf16 v[160:175], a[188:191], v[112:115], v[160:175]\n"
        "v_mfma_f32_32x32x16_bf16 v[160:175], a[208:211], v[120:123], v[160:175]\n"
        "s_nop 7\n"
        "s_nop 0\n"
        "ds_write_b128 v26, v[176:179] offset:27648\n"
        "ds_write_b128 v26, v[180:183] offset:28672\n"
        "v_mfma_f32_32x32x16_bf16 v[160:175], a[212:215], v[128:131], v[160:175]\n"
        "ds_write_b128 v26, v[184:187] offset:29696\n"
        "ds_write_b128 v26, v[188:191] offset:30720\n"
        "v_mfma_f32_32x32x16_bf16 v[160:175], a[216:219], v[136:139], v[160:175]\n"
        "v_mfma_f32_32x32x16_bf16 v[144:159], a[72:75], v[104:107], v[144:159]\n"
        "v_mfma_f32_32x32x16_bf16 v[144:159], a[76:79], v[112:115], v[144:159]\n"
        "v_mfma_f32_32x32x16_bf16 v[144:159], a[96:99], v[120:123], v[144:159]\n"
        "s_nop 7\n"
        "s_nop 0\n"
        "ds_write_b128 v25, v[160:163] offset:23552\n"
        "ds_write_b128 v25, v[164:167] offset:24576\n"
        "v_mfma_f32_32x32x16_bf16 v[144:159], a[100:103], v[128:131], v[144:159]\n"
        "ds_write_b128 v25, v[168:171] offset:25600\n"
        "ds_write_b128 v25, v[172:175] offset:26624\n"
        "v_mfma_f32_32x32x16_bf16 v[144:159], a[104:107], v[136:139], v[144:159]\n"
        "s_waitcnt lgkmcnt(0)\n"
        "s_barrier\n"
        "ds_read_b128 v[64:67], v24 offset:23552\n"
        "ds_read_b128 v[68:71], v24 offset:24576\n"
        "ds_read_b128 v[72:75], v24 offset:25600\n"
        "ds_read_b128 v[76:79], v24 offset:26624\n"
        "ds_read_b128 v[80:83], v24 offset:27648\n"
        "ds_read_b128 v[84:87], v24 offset:28672\n"
        "ds_read_b128 v[88:91], v24 offset:29696\n"
        "ds_read_b128 v[92:95], v24 offset:30720\n"
        "ds_read_b128 v[96:99], v24 offset:31744\n"
        "ds_read_b128 v[100:103], v24 offset:32768\n"
        "ds_read_b128 v[104:107], v24 offset:33792\n"
        "ds_read_b128 v[108:111], v24 offset:34816\n"
        "s_mul_i32 s35, s49, 256\n"
        "s_add_u32 s35, s35, s54\n"
        "s_add_u32 s35, s35, 208896\n"
        "s_mul_i32 s36, s48, 2048\n"
        "s_mul_i32 s39, s49, 128\n"
        "s_add_u32 s36, s36, s39\n"
        "s_add_u32 s36, s36, 657408\n"
        "s_add_u32 s34, s34, 147456\n"
        "s_waitcnt lgkmcnt(11)\n"
        "v_pk_add_f32 v[144:145], v[144:145], v[64:65]\n"
        "v_pk_add_f32 v[146:147], v[146:147], v[66:67]\n"
        "s_waitcnt lgkmcnt(10)\n"
        "v_pk_add_f32 v[148:149], v[148:149], v[68:69]\n"
        "v_pk_add_f32 v[150:151], v[150:151], v[70:71]\n"
        "s_waitcnt lgkmcnt(9)\n"
        "v_pk_add_f32 v[152:153], v[152:153], v[72:73]\n"
        "v_pk_add_f32 v[154:155], v[154:155], v[74:75]\n"
        "s_waitcnt lgkmcnt(8)\n"
        "v_pk_add_f32 v[156:157], v[156:157], v[76:77]\n"
        "v_pk_add_f32 v[158:159], v[158:159], v[78:79]\n"
        "s_waitcnt lgkmcnt(7)\n"
        "v_pk_add_f32 v[144:145], v[144:145], v[80:81]\n"
        "v_pk_add_f32 v[146:147], v[146:147], v[82:83]\n"
        "s_waitcnt lgkmcnt(6)\n"
        "v_pk_add_f32 v[148:149], v[148:149], v[84:85]\n"
        "v_pk_add_f32 v[150:151], v[150:151], v[86:87]\n"
        "s_waitcnt lgkmcnt(5)\n"
        "v_pk_add_f32 v[152:153], v[152:153], v[88:89]\n"
        "v_pk_add_f32 v[154:155], v[154:155], v[90:91]\n"
        "s_waitcnt lgkmcnt(4)\n"
        "v_pk_add_f32 v[156:157], v[156:157], v[92:93]\n"
        "v_pk_add_f32 v[158:159], v[158:159], v[94:95]\n"
        "s_waitcnt lgkmcnt(3)\n"
        "v_pk_add_f32 v[144:145], v[144:145], v[96:97]\n"
        "v_pk_add_f32 v[146:147], v[146:147], v[98:99]\n"
        "s_waitcnt lgkmcnt(2)\n"
        "v_pk_add_f32 v[148:149], v[148:149], v[100:101]\n"
        "v_pk_add_f32 v[150:151], v[150:151], v[102:103]\n"
        "s_waitcnt lgkmcnt(1)\n"
        "v_pk_add_f32 v[152:153], v[152:153], v[104:105]\n"
        "v_pk_add_f32 v[154:155], v[154:155], v[106:107]\n"
        "s_waitcnt lgkmcnt(0)\n"
        "v_pk_add_f32 v[156:157], v[156:157], v[108:109]\n"
        "v_pk_add_f32 v[158:159], v[158:159], v[110:111]\n"
        "s_cmp_lg_u32 s28, 0\n"
        "s_cbranch_scc1 .Lfinal\n"
        "v_cvt_pk_bf16_f32 v24, v144, v145\n"
        "v_cvt_pk_bf16_f32 v25, v146, v147\n"
        "v_cvt_pk_bf16_f32 v26, v148, v149\n"
        "v_cvt_pk_bf16_f32 v27, v150, v151\n"
        "buffer_store_dwordx4 v[24:27], v1, s[8:11], s34 offen offset:0 sc1\n"
        "v_cvt_pk_bf16_f32 v28, v152, v153\n"
        "v_cvt_pk_bf16_f32 v29, v154, v155\n"
        "v_cvt_pk_bf16_f32 v30, v156, v157\n"
        "v_cvt_pk_bf16_f32 v31, v158, v159\n"
        "buffer_store_dwordx4 v[28:31], v1, s[8:11], s34 offen offset:1024 sc1\n"
        "v_pk_add_f32 v[16:17], v[144:145], v[146:147]\n"
        "v_pk_mul_f32 v[18:19], v[144:145], v[144:145]\n"
        "v_pk_fma_f32 v[18:19], v[146:147], v[146:147], v[18:19]\n"
        "v_pk_add_f32 v[16:17], v[16:17], v[148:149]\n"
        "v_pk_fma_f32 v[18:19], v[148:149], v[148:149], v[18:19]\n"
        "v_pk_add_f32 v[16:17], v[16:17], v[150:151]\n"
        "v_pk_fma_f32 v[18:19], v[150:151], v[150:151], v[18:19]\n"
        "v_pk_add_f32 v[16:17], v[16:17], v[152:153]\n"
        "v_pk_fma_f32 v[18:19], v[152:153], v[152:153], v[18:19]\n"
        "v_pk_add_f32 v[16:17], v[16:17], v[154:155]\n"
        "v_pk_fma_f32 v[18:19], v[154:155], v[154:155], v[18:19]\n"
        "v_pk_add_f32 v[16:17], v[16:17], v[156:157]\n"
        "v_pk_fma_f32 v[18:19], v[156:157], v[156:157], v[18:19]\n"
        "v_pk_add_f32 v[16:17], v[16:17], v[158:159]\n"
        "v_pk_fma_f32 v[18:19], v[158:159], v[158:159], v[18:19]\n"
        "v_add_f32 v16, v16, v17\n"
        "v_add_f32 v17, v18, v19\n"
        "v_mov_b32 v18, v16\n"
        "v_mov_b32 v19, v17\n"
        "s_nop 0\n"
        "v_permlane32_swap_b32 v18, v16\n"
        "v_permlane32_swap_b32 v19, v17\n"
        "v_pk_add_f32 v[16:17], v[16:17], v[18:19]\n"
        "buffer_store_dwordx2 v[16:17], v8, s[8:11], s35 offen sc1\n"
        "v_mov_b32 v16, s96\n"
        "v_mov_b32 v17, s97\n"
        "v_mov_b32 v18, s96\n"
        "v_mov_b32 v19, s97\n"
        "s_waitcnt vmcnt(0)\n"
        "s_mov_b64 exec, 0xff\n"
        "buffer_store_dwordx4 v[16:19], v1, s[8:11], s36 offen sc1\n"
        "s_mov_b64 exec, -1\n"
        "s_branch .Lnext\n"
        ".Lffn2_idle:\n"
        "v_mov_b32 v16, s96\n"
        "v_mov_b32 v17, s97\n"
        "v_mov_b32 v18, s96\n"
        "v_mov_b32 v19, s97\n"
        "s_waitcnt vmcnt(0)\n"
        "s_mov_b64 exec, 0xff\n"
        "buffer_store_dwordx4 v[16:19], v1, s[8:11], s51 offen sc1\n"
        "s_mov_b64 exec, -1\n"
        "s_barrier\n"
        "s_cmp_lg_u32 s28, 0\n"
        "s_cbranch_scc1 .Lend\n"
        "s_mul_i32 s38, s24, 22528\n"
        "s_min_u32 s38, s38, 66560\n"
        "s_add_u32 m0, s38, 0\n"
        "s_mul_i32 s52, s26, 89088\n"
        "s_add_u32 s52, s52, s38\n"
        "s_add_u32 s52, s52, 1574912\n"
        "buffer_load_dwordx4 v1, s[8:11], s52 offen offset:0 sc1 lds\n"
        "buffer_load_dwordx4 v1, s[8:11], s52 offen offset:1024 sc1 lds\n"
        "buffer_load_dwordx4 v1, s[8:11], s52 offen offset:2048 sc1 lds\n"
        "buffer_load_dwordx4 v1, s[8:11], s52 offen offset:3072 sc1 lds\n"
        "s_add_u32 m0, m0, 4096\n"
        "s_add_u32 s52, s52, 4096\n"
        "buffer_load_dwordx4 v1, s[8:11], s52 offen offset:0 sc1 lds\n"
        "buffer_load_dwordx4 v1, s[8:11], s52 offen offset:1024 sc1 lds\n"
        "buffer_load_dwordx4 v1, s[8:11], s52 offen offset:2048 sc1 lds\n"
        "buffer_load_dwordx4 v1, s[8:11], s52 offen offset:3072 sc1 lds\n"
        "s_add_u32 m0, m0, 4096\n"
        "s_add_u32 s52, s52, 4096\n"
        "buffer_load_dwordx4 v1, s[8:11], s52 offen offset:0 sc1 lds\n"
        "buffer_load_dwordx4 v1, s[8:11], s52 offen offset:1024 sc1 lds\n"
        "buffer_load_dwordx4 v1, s[8:11], s52 offen offset:2048 sc1 lds\n"
        ".Lnext:\n"
        "s_mov_b32 s28, 1\n"
        "s_mov_b32 s29, 786432\n"
        "s_mul_i32 s53, s27, 212992\n"
        "s_mul_i32 s39, s28, 13631488\n"
        "s_add_u32 s53, s53, s39\n"
        "s_add_u32 s53, s53, 15452160\n"
        "s_mul_i32 s54, s48, 212992\n"
        "s_mul_i32 s39, s28, 13631488\n"
        "s_add_u32 s54, s54, s39\n"
        "s_add_u32 s54, s54, 15452160\n"
        "s_add_u32 s30, s53, -13451264\n"
        "s_mul_i32 s34, s26, 262144\n"
        "s_mul_i32 s39, s27, 4096\n"
        "s_add_u32 s34, s34, s39\n"
        "s_mul_i32 s39, s28, 4194304\n"
        "s_add_u32 s34, s34, s39\n"
        "s_add_u32 s34, s34, s47\n"
        "s_mul_i32 s35, s26, 8192\n"
        "s_mul_i32 s39, s24, 2048\n"
        "s_add_u32 s35, s35, s39\n"
        "s_mul_i32 s39, s25, 128\n"
        "s_add_u32 s35, s35, s39\n"
        "s_add_u32 s35, s35, s29\n"
        "s_add_u32 s35, s35, 2048\n"
        "s_mul_i32 s32, s27, 2048\n"
        "s_add_u32 s32, s32, 657408\n"
        "s_mov_b32 s44, 0x20000\n"
        "s_cmp_lt_u32 s26, 14\n"
        "s_cbranch_scc1 .Lbusy15\n"
        "s_sleep 80\n"
        ".Lbusy15:\n"
        "s_sleep 10\n"
        "buffer_load_dwordx2 v[16:17], v4, s[8:11], s32 offen sc1\n"
        "s_mul_i32 s38, s24, 22528\n"
        "s_min_u32 s38, s38, 66560\n"
        "s_add_u32 m0, s38, 11264\n"
        "s_mul_i32 s52, s26, 89088\n"
        "s_add_u32 s52, s52, s38\n"
        "s_add_u32 s52, s52, 1586176\n"
        "buffer_load_dwordx4 v1, s[8:11], s52 offen offset:0 sc1 lds\n"
        "buffer_load_dwordx4 v1, s[8:11], s52 offen offset:1024 sc1 lds\n"
        "buffer_load_dwordx4 v1, s[8:11], s52 offen offset:2048 sc1 lds\n"
        "buffer_load_dwordx4 v1, s[8:11], s52 offen offset:3072 sc1 lds\n"
        "s_add_u32 m0, m0, 4096\n"
        "s_add_u32 s52, s52, 4096\n"
        "buffer_load_dwordx4 v1, s[8:11], s52 offen offset:0 sc1 lds\n"
        "buffer_load_dwordx4 v1, s[8:11], s52 offen offset:1024 sc1 lds\n"
        "buffer_load_dwordx4 v1, s[8:11], s52 offen offset:2048 sc1 lds\n"
        "buffer_load_dwordx4 v1, s[8:11], s52 offen offset:3072 sc1 lds\n"
        "s_add_u32 m0, m0, 4096\n"
        "s_add_u32 s52, s52, 4096\n"
        "buffer_load_dwordx4 v1, s[8:11], s52 offen offset:0 sc1 lds\n"
        "buffer_load_dwordx4 v1, s[8:11], s52 offen offset:1024 sc1 lds\n"
        "buffer_load_dwordx4 v1, s[8:11], s52 offen offset:2048 sc1 lds\n"
        "s_waitcnt vmcnt(11)\n"
        "v_cmp_ne_u64 vcc, s[96:97], v[16:17]\n"
        "s_cbranch_vccz .Lpolled14\n"
        ".Lpoll13:\n"
        "buffer_load_dwordx2 v[16:17], v4, s[8:11], s32 offen sc1\n"
        "s_waitcnt vmcnt(0)\n"
        "v_cmp_ne_u64 vcc, s[96:97], v[16:17]\n"
        "s_cbranch_vccz .Lpolled14\n"
        "s_sub_u32 s44, s44, 1\n"
        "s_cbranch_scc0 .Lpoll13\n"
        ".Lpolled14:\n"
        "buffer_load_dwordx4 v[32:35], v1, s[8:11], s30 offen offset:0 sc1\n"
        "buffer_load_dwordx4 v[36:39], v1, s[8:11], s30 offen offset:1024 sc1\n"
        "buffer_load_dwordx2 v[144:145], v7, s[8:11], s30 offen offset:0 sc1\n"
        "buffer_load_dwordx2 v[146:147], v7, s[8:11], s30 offen offset:256 sc1\n"
        "buffer_load_dwordx2 v[148:149], v7, s[8:11], s30 offen offset:512 sc1\n"
        "buffer_load_dwordx2 v[150:151], v7, s[8:11], s30 offen offset:768 sc1\n"
        "buffer_load_dwordx2 v[152:153], v7, s[8:11], s30 offen offset:1024 sc1\n"
        "buffer_load_dwordx2 v[154:155], v7, s[8:11], s30 offen offset:1280 sc1\n"
        "buffer_load_dwordx2 v[156:157], v7, s[8:11], s30 offen offset:1536 sc1\n"
        "buffer_load_dwordx4 v[40:43], v1, s[8:11], s30 offen offset:2048 sc1\n"
        "buffer_load_dwordx4 v[44:47], v1, s[8:11], s30 offen offset:3072 sc1\n"
        "s_add_u32 s30, s30, 4096\n"
        "buffer_load_dwordx4 v[48:51], v1, s[8:11], s30 offen offset:0 sc1\n"
        "buffer_load_dwordx4 v[52:55], v1, s[8:11], s30 offen offset:1024 sc1\n"
        "buffer_load_dwordx4 v[56:59], v1, s[8:11], s30 offen offset:2048 sc1\n"
        "buffer_load_dwordx4 v[60:63], v1, s[8:11], s30 offen offset:3072 sc1\n"
        "s_add_u32 s30, s30, 4096\n"
        "buffer_load_dwordx4 v[64:67], v1, s[8:11], s30 offen offset:0 sc1\n"
        "buffer_load_dwordx4 v[68:71], v1, s[8:11], s30 offen offset:1024 sc1\n"
        "s_waitcnt vmcnt(17)\n"
        "s_barrier\n"
        "ds_read_b128 v[208:211], v1 offset:29696\n"
        "ds_read_b128 v[212:215], v1 offset:59392\n"
        "ds_read_b128 v[216:219], v1 offset:30720\n"
        "ds_read_b128 v[220:223], v1 offset:60416\n"
        "ds_read_b128 v[224:227], v1 offset:31744\n"
        "ds_read_b128 v[228:231], v1 offset:61440\n"
        "ds_read_b128 v[232:235], v1 offset:32768\n"
        "ds_read_b128 v[236:239], v1 offset:62464\n"
        "s_waitcnt vmcnt(16) lgkmcnt(7)\n"
        "v_mfma_f32_32x32x16_bf16 v[160:175], v[208:211], v[32:35], 0\n"
        "ds_read_b128 v[240:243], v1 offset:33792\n"
        "buffer_load_dwordx4 v[72:75], v1, s[8:11], s30 offen offset:2048 sc1\n"
        "s_waitcnt lgkmcnt(7)\n"
        "v_mfma_f32_32x32x16_bf16 v[176:191], v[32:35], v[212:215], 0\n"
        "ds_read_b128 v[244:247], v1 offset:63488\n"
        "s_waitcnt vmcnt(16) lgkmcnt(7)\n"
        "v_mfma_f32_32x32x16_bf16 v[160:175], v[216:219], v[36:39], v[160:175]\n"
        "ds_read_b128 v[248:251], v1 offset:34816\n"
        "buffer_load_dwordx4 v[76:79], v1, s[8:11], s30 offen offset:3072 sc1\n"
        "s_waitcnt lgkmcnt(7)\n"
        "v_mfma_f32_32x32x16_bf16 v[176:191], v[36:39], v[220:223], v[176:191]\n"
        "ds_read_b128 v[252:255], v1 offset:64512\n"
        "s_waitcnt vmcnt(9) lgkmcnt(7)\n"
        "v_mfma_f32_32x32x16_bf16 v[160:175], v[224:227], v[40:43], v[160:175]\n"
        "ds_read_b128 v[208:211], v1 offset:35840\n"
        "s_add_u32 s30, s30, 4096\n"
        "buffer_load_dwordx4 v[80:83], v1, s[8:11], s30 offen offset:0 sc1\n"
        "s_waitcnt lgkmcnt(7)\n"
        "v_mfma_f32_32x32x16_bf16 v[176:191], v[40:43], v[228:231], v[176:191]\n"
        "ds_read_b128 v[212:215], v2 offset:0\n"
        "s_waitcnt vmcnt(9) lgkmcnt(7)\n"
        "v_mfma_f32_32x32x16_bf16 v[160:175], v[232:235], v[44:47], v[160:175]\n"
        "ds_read_b128 v[216:219], v1 offset:36864\n"
        "buffer_load_dwordx4 v[84:87], v1, s[8:11], s30 offen offset:1024 sc1\n"
        "v_add_f32 v144, v144, v146\n"
        "v_add_f32 v145, v145, v147\n"
        "s_waitcnt lgkmcnt(7)\n"
        "v_mfma_f32_32x32x16_bf16 v[176:191], v[44:47], v[236:239], v[176:191]\n"
        "ds_read_b128 v[220:223], v2 offset:1024\n"
        "v_add_f32 v144, v144, v148\n"
        "v_add_f32 v145, v145, v149\n"
        "s_waitcnt vmcnt(9) lgkmcnt(7)\n"
        "v_mfma_f32_32x32x16_bf16 v[160:175], v[240:243], v[48:51], v[160:175]\n"
        "ds_read_b128 v[224:227], v1 offset:37888\n"
        "buffer_load_dwordx4 v[88:91], v1, s[8:11], s30 offen offset:2048 sc1\n"
        "v_add_f32 v144, v144, v150\n"
        "v_add_f32 v145, v145, v151\n"
        "s_waitcnt lgkmcnt(7)\n"
        "v_mfma_f32_32x32x16_bf16 v[176:191], v[48:51], v[244:247], v[176:191]\n"
        "ds_read_b128 v[228:231], v2 offset:2048\n"
        "v_add_f32 v144, v144, v152\n"
        "v_add_f32 v145, v145, v153\n"
        "s_waitcnt vmcnt(9) lgkmcnt(7)\n"
        "v_mfma_f32_32x32x16_bf16 v[160:175], v[248:251], v[52:55], v[160:175]\n"
        "ds_read_b128 v[232:235], v1 offset:38912\n"
        "buffer_load_dwordx4 v[92:95], v1, s[8:11], s30 offen offset:3072 sc1\n"
        "v_add_f32 v144, v144, v154\n"
        "v_add_f32 v145, v145, v155\n"
        "s_waitcnt lgkmcnt(7)\n"
        "v_mfma_f32_32x32x16_bf16 v[176:191], v[52:55], v[252:255], v[176:191]\n"
        "ds_read_b128 v[236:239], v2 offset:3072\n"
        "v_add_f32 v144, v144, v156\n"
        "v_add_f32 v145, v145, v157\n"
        "s_waitcnt vmcnt(9) lgkmcnt(7)\n"
        "v_mfma_f32_32x32x16_bf16 v[160:175], v[208:211], v[56:59], v[160:175]\n"
        "ds_read_b128 v[240:243], v1 offset:39936\n"
        "s_add_u32 s30, s30, 4096\n"
        "buffer_load_dwordx4 v[96:99], v1, s[8:11], s30 offen offset:0 sc1\n"
        "v_mov_b32 v16, v144\n"
        "v_mov_b32 v17, v145\n"
        "s_waitcnt lgkmcnt(7)\n"
        "v_mfma_f32_32x32x16_bf16 v[176:191], v[56:59], v[212:215], v[176:191]\n"
        "ds_read_b128 v[244:247], v2 offset:4096\n"
        "s_nop 0\n"
        "v_permlane32_swap_b32 v16, v144\n"
        "s_waitcnt vmcnt(9) lgkmcnt(7)\n"
        "v_mfma_f32_32x32x16_bf16 v[160:175], v[216:219], v[60:63], v[160:175]\n"
        "ds_read_b128 v[248:251], v1 offset:40960\n"
        "buffer_load_dwordx4 v[100:103], v1, s[8:11], s30 offen offset:1024 sc1\n"
        "v_permlane32_swap_b32 v17, v145\n"
        "v_add_f32 v144, v144, v16\n"
        "s_waitcnt lgkmcnt(7)\n"
        "v_mfma_f32_32x32x16_bf16 v[176:191], v[60:63], v[220:223], v[176:191]\n"
        "ds_read_b128 v[252:255], v2 offset:5120\n"
        "v_add_f32 v145, v145, v17\n"
        "v_mul_f32 v144, 0xbb124925, v144\n"
        "s_waitcnt vmcnt(9) lgkmcnt(7)\n"
        "v_mfma_f32_32x32x16_bf16 v[160:175], v[224:227], v[64:67], v[160:175]\n"
        "ds_read_b128 v[208:211], v1 offset:41984\n"
        "buffer_load_dwordx4 v[104:107], v1, s[8:11], s30 offen offset:2048 sc1\n"
        "v_mul_f32 v145, 0x3b124925, v145\n"
        "v_fma_f32 v145, -v144, v144, v145\n"
        "s_waitcnt lgkmcnt(7)\n"
        "v_mfma_f32_32x32x16_bf16 v[176:191], v[64:67], v[228:231], v[176:191]\n"
        "ds_read_b128 v[212:215], v2 offset:6144\n"
        "v_max_f32 v145, 0, v145\n"
        "v_add_f32 v145, 0x3727c5ac, v145\n"
        "s_waitcnt vmcnt(9) lgkmcnt(7)\n"
        "v_mfma_f32_32x32x16_bf16 v[160:175], v[232:235], v[68:71], v[160:175]\n"
        "ds_read_b128 v[216:219], v1 offset:43008\n"
        "buffer_load_dwordx4 v[108:111], v1, s[8:11], s30 offen offset:3072 sc1\n"
        "v_rsq_f32 v152, v145\n"
        "s_nop 0\n"
        "s_waitcnt lgkmcnt(7)\n"
        "v_mfma_f32_32x32x16_bf16 v[176:191], v[68:71], v[236:239], v[176:191]\n"
        "ds_read_b128 v[220:223], v2 offset:7168\n"
        "v_mul_f32 v154, v145, v152\n"
        "v_cvt_pk_bf16_f32 v149, v144, v154\n"
        "s_waitcnt vmcnt(9) lgkmcnt(7)\n"
        "v_mfma_f32_32x32x16_bf16 v[160:175], v[240:243], v[72:75], v[160:175]\n"
        "ds_read_b128 v[224:227], v1 offset:44032\n"
        "s_add_u32 s30, s30, 4096\n"
        "buffer_load_dwordx4 v[112:115], v1, s[8:11], s30 offen offset:0 sc1\n"
        "v_lshlrev_b32 v16, 16, v149\n"
        "v_and_b32 v17, 0xffff0000, v149\n"
        "s_waitcnt lgkmcnt(7)\n"
        "v_mfma_f32_32x32x16_bf16 v[176:191], v[72:75], v[244:247], v[176:191]\n"
        "ds_read_b128 v[228:231], v2 offset:8192\n"
        "v_sub_f32 v16, v144, v16\n"
        "v_sub_f32 v17, v154, v17\n"
        "s_waitcnt vmcnt(9) lgkmcnt(7)\n"
        "v_mfma_f32_32x32x16_bf16 v[160:175], v[248:251], v[76:79], v[160:175]\n"
        "ds_read_b128 v[232:235], v1 offset:45056\n"
        "buffer_load_dwordx4 v[116:119], v1, s[8:11], s30 offen offset:1024 sc1\n"
        "v_cvt_pk_bf16_f32 v16, v16, v17\n"
        "v_and_b32 v17, 0xffff, v149\n"
        "s_waitcnt lgkmcnt(7)\n"
        "v_mfma_f32_32x32x16_bf16 v[176:191], v[76:79], v[252:255], v[176:191]\n"
        "ds_read_b128 v[236:239], v2 offset:9216\n"
        "v_lshl_or_b32 v148, v16, 16, v17\n"
        "v_lshrrev_b32 v150, 16, v16\n"
        "s_waitcnt vmcnt(9) lgkmcnt(7)\n"
        "v_mfma_f32_32x32x16_bf16 v[160:175], v[208:211], v[80:83], v[160:175]\n"
        "ds_read_b128 v[240:243], v1 offset:46080\n"
        "buffer_load_dwordx4 v[120:123], v1, s[8:11], s30 offen offset:2048 sc1\n"
        "v_mov_b32 v151, 0\n"
        "ds_write_b32 v10, v152\n"
        "s_waitcnt lgkmcnt(8)\n"
        "v_mfma_f32_32x32x16_bf16 v[176:191], v[80:83], v[212:215], v[176:191]\n"
        "ds_read_b128 v[244:247], v2 offset:10240\n"
        "v_mul_f32 v156, 0x3e8b97f6, v152\n"
        "ds_read_b128 v[16:19], v11 offset:0\n"
        "ds_read_b128 v[20:23], v11 offset:32\n"
        "ds_read_b128 v[24:27], v11 offset:64\n"
        "ds_read_b128 v[28:31], v11 offset:96\n"
        "s_waitcnt vmcnt(9) lgkmcnt(12)\n"
        "v_mfma_f32_32x32x16_bf16 v[160:175], v[216:219], v[84:87], v[160:175]\n"
        "ds_read_b128 v[248:251], v1 offset:47104\n"
        "buffer_load_dwordx4 v[124:127], v1, s[8:11], s30 offen offset:3072 sc1\n"
        "s_waitcnt lgkmcnt(12)\n"
        "v_mfma_f32_32x32x16_bf16 v[176:191], v[84:87], v[220:223], v[176:191]\n"
        "ds_read_b128 v[252:255], v2 offset:11264\n"
        "s_waitcnt vmcnt(9) lgkmcnt(12)\n"
        "v_mfma_f32_32x32x16_bf16 v[160:175], v[224:227], v[88:91], v[160:175]\n"
        "ds_read_b128 v[208:211], v1 offset:48128\n"
        "s_add_u32 s30, s30, 4096\n"
        "buffer_load_dwordx4 v[128:131], v1, s[8:11], s30 offen offset:0 sc1\n"
        "s_waitcnt lgkmcnt(12)\n"
        "v_mfma_f32_32x32x16_bf16 v[176:191], v[88:91], v[228:231], v[176:191]\n"
        "ds_read_b128 v[212:215], v2 offset:12288\n"
        "s_waitcnt vmcnt(9) lgkmcnt(12)\n"
        "v_mfma_f32_32x32x16_bf16 v[160:175], v[232:235], v[92:95], v[160:175]\n"
        "ds_read_b128 v[216:219], v1 offset:49152\n"
        "buffer_load_dwordx4 v[132:135], v1, s[8:11], s30 offen offset:1024 sc1\n"
        "s_waitcnt lgkmcnt(12)\n"
        "v_mfma_f32_32x32x16_bf16 v[176:191], v[92:95], v[236:239], v[176:191]\n"
        "ds_read_b128 v[220:223], v2 offset:13312\n"
        "s_waitcnt vmcnt(9) lgkmcnt(12)\n"
        "v_mfma_f32_32x32x16_bf16 v[160:175], v[240:243], v[96:99], v[160:175]\n"
        "ds_read_b128 v[224:227], v1 offset:50176\n"
        "buffer_load_dwordx4 v[136:139], v1, s[8:11], s30 offen offset:2048 sc1\n"
        "s_waitcnt lgkmcnt(11)\n"
        "v_mfma_f32_32x32x16_bf16 v[176:191], v[96:99], v[244:247], v[176:191]\n"
        "ds_read_b128 v[228:231], v2 offset:14336\n"
        "s_waitcnt vmcnt(9) lgkmcnt(7)\n"
        "v_mfma_f32_32x32x16_bf16 v[160:175], v[248:251], v[100:103], v[160:175]\n"
        "ds_read_b128 v[232:235], v1 offset:51200\n"
        "buffer_load_dwordx4 v[140:143], v1, s[8:11], s30 offen offset:3072 sc1\n"
        "s_waitcnt lgkmcnt(7)\n"
        "v_mfma_f32_32x32x16_bf16 v[176:191], v[100:103], v[252:255], v[176:191]\n"
        "ds_read_b128 v[236:239], v2 offset:15360\n"
        "s_waitcnt vmcnt(9) lgkmcnt(7)\n"
        "v_mfma_f32_32x32x16_bf16 v[160:175], v[208:211], v[104:107], v[160:175]\n"
        "ds_read_b128 v[240:243], v1 offset:52224\n"
        "s_waitcnt lgkmcnt(7)\n"
        "v_mfma_f32_32x32x16_bf16 v[176:191], v[104:107], v[212:215], v[176:191]\n"
        "ds_read_b128 v[244:247], v2 offset:16384\n"
        "s_waitcnt vmcnt(8) lgkmcnt(7)\n"
        "v_mfma_f32_32x32x16_bf16 v[160:175], v[216:219], v[108:111], v[160:175]\n"
        "ds_read_b128 v[248:251], v1 offset:53248\n"
        "s_waitcnt lgkmcnt(7)\n"
        "v_mfma_f32_32x32x16_bf16 v[176:191], v[108:111], v[220:223], v[176:191]\n"
        "ds_read_b128 v[252:255], v2 offset:17408\n"
        "s_waitcnt vmcnt(7) lgkmcnt(7)\n"
        "v_mfma_f32_32x32x16_bf16 v[160:175], v[224:227], v[112:115], v[160:175]\n"
        "ds_read_b128 v[208:211], v1 offset:54272\n"
        "s_waitcnt lgkmcnt(7)\n"
        "v_mfma_f32_32x32x16_bf16 v[176:191], v[112:115], v[228:231], v[176:191]\n"
        "ds_read_b128 v[212:215], v2 offset:18432\n"
        "s_waitcnt vmcnt(6) lgkmcnt(7)\n"
        "v_mfma_f32_32x32x16_bf16 v[160:175], v[232:235], v[116:119], v[160:175]\n"
        "ds_read_b128 v[216:219], v1 offset:55296\n"
        "s_waitcnt lgkmcnt(7)\n"
        "v_mfma_f32_32x32x16_bf16 v[176:191], v[116:119], v[236:239], v[176:191]\n"
        "ds_read_b128 v[220:223], v2 offset:19456\n"
        "s_waitcnt vmcnt(5) lgkmcnt(7)\n"
        "v_mfma_f32_32x32x16_bf16 v[160:175], v[240:243], v[120:123], v[160:175]\n"
        "ds_read_b128 v[224:227], v1 offset:56320\n"
        "s_waitcnt lgkmcnt(7)\n"
        "v_mfma_f32_32x32x16_bf16 v[176:191], v[120:123], v[244:247], v[176:191]\n"
        "ds_read_b128 v[228:231], v2 offset:20480\n"
        "s_waitcnt vmcnt(4) lgkmcnt(7)\n"
        "v_mfma_f32_32x32x16_bf16 v[160:175], v[248:251], v[124:127], v[160:175]\n"
        "ds_read_b128 v[232:235], v1 offset:57344\n"
        "s_waitcnt lgkmcnt(7)\n"
        "v_mfma_f32_32x32x16_bf16 v[176:191], v[124:127], v[252:255], v[176:191]\n"
        "ds_read_b128 v[236:239], v2 offset:21504\n"
        "s_waitcnt vmcnt(3) lgkmcnt(7)\n"
        "v_mfma_f32_32x32x16_bf16 v[160:175], v[208:211], v[128:131], v[160:175]\n"
        "ds_read_b128 v[240:243], v1 offset:0\n"
        "s_waitcnt lgkmcnt(7)\n"
        "v_mfma_f32_32x32x16_bf16 v[176:191], v[128:131], v[212:215], v[176:191]\n"
        "ds_read_b128 v[244:247], v1 offset:1024\n"
        "s_waitcnt vmcnt(2) lgkmcnt(7)\n"
        "v_mfma_f32_32x32x16_bf16 v[160:175], v[216:219], v[132:135], v[160:175]\n"
        "ds_read_b128 v[248:251], v1 offset:2048\n"
        "s_waitcnt lgkmcnt(7)\n"
        "v_mfma_f32_32x32x16_bf16 v[176:191], v[132:135], v[220:223], v[176:191]\n"
        "ds_read_b128 v[252:255], v1 offset:3072\n"
        "s_waitcnt vmcnt(1) lgkmcnt(7)\n"
        "v_mfma_f32_32x32x16_bf16 v[160:175], v[224:227], v[136:139], v[160:175]\n"
        "ds_read_b128 v[208:211], v1 offset:4096\n"
        "s_waitcnt lgkmcnt(7)\n"
        "v_mfma_f32_32x32x16_bf16 v[176:191], v[136:139], v[228:231], v[176:191]\n"
        "ds_read_b128 v[212:215], v1 offset:5120\n"
        "s_waitcnt vmcnt(0) lgkmcnt(7)\n"
        "v_mfma_f32_32x32x16_bf16 v[160:175], v[232:235], v[140:143], v[160:175]\n"
        "ds_read_b128 v[216:219], v1 offset:6144\n"
        "s_waitcnt lgkmcnt(7)\n"
        "v_mfma_f32_32x32x16_bf16 v[176:191], v[140:143], v[236:239], v[176:191]\n"
        "ds_read_b128 v[220:223], v1 offset:7168\n"
        "v_mfma_f32_32x32x16_bf16 v[160:175], a[240:243], v[148:151], v[160:175]\n"
        "v_mfma_f32_32x32x16_bf16 v[176:191], v[148:151], a[244:247], v[176:191]\n"
        "s_nop 7\n"
        "s_nop 2\n"
        "v_pk_mul_f32 v[160:161], v[160:161], v[152:153] op_sel_hi:[1,0]\n"
        "v_pk_mul_f32 v[162:163], v[162:163], v[152:153] op_sel_hi:[1,0]\n"
        "v_pk_mul_f32 v[164:165], v[164:165], v[152:153] op_sel_hi:[1,0]\n"
        "v_pk_mul_f32 v[166:167], v[166:167], v[152:153] op_sel_hi:[1,0]\n"
        "v_cvt_pk_bf16_f32 v160, v160, v161\n"
        "v_cvt_pk_bf16_f32 v161, v162, v163\n"
        "v_cvt_pk_bf16_f32 v162, v164, v165\n"
        "v_cvt_pk_bf16_f32 v163, v166, v167\n"
        "buffer_store_dwordx4 v[160:163], v1, s[8:11], s34 offen offset:0 sc1\n"
        "v_pk_mul_f32 v[168:169], v[168:169], v[152:153] op_sel_hi:[1,0]\n"
        "v_pk_mul_f32 v[170:171], v[170:171], v[152:153] op_sel_hi:[1,0]\n"
        "v_pk_mul_f32 v[172:173], v[172:173], v[152:153] op_sel_hi:[1,0]\n"
        "v_pk_mul_f32 v[174:175], v[174:175], v[152:153] op_sel_hi:[1,0]\n"
        "v_cvt_pk_bf16_f32 v164, v168, v169\n"
        "v_cvt_pk_bf16_f32 v165, v170, v171\n"
        "v_cvt_pk_bf16_f32 v166, v172, v173\n"
        "v_cvt_pk_bf16_f32 v167, v174, v175\n"
        "v_mov_b32 v154, 0x3f80\n"
        "v_cndmask_b32 v166, v166, v154, s[40:41]\n"
        "v_cndmask_b32 v167, v167, 0, s[40:41]\n"
        "buffer_store_dwordx4 v[164:167], v1, s[8:11], s34 offen offset:1024 sc1\n"
        "v_mov_b32 v154, 0x3f803f80\n"
        "v_cndmask_b32 v154, 0, v154, s[42:43]\n"
        "v_pk_mul_f32 v[176:177], v[176:177], v[16:17]\n"
        "v_pk_mul_f32 v[178:179], v[178:179], v[18:19]\n"
        "v_pk_mul_f32 v[180:181], v[180:181], v[20:21]\n"
        "v_pk_mul_f32 v[182:183], v[182:183], v[22:23]\n"
        "v_cvt_pk_bf16_f32 v168, v176, v177\n"
        "v_cvt_pk_bf16_f32 v169, v178, v179\n"
        "v_cvt_pk_bf16_f32 v170, v180, v181\n"
        "v_cvt_pk_bf16_f32 v171, v182, v183\n"
        "v_cndmask_b32 v168, v168, v154, s[76:77]\n"
        "v_cndmask_b32 v169, v169, v154, s[76:77]\n"
        "v_cndmask_b32 v170, v170, v154, s[76:77]\n"
        "v_cndmask_b32 v171, v171, v154, s[76:77]\n"
        "buffer_store_dwordx4 v[168:171], v1, s[8:11], s34 offen offset:2048 sc1\n"
        "v_pk_mul_f32 v[184:185], v[184:185], v[24:25]\n"
        "v_pk_mul_f32 v[186:187], v[186:187], v[26:27]\n"
        "v_pk_mul_f32 v[188:189], v[188:189], v[28:29]\n"
        "v_pk_mul_f32 v[190:191], v[190:191], v[30:31]\n"
        "v_cvt_pk_bf16_f32 v172, v184, v185\n"
        "v_cvt_pk_bf16_f32 v173, v186, v187\n"
        "v_cvt_pk_bf16_f32 v174, v188, v189\n"
        "v_cvt_pk_bf16_f32 v175, v190, v191\n"
        "v_cndmask_b32 v172, v172, v154, s[76:77]\n"
        "v_cndmask_b32 v173, v173, v154, s[76:77]\n"
        "v_cndmask_b32 v174, v174, v154, s[76:77]\n"
        "v_cndmask_b32 v175, v175, v154, s[76:77]\n"
        "buffer_store_dwordx4 v[172:175], v1, s[8:11], s34 offen offset:3072 sc1\n"
        "s_waitcnt lgkmcnt(7)\n"
        "v_mfma_f32_32x32x16_bf16 v[192:207], v[240:243], v[32:35], 0\n"
        "ds_read_b128 v[224:227], v1 offset:8192\n"
        "s_waitcnt lgkmcnt(7)\n"
        "v_mfma_f32_32x32x16_bf16 v[192:207], v[244:247], v[36:39], v[192:207]\n"
        "ds_read_b128 v[228:231], v1 offset:9216\n"
        "s_waitcnt lgkmcnt(7)\n"
        "v_mfma_f32_32x32x16_bf16 v[192:207], v[248:251], v[40:43], v[192:207]\n"
        "ds_read_b128 v[232:235], v1 offset:10240\n"
        "s_waitcnt lgkmcnt(7)\n"
        "v_mfma_f32_32x32x16_bf16 v[192:207], v[252:255], v[44:47], v[192:207]\n"
        "ds_read_b128 v[236:239], v1 offset:11264\n"
        "s_waitcnt lgkmcnt(7)\n"
        "v_mfma_f32_32x32x16_bf16 v[192:207], v[208:211], v[48:51], v[192:207]\n"
        "ds_read_b128 v[240:243], v1 offset:12288\n"
        "s_waitcnt lgkmcnt(7)\n"
        "v_mfma_f32_32x32x16_bf16 v[192:207], v[212:215], v[52:55], v[192:207]\n"
        "ds_read_b128 v[244:247], v1 offset:13312\n"
        "s_waitcnt lgkmcnt(7)\n"
        "v_mfma_f32_32x32x16_bf16 v[192:207], v[216:219], v[56:59], v[192:207]\n"
        "ds_read_b128 v[248:251], v1 offset:14336\n"
        "s_waitcnt lgkmcnt(7)\n"
        "v_mfma_f32_32x32x16_bf16 v[192:207], v[220:223], v[60:63], v[192:207]\n"
        "ds_read_b128 v[252:255], v1 offset:15360\n"
        "s_waitcnt lgkmcnt(7)\n"
        "v_mfma_f32_32x32x16_bf16 v[192:207], v[224:227], v[64:67], v[192:207]\n"
        "ds_read_b128 v[208:211], v1 offset:16384\n"
        "s_waitcnt lgkmcnt(7)\n"
        "v_mfma_f32_32x32x16_bf16 v[192:207], v[228:231], v[68:71], v[192:207]\n"
        "ds_read_b128 v[212:215], v1 offset:17408\n"
        "s_waitcnt lgkmcnt(7)\n"
        "v_mfma_f32_32x32x16_bf16 v[192:207], v[232:235], v[72:75], v[192:207]\n"
        "ds_read_b128 v[216:219], v1 offset:18432\n"
        "s_waitcnt lgkmcnt(7)\n"
        "v_mfma_f32_32x32x16_bf16 v[192:207], v[236:239], v[76:79], v[192:207]\n"
        "ds_read_b128 v[220:223], v1 offset:19456\n"
        "s_waitcnt lgkmcnt(7)\n"
        "v_mfma_f32_32x32x16_bf16 v[192:207], v[240:243], v[80:83], v[192:207]\n"
        "ds_read_b128 v[224:227], v1 offset:20480\n"
        "s_waitcnt lgkmcnt(7)\n"
        "v_mfma_f32_32x32x16_bf16 v[192:207], v[244:247], v[84:87], v[192:207]\n"
        "ds_read_b128 v[228:231], v1 offset:21504\n"
        "s_waitcnt lgkmcnt(7)\n"
        "v_mfma_f32_32x32x16_bf16 v[192:207], v[248:251], v[88:91], v[192:207]\n"
        "ds_read_b128 v[232:235], v1 offset:22528\n"
        "s_waitcnt lgkmcnt(7)\n"
        "v_mfma_f32_32x32x16_bf16 v[192:207], v[252:255], v[92:95], v[192:207]\n"
        "ds_read_b128 v[236:239], v1 offset:23552\n"
        "s_waitcnt lgkmcnt(7)\n"
        "v_mfma_f32_32x32x16_bf16 v[192:207], v[208:211], v[96:99], v[192:207]\n"
        "ds_read_b128 v[240:243], v1 offset:24576\n"
        "s_waitcnt lgkmcnt(7)\n"
        "v_mfma_f32_32x32x16_bf16 v[192:207], v[212:215], v[100:103], v[192:207]\n"
        "ds_read_b128 v[244:247], v1 offset:25600\n"
        "s_waitcnt lgkmcnt(7)\n"
        "v_mfma_f32_32x32x16_bf16 v[192:207], v[216:219], v[104:107], v[192:207]\n"
        "ds_read_b128 v[248:251], v1 offset:26624\n"
        "s_waitcnt lgkmcnt(7)\n"
        "v_mfma_f32_32x32x16_bf16 v[192:207], v[220:223], v[108:111], v[192:207]\n"
        "ds_read_b128 v[252:255], v1 offset:27648\n"
        "s_waitcnt lgkmcnt(7)\n"
        "v_mfma_f32_32x32x16_bf16 v[192:207], v[224:227], v[112:115], v[192:207]\n"
        "s_waitcnt lgkmcnt(6)\n"
        "v_mfma_f32_32x32x16_bf16 v[192:207], v[228:231], v[116:119], v[192:207]\n"
        "s_waitcnt lgkmcnt(5)\n"
        "v_mfma_f32_32x32x16_bf16 v[192:207], v[232:235], v[120:123], v[192:207]\n"
        "s_waitcnt lgkmcnt(4)\n"
        "v_mfma_f32_32x32x16_bf16 v[192:207], v[236:239], v[124:127], v[192:207]\n"
        "s_waitcnt vmcnt(0)\n"
        "v_mov_b32 v16, s96\n"
        "v_mov_b32 v17, s97\n"
        "v_mov_b32 v18, s96\n"
        "v_mov_b32 v19, s97\n"
        "s_mov_b64 exec, 0xff\n"
        "buffer_store_dwordx4 v[16:19], v1, s[8:11], s35 offen sc1\n"
        "s_mov_b64 exec, -1\n"
        "s_waitcnt lgkmcnt(3)\n"
        "v_mfma_f32_32x32x16_bf16 v[192:207], v[240:243], v[128:131], v[192:207]\n"
        "s_waitcnt lgkmcnt(2)\n"
        "v_mfma_f32_32x32x16_bf16 v[192:207], v[244:247], v[132:135], v[192:207]\n"
        "s_waitcnt lgkmcnt(1)\n"
        "v_mfma_f32_32x32x16_bf16 v[192:207], v[248:251], v[136:139], v[192:207]\n"
        "s_waitcnt lgkmcnt(0)\n"
        "v_mfma_f32_32x32x16_bf16 v[192:207], v[252:255], v[140:143], v[192:207]\n"
        "v_mfma_f32_32x32x16_bf16 v[192:207], a[248:251], v[148:151], v[192:207]\n"
        "s_nop 7\n"
        "s_nop 3\n"
        "v_pk_mul_f32 v[192:193], v[192:193], v[156:157] op_sel_hi:[1,0]\n"
        "v_pk_mul_f32 v[194:195], v[194:195], v[156:157] op_sel_hi:[1,0]\n"
        "v_pk_mul_f32 v[196:197], v[196:197], v[156:157] op_sel_hi:[1,0]\n"
        "v_pk_mul_f32 v[198:199], v[198:199], v[156:157] op_sel_hi:[1,0]\n"
        "v_pk_mul_f32 v[200:201], v[200:201], v[156:157] op_sel_hi:[1,0]\n"
        "v_pk_mul_f32 v[202:203], v[202:203], v[156:157] op_sel_hi:[1,0]\n"
        "v_pk_mul_f32 v[204:205], v[204:205], v[156:157] op_sel_hi:[1,0]\n"
        "v_pk_mul_f32 v[206:207], v[206:207], v[156:157] op_sel_hi:[1,0]\n"
        "v_cvt_pk_bf16_f32 v192, v192, v193\n"
        "v_cvt_pk_bf16_f32 v193, v194, v195\n"
        "v_cvt_pk_bf16_f32 v194, v196, v197\n"
        "v_cvt_pk_bf16_f32 v195, v198, v199\n"
        "v_cvt_pk_bf16_f32 v196, v200, v201\n"
        "v_cvt_pk_bf16_f32 v197, v202, v203\n"
        "v_cvt_pk_bf16_f32 v198, v204, v205\n"
        "v_cvt_pk_bf16_f32 v199, v206, v207\n"
        "v_cndmask_b32 v198, v198, 0, s[40:41]\n"
        "v_cndmask_b32 v199, v199, 0, s[40:41]\n"
        "s_lshl_b32 s36, s24, 11\n"
        "v_add_u32 v0, s36, v3\n"
        "ds_write_b128 v0, v[192:195] offset:24576\n"
        "ds_write_b128 v0, v[196:199] offset:25600\n"
        "s_branch .Llayer\n"
        ".Lfinal:\n"
        "v_cvt_pk_bf16_f32 v24, v144, v145\n"
        "v_cvt_pk_bf16_f32 v25, v146, v147\n"
        "v_cvt_pk_bf16_f32 v26, v148, v149\n"
        "v_cvt_pk_bf16_f32 v27, v150, v151\n"
        "v_cvt_pk_bf16_f32 v28, v152, v153\n"
        "v_cvt_pk_bf16_f32 v29, v154, v155\n"
        "v_cvt_pk_bf16_f32 v30, v156, v157\n"
        "v_cvt_pk_bf16_f32 v31, v158, v159\n"
        "s_mul_i32 s30, s48, 28672\n"
        "s_mul_i32 s39, s49, 64\n"
        "s_add_u32 s30, s30, s39\n"
        "v_permlane32_swap_b32 v24, v26\n"
        "v_permlane32_swap_b32 v25, v27\n"
        "buffer_store_dwordx4 v[24:27], v12, s[12:15], s30 offen offset:0\n"
        "v_permlane32_swap_b32 v28, v30\n"
        "v_permlane32_swap_b32 v29, v31\n"
        "buffer_store_dwordx4 v[28:31], v12, s[12:15], s30 offen offset:32\n"
        "s_or_b32 s30, s55, s24\n"
        "s_cmp_lg_u32 s30, 0\n"
        "s_cbranch_scc1 .Lend\n"
        "v_mov_b32 v16, s96\n"
        "v_mov_b32 v17, s97\n"
        "s_mov_b64 exec, 1\n"
        "buffer_store_dwordx2 v[16:17], off, s[8:11], 0\n"
        "s_mov_b64 exec, -1\n"
        ".Lend:\n"
        "s_endpgm\n"
        ".Lupoll:\n"
        "s_mov_b32 s44, 0x20000\n"
        ".Lpoll16:\n"
        "buffer_load_dwordx2 v[16:17], v13, s[8:11], 0 offen sc1\n"
        "s_waitcnt vmcnt(0)\n"
        "v_cmp_ne_u64 vcc, s[96:97], v[16:17]\n"
        "s_cbranch_vccz .Lpolled17\n"
        "s_sub_u32 s44, s44, 1\n"
        "s_cbranch_scc0 .Lpoll16\n"
        ".Lpolled17:\n"
        "s_branch .Latt1\n"
        ".Lslow:\n"
        "s_lshl_b32 s51, s24, 12\n"
        "s_or_b32 s20, s51, s60\n"
        "v_cndmask_b32 v38, v38, 0, s[40:41]\n"
        "v_mov_b32 v242, 0xff800000\n"
        "v_cndmask_b32 v46, v46, 0, s[40:41]\n"
        "v_mov_b32 v243, 0xff800000\n"
        "v_cndmask_b32 v54, v54, 0, s[40:41]\n"
        "v_mov_b32 v244, 0xff800000\n"
        "v_cndmask_b32 v62, v62, 0, s[40:41]\n"
        "v_mov_b32 v245, 0xff800000\n"
        "s_waitcnt lgkmcnt(0)\n"
        "v_mov_b32 v96, 0\n"
        "v_mov_b32 v97, 0\n"
        "v_mov_b32 v98, 0\n"
        "v_mov_b32 v99, 0\n"
        "v_mov_b32 v100, 0\n"
        "v_mov_b32 v101, 0\n"
        "v_mov_b32 v102, 0\n"
        "v_mov_b32 v103, 0\n"
        "v_mov_b32 v104, 0\n"
        "v_mov_b32 v105, 0\n"
        "v_mov_b32 v106, 0\n"
        "v_mov_b32 v107, 0\n"
        "v_mov_b32 v108, 0\n"
        "v_mov_b32 v109, 0\n"
        "v_mov_b32 v110, 0\n"
        "v_mov_b32 v111, 0\n"
        "v_mov_b32 v112, 0\n"
        "v_mov_b32 v113, 0\n"
        "v_mov_b32 v114, 0\n"
        "v_mov_b32 v115, 0\n"
        "v_mov_b32 v116, 0\n"
        "v_mov_b32 v117, 0\n"
        "v_mov_b32 v118, 0\n"
        "v_mov_b32 v119, 0\n"
        "v_mov_b32 v120, 0\n"
        "v_mov_b32 v121, 0\n"
        "v_mov_b32 v122, 0\n"
        "v_mov_b32 v123, 0\n"
        "v_mov_b32 v124, 0\n"
        "v_mov_b32 v125, 0\n"
        "v_mov_b32 v126, 0\n"
        "v_mov_b32 v127, 0\n"
        "v_mov_b32 v128, 0\n"
        "v_mov_b32 v129, 0\n"
        "v_mov_b32 v130, 0\n"
        "v_mov_b32 v131, 0\n"
        "v_mov_b32 v132, 0\n"
        "v_mov_b32 v133, 0\n"
        "v_mov_b32 v134, 0\n"
        "v_mov_b32 v135, 0\n"
        "v_mov_b32 v136, 0\n"
        "v_mov_b32 v137, 0\n"
        "v_mov_b32 v138, 0\n"
        "v_mov_b32 v139, 0\n"
        "v_mov_b32 v140, 0\n"
        "v_mov_b32 v141, 0\n"
        "v_mov_b32 v142, 0\n"
        "v_mov_b32 v143, 0\n"
        "v_mov_b32 v144, 0\n"
        "v_mov_b32 v145, 0\n"
        "v_mov_b32 v146, 0\n"
        "v_mov_b32 v147, 0\n"
        "v_mov_b32 v148, 0\n"
        "v_mov_b32 v149, 0\n"
        "v_mov_b32 v150, 0\n"
        "v_mov_b32 v151, 0\n"
        "v_mov_b32 v152, 0\n"
        "v_mov_b32 v153, 0\n"
        "v_mov_b32 v154, 0\n"
        "v_mov_b32 v155, 0\n"
        "v_mov_b32 v156, 0\n"
        "v_mov_b32 v157, 0\n"
        "v_mov_b32 v158, 0\n"
        "v_mov_b32 v159, 0\n"
        "s_mov_b32 s50, 16\n"
        ".Lslow1:\n"
        "global_load_dwordx4 v[160:163], v1, s[20:21] offset:0 sc1\n"
        "global_load_dwordx4 v[164:167], v1, s[20:21] offset:1024 sc1\n"
        "global_load_dwordx4 v[168:171], v1, s[20:21] offset:2048 sc1\n"
        "global_load_dwordx4 v[172:175], v1, s[20:21] offset:3072 sc1\n"
        "s_add_u32 s20, s20, 0x4000\n"
        "s_waitcnt vmcnt(0)\n"
        "v_mfma_f32_32x32x16_bf16 v[64:79], v[160:163], v[32:35], 0\n"
        "v_mfma_f32_32x32x16_bf16 v[64:79], v[164:167], v[36:39], v[64:79]\n"
        "s_nop 7\n"
        "s_nop 3\n"
        "v_max3_f32 v16, v64, v65, v66\n"
        "v_max3_f32 v17, v67, v68, v69\n"
        "v_max3_f32 v16, v16, v70, v71\n"
        "v_max3_f32 v17, v17, v72, v73\n"
        "v_max3_f32 v16, v16, v74, v75\n"
        "v_max3_f32 v17, v17, v76, v77\n"
        "v_max3_f32 v16, v16, v78, v79\n"
        "v_max3_f32 v16, v16, v17, v242\n"
        "v_mov_b32 v17, v16\n"
        "s_nop 1\n"
        "v_permlane32_swap_b32 v16, v17\n"
        "v_max_f32 v16, v16, v17\n"
        "v_sub_f32 v18, v242, v16\n"
        "v_exp_f32 v18, v18\n"
        "v_mov_b32 v242, v16\n"
        "v_sub_f32 v64, v64, v16\n"
        "v_sub_f32 v65, v65, v16\n"
        "v_sub_f32 v66, v66, v16\n"
        "v_sub_f32 v67, v67, v16\n"
        "v_sub_f32 v68, v68, v16\n"
        "v_sub_f32 v69, v69, v16\n"
        "v_sub_f32 v70, v70, v16\n"
        "v_sub_f32 v71, v71, v16\n"
        "v_sub_f32 v72, v72, v16\n"
        "v_sub_f32 v73, v73, v16\n"
        "v_sub_f32 v74, v74, v16\n"
        "v_sub_f32 v75, v75, v16\n"
        "v_sub_f32 v76, v76, v16\n"
        "v_sub_f32 v77, v77, v16\n"
        "v_sub_f32 v78, v78, v16\n"
        "v_sub_f32 v79, v79, v16\n"
        "v_exp_f32 v64, v64\n"
        "v_exp_f32 v65, v65\n"
        "v_exp_f32 v66, v66\n"
        "v_exp_f32 v67, v67\n"
        "v_exp_f32 v68, v68\n"
        "v_exp_f32 v69, v69\n"
        "v_exp_f32 v70, v70\n"
        "v_exp_f32 v71, v71\n"
        "v_exp_f32 v72, v72\n"
        "v_exp_f32 v73, v73\n"
        "v_exp_f32 v74, v74\n"
        "v_exp_f32 v75, v75\n"
        "v_exp_f32 v76, v76\n"
        "v_exp_f32 v77, v77\n"
        "v_exp_f32 v78, v78\n"
        "v_exp_f32 v79, v79\n"
        "v_mul_f32 v96, v96, v18\n"
        "v_mul_f32 v97, v97, v18\n"
        "v_mul_f32 v98, v98, v18\n"
        "v_mul_f32 v99, v99, v18\n"
        "v_mul_f32 v100, v100, v18\n"
        "v_mul_f32 v101, v101, v18\n"
        "v_mul_f32 v102, v102, v18\n"
        "v_mul_f32 v103, v103, v18\n"
        "v_mul_f32 v104, v104, v18\n"
        "v_mul_f32 v105, v105, v18\n"
        "v_mul_f32 v106, v106, v18\n"
        "v_mul_f32 v107, v107, v18\n"
        "v_mul_f32 v108, v108, v18\n"
        "v_mul_f32 v109, v109, v18\n"
        "v_mul_f32 v110, v110, v18\n"
        "v_mul_f32 v111, v111, v18\n"
        "v_cvt_pk_bf16_f32 v224, v64, v65\n"
        "v_cvt_pk_bf16_f32 v225, v66, v67\n"
        "v_cvt_pk_bf16_f32 v226, v68, v69\n"
        "v_cvt_pk_bf16_f32 v227, v70, v71\n"
        "v_cvt_pk_bf16_f32 v228, v72, v73\n"
        "v_cvt_pk_bf16_f32 v229, v74, v75\n"
        "v_cvt_pk_bf16_f32 v230, v76, v77\n"
        "v_cvt_pk_bf16_f32 v231, v78, v79\n"
        "v_mfma_f32_32x32x16_bf16 v[96:111], v[168:171], v[224:227], v[96:111]\n"
        "s_nop 0\n"
        "v_mfma_f32_32x32x16_bf16 v[96:111], v[172:175], v[228:231], v[96:111]\n"
        "v_mfma_f32_32x32x16_bf16 v[64:79], v[160:163], v[40:43], 0\n"
        "v_mfma_f32_32x32x16_bf16 v[64:79], v[164:167], v[44:47], v[64:79]\n"
        "s_nop 7\n"
        "s_nop 3\n"
        "v_max3_f32 v16, v64, v65, v66\n"
        "v_max3_f32 v17, v67, v68, v69\n"
        "v_max3_f32 v16, v16, v70, v71\n"
        "v_max3_f32 v17, v17, v72, v73\n"
        "v_max3_f32 v16, v16, v74, v75\n"
        "v_max3_f32 v17, v17, v76, v77\n"
        "v_max3_f32 v16, v16, v78, v79\n"
        "v_max3_f32 v16, v16, v17, v243\n"
        "v_mov_b32 v17, v16\n"
        "s_nop 1\n"
        "v_permlane32_swap_b32 v16, v17\n"
        "v_max_f32 v16, v16, v17\n"
        "v_sub_f32 v18, v243, v16\n"
        "v_exp_f32 v18, v18\n"
        "v_mov_b32 v243, v16\n"
        "v_sub_f32 v64, v64, v16\n"
        "v_sub_f32 v65, v65, v16\n"
        "v_sub_f32 v66, v66, v16\n"
        "v_sub_f32 v67, v67, v16\n"
        "v_sub_f32 v68, v68, v16\n"
        "v_sub_f32 v69, v69, v16\n"
        "v_sub_f32 v70, v70, v16\n"
        "v_sub_f32 v71, v71, v16\n"
        "v_sub_f32 v72, v72, v16\n"
        "v_sub_f32 v73, v73, v16\n"
        "v_sub_f32 v74, v74, v16\n"
        "v_sub_f32 v75, v75, v16\n"
        "v_sub_f32 v76, v76, v16\n"
        "v_sub_f32 v77, v77, v16\n"
        "v_sub_f32 v78, v78, v16\n"
        "v_sub_f32 v79, v79, v16\n"
        "v_exp_f32 v64, v64\n"
        "v_exp_f32 v65, v65\n"
        "v_exp_f32 v66, v66\n"
        "v_exp_f32 v67, v67\n"
        "v_exp_f32 v68, v68\n"
        "v_exp_f32 v69, v69\n"
        "v_exp_f32 v70, v70\n"
        "v_exp_f32 v71, v71\n"
        "v_exp_f32 v72, v72\n"
        "v_exp_f32 v73, v73\n"
        "v_exp_f32 v74, v74\n"
        "v_exp_f32 v75, v75\n"
        "v_exp_f32 v76, v76\n"
        "v_exp_f32 v77, v77\n"
        "v_exp_f32 v78, v78\n"
        "v_exp_f32 v79, v79\n"
        "v_mul_f32 v112, v112, v18\n"
        "v_mul_f32 v113, v113, v18\n"
        "v_mul_f32 v114, v114, v18\n"
        "v_mul_f32 v115, v115, v18\n"
        "v_mul_f32 v116, v116, v18\n"
        "v_mul_f32 v117, v117, v18\n"
        "v_mul_f32 v118, v118, v18\n"
        "v_mul_f32 v119, v119, v18\n"
        "v_mul_f32 v120, v120, v18\n"
        "v_mul_f32 v121, v121, v18\n"
        "v_mul_f32 v122, v122, v18\n"
        "v_mul_f32 v123, v123, v18\n"
        "v_mul_f32 v124, v124, v18\n"
        "v_mul_f32 v125, v125, v18\n"
        "v_mul_f32 v126, v126, v18\n"
        "v_mul_f32 v127, v127, v18\n"
        "v_cvt_pk_bf16_f32 v224, v64, v65\n"
        "v_cvt_pk_bf16_f32 v225, v66, v67\n"
        "v_cvt_pk_bf16_f32 v226, v68, v69\n"
        "v_cvt_pk_bf16_f32 v227, v70, v71\n"
        "v_cvt_pk_bf16_f32 v228, v72, v73\n"
        "v_cvt_pk_bf16_f32 v229, v74, v75\n"
        "v_cvt_pk_bf16_f32 v230, v76, v77\n"
        "v_cvt_pk_bf16_f32 v231, v78, v79\n"
        "v_mfma_f32_32x32x16_bf16 v[112:127], v[168:171], v[224:227], v[112:127]\n"
        "s_nop 0\n"
        "v_mfma_f32_32x32x16_bf16 v[112:127], v[172:175], v[228:231], v[112:127]\n"
        "v_mfma_f32_32x32x16_bf16 v[64:79], v[160:163], v[48:51], 0\n"
        "v_mfma_f32_32x32x16_bf16 v[64:79], v[164:167], v[52:55], v[64:79]\n"
        "s_nop 7\n"
        "s_nop 3\n"
        "v_max3_f32 v16, v64, v65, v66\n"
        "v_max3_f32 v17, v67, v68, v69\n"
        "v_max3_f32 v16, v16, v70, v71\n"
        "v_max3_f32 v17, v17, v72, v73\n"
        "v_max3_f32 v16, v16, v74, v75\n"
        "v_max3_f32 v17, v17, v76, v77\n"
        "v_max3_f32 v16, v16, v78, v79\n"
        "v_max3_f32 v16, v16, v17, v244\n"
        "v_mov_b32 v17, v16\n"
        "s_nop 1\n"
        "v_permlane32_swap_b32 v16, v17\n"
        "v_max_f32 v16, v16, v17\n"
        "v_sub_f32 v18, v244, v16\n"
        "v_exp_f32 v18, v18\n"
        "v_mov_b32 v244, v16\n"
        "v_sub_f32 v64, v64, v16\n"
        "v_sub_f32 v65, v65, v16\n"
        "v_sub_f32 v66, v66, v16\n"
        "v_sub_f32 v67, v67, v16\n"
        "v_sub_f32 v68, v68, v16\n"
        "v_sub_f32 v69, v69, v16\n"
        "v_sub_f32 v70, v70, v16\n"
        "v_sub_f32 v71, v71, v16\n"
        "v_sub_f32 v72, v72, v16\n"
        "v_sub_f32 v73, v73, v16\n"
        "v_sub_f32 v74, v74, v16\n"
        "v_sub_f32 v75, v75, v16\n"
        "v_sub_f32 v76, v76, v16\n"
        "v_sub_f32 v77, v77, v16\n"
        "v_sub_f32 v78, v78, v16\n"
        "v_sub_f32 v79, v79, v16\n"
        "v_exp_f32 v64, v64\n"
        "v_exp_f32 v65, v65\n"
        "v_exp_f32 v66, v66\n"
        "v_exp_f32 v67, v67\n"
        "v_exp_f32 v68, v68\n"
        "v_exp_f32 v69, v69\n"
        "v_exp_f32 v70, v70\n"
        "v_exp_f32 v71, v71\n"
        "v_exp_f32 v72, v72\n"
        "v_exp_f32 v73, v73\n"
        "v_exp_f32 v74, v74\n"
        "v_exp_f32 v75, v75\n"
        "v_exp_f32 v76, v76\n"
        "v_exp_f32 v77, v77\n"
        "v_exp_f32 v78, v78\n"
        "v_exp_f32 v79, v79\n"
        "v_mul_f32 v128, v128, v18\n"
        "v_mul_f32 v129, v129, v18\n"
        "v_mul_f32 v130, v130, v18\n"
        "v_mul_f32 v131, v131, v18\n"
        "v_mul_f32 v132, v132, v18\n"
        "v_mul_f32 v133, v133, v18\n"
        "v_mul_f32 v134, v134, v18\n"
        "v_mul_f32 v135, v135, v18\n"
        "v_mul_f32 v136, v136, v18\n"
        "v_mul_f32 v137, v137, v18\n"
        "v_mul_f32 v138, v138, v18\n"
        "v_mul_f32 v139, v139, v18\n"
        "v_mul_f32 v140, v140, v18\n"
        "v_mul_f32 v141, v141, v18\n"
        "v_mul_f32 v142, v142, v18\n"
        "v_mul_f32 v143, v143, v18\n"
        "v_cvt_pk_bf16_f32 v224, v64, v65\n"
        "v_cvt_pk_bf16_f32 v225, v66, v67\n"
        "v_cvt_pk_bf16_f32 v226, v68, v69\n"
        "v_cvt_pk_bf16_f32 v227, v70, v71\n"
        "v_cvt_pk_bf16_f32 v228, v72, v73\n"
        "v_cvt_pk_bf16_f32 v229, v74, v75\n"
        "v_cvt_pk_bf16_f32 v230, v76, v77\n"
        "v_cvt_pk_bf16_f32 v231, v78, v79\n"
        "v_mfma_f32_32x32x16_bf16 v[128:143], v[168:171], v[224:227], v[128:143]\n"
        "s_nop 0\n"
        "v_mfma_f32_32x32x16_bf16 v[128:143], v[172:175], v[228:231], v[128:143]\n"
        "v_mfma_f32_32x32x16_bf16 v[64:79], v[160:163], v[56:59], 0\n"
        "v_mfma_f32_32x32x16_bf16 v[64:79], v[164:167], v[60:63], v[64:79]\n"
        "s_nop 7\n"
        "s_nop 3\n"
        "v_max3_f32 v16, v64, v65, v66\n"
        "v_max3_f32 v17, v67, v68, v69\n"
        "v_max3_f32 v16, v16, v70, v71\n"
        "v_max3_f32 v17, v17, v72, v73\n"
        "v_max3_f32 v16, v16, v74, v75\n"
        "v_max3_f32 v17, v17, v76, v77\n"
        "v_max3_f32 v16, v16, v78, v79\n"
        "v_max3_f32 v16, v16, v17, v245\n"
        "v_mov_b32 v17, v16\n"
        "s_nop 1\n"
        "v_permlane32_swap_b32 v16, v17\n"
        "v_max_f32 v16, v16, v17\n"
        "v_sub_f32 v18, v245, v16\n"
        "v_exp_f32 v18, v18\n"
        "v_mov_b32 v245, v16\n"
        "v_sub_f32 v64, v64, v16\n"
        "v_sub_f32 v65, v65, v16\n"
        "v_sub_f32 v66, v66, v16\n"
        "v_sub_f32 v67, v67, v16\n"
        "v_sub_f32 v68, v68, v16\n"
        "v_sub_f32 v69, v69, v16\n"
        "v_sub_f32 v70, v70, v16\n"
        "v_sub_f32 v71, v71, v16\n"
        "v_sub_f32 v72, v72, v16\n"
        "v_sub_f32 v73, v73, v16\n"
        "v_sub_f32 v74, v74, v16\n"
        "v_sub_f32 v75, v75, v16\n"
        "v_sub_f32 v76, v76, v16\n"
        "v_sub_f32 v77, v77, v16\n"
        "v_sub_f32 v78, v78, v16\n"
        "v_sub_f32 v79, v79, v16\n"
        "v_exp_f32 v64, v64\n"
        "v_exp_f32 v65, v65\n"
        "v_exp_f32 v66, v66\n"
        "v_exp_f32 v67, v67\n"
        "v_exp_f32 v68, v68\n"
        "v_exp_f32 v69, v69\n"
        "v_exp_f32 v70, v70\n"
        "v_exp_f32 v71, v71\n"
        "v_exp_f32 v72, v72\n"
        "v_exp_f32 v73, v73\n"
        "v_exp_f32 v74, v74\n"
        "v_exp_f32 v75, v75\n"
        "v_exp_f32 v76, v76\n"
        "v_exp_f32 v77, v77\n"
        "v_exp_f32 v78, v78\n"
        "v_exp_f32 v79, v79\n"
        "v_mul_f32 v144, v144, v18\n"
        "v_mul_f32 v145, v145, v18\n"
        "v_mul_f32 v146, v146, v18\n"
        "v_mul_f32 v147, v147, v18\n"
        "v_mul_f32 v148, v148, v18\n"
        "v_mul_f32 v149, v149, v18\n"
        "v_mul_f32 v150, v150, v18\n"
        "v_mul_f32 v151, v151, v18\n"
        "v_mul_f32 v152, v152, v18\n"
        "v_mul_f32 v153, v153, v18\n"
        "v_mul_f32 v154, v154, v18\n"
        "v_mul_f32 v155, v155, v18\n"
        "v_mul_f32 v156, v156, v18\n"
        "v_mul_f32 v157, v157, v18\n"
        "v_mul_f32 v158, v158, v18\n"
        "v_mul_f32 v159, v159, v18\n"
        "v_cvt_pk_bf16_f32 v224, v64, v65\n"
        "v_cvt_pk_bf16_f32 v225, v66, v67\n"
        "v_cvt_pk_bf16_f32 v226, v68, v69\n"
        "v_cvt_pk_bf16_f32 v227, v70, v71\n"
        "v_cvt_pk_bf16_f32 v228, v72, v73\n"
        "v_cvt_pk_bf16_f32 v229, v74, v75\n"
        "v_cvt_pk_bf16_f32 v230, v76, v77\n"
        "v_cvt_pk_bf16_f32 v231, v78, v79\n"
        "v_mfma_f32_32x32x16_bf16 v[144:159], v[168:171], v[224:227], v[144:159]\n"
        "s_nop 0\n"
        "v_mfma_f32_32x32x16_bf16 v[144:159], v[172:175], v[228:231], v[144:159]\n"
        "s_sub_u32 s50, s50, 1\n"
        "s_cmp_lg_u32 s50, 0\n"
        "s_nop 7\n"
        "s_nop 7\n"
        "s_cbranch_scc1 .Lslow1\n"
        "v_sub_f32 v242, 0, v242\n"
        "v_sub_f32 v243, 0, v243\n"
        "v_sub_f32 v244, 0, v244\n"
        "v_sub_f32 v245, 0, v245\n"
        "ds_write_b128 v21, v[112:115] offset:38912\n"
        "ds_write_b128 v21, v[116:119] offset:39936\n"
        "ds_write_b128 v21, v[120:123] offset:40960\n"
        "ds_write_b128 v21, v[124:127] offset:41984\n"
        "ds_write_b128 v22, v[128:131] offset:43008\n"
        "ds_write_b128 v22, v[132:135] offset:44032\n"
        "ds_write_b128 v22, v[136:139] offset:45056\n"
        "ds_write_b128 v22, v[140:143] offset:46080\n"
        "ds_write_b128 v23, v[144:147] offset:47104\n"
        "ds_write_b128 v23, v[148:151] offset:48128\n"
        "ds_write_b128 v23, v[152:155] offset:49152\n"
        "ds_write_b128 v23, v[156:159] offset:50176\n"
        "ds_write_b32 v25, v243\n"
        "ds_write_b32 v26, v244\n"
        "ds_write_b32 v27, v245\n"
        "v_mov_b32 v16, 155136\n"
        "ds_write_b32 v16, v16\n"
        "s_branch .Lcombine\n"
        ".Lrefactor:\n"
        "ds_read2_b32 v[30:31], v24 offset0:32 offset1:64\n"
        "ds_read_b32 v29, v24\n"
        "s_waitcnt lgkmcnt(0)\n"
        "v_min3_f32 v28, v242, v29, v30\n"
        "v_min_f32 v28, v28, v31\n"
        "v_sub_f32 v248, v28, v242\n"
        "v_sub_f32 v250, v28, v29\n"
        "v_sub_f32 v252, v28, v30\n"
        "v_sub_f32 v254, v28, v31\n"
        "v_exp_f32 v248, v248\n"
        "v_exp_f32 v250, v250\n"
        "v_exp_f32 v252, v252\n"
        "v_exp_f32 v254, v254\n"
        "s_branch .Lfactored\n"
        ".Lwq_ld:\n"
        "s_lshl_b32 s35, s24, 12\n"
        "s_add_u32 m0, s35, 114688\n"
        "s_nop 0\n"
        "buffer_load_dwordx4 v1, s[60:63], s33 offen lds\n"
        "s_add_u32 s33, s33, 2688\n"
        "s_add_u32 m0, s35, 115712\n"
        "s_nop 0\n"
        "buffer_load_dwordx4 v1, s[60:63], s33 offen lds\n"
        "s_add_u32 s33, s33, 2688\n"
        "s_add_u32 m0, s35, 116736\n"
        "s_nop 0\n"
        "buffer_load_dwordx4 v1, s[60:63], s33 offen lds\n"
        "s_add_u32 s33, s33, 2688\n"
        "s_add_u32 m0, s35, 117760\n"
        "s_nop 0\n"
        "buffer_load_dwordx4 v1, s[60:63], s33 offen lds\n"
        "s_add_u32 s33, s33, 2688\n"
        "s_branch .Lwq_ld_done\n"
        ".Lwq_tr:\n"
        "s_mov_b32 vcc_lo, 0xf0000000\n"
        "s_mov_b32 vcc_hi, 0xf0000000\n"
        "s_waitcnt vmcnt(6)\n"
        "s_barrier\n"
        "ds_read_b64_tr_b16 v[192:193], v243 offset:0\n"
        "ds_read_b64_tr_b16 v[194:195], v243 offset:8192\n"
        "ds_read_b64_tr_b16 v[196:197], v243 offset:56\n"
        "ds_read_b64_tr_b16 v[198:199], v243 offset:8248\n"
        "ds_read_b64_tr_b16 v[200:201], v243 offset:112\n"
        "ds_read_b64_tr_b16 v[202:203], v243 offset:8304\n"
        "ds_read_b64_tr_b16 v[204:205], v243 offset:168\n"
        "ds_read_b64_tr_b16 v[206:207], v243 offset:8360\n"
        "s_waitcnt lgkmcnt(6)\n"
        "v_cndmask_b32 v192, v192, 0, vcc\n"
        "v_cndmask_b32 v193, v193, 0, vcc\n"
        "v_cndmask_b32 v194, v194, 0, vcc\n"
        "v_cndmask_b32 v195, v195, 0, vcc\n"
        "buffer_store_dwordx4 v[192:195], v1, s[8:11], s94 offen sc1\n"
        "s_add_u32 s94, s94, 89088\n"
        "s_waitcnt lgkmcnt(4)\n"
        "v_cndmask_b32 v196, v196, 0, vcc\n"
        "v_cndmask_b32 v197, v197, 0, vcc\n"
        "v_cndmask_b32 v198, v198, 0, vcc\n"
        "v_cndmask_b32 v199, v199, 0, vcc\n"
        "buffer_store_dwordx4 v[196:199], v1, s[8:11], s94 offen sc1\n"
        "s_add_u32 s94, s94, 89088\n"
        "s_waitcnt lgkmcnt(2)\n"
        "v_cndmask_b32 v200, v200, 0, vcc\n"
        "v_cndmask_b32 v201, v201, 0, vcc\n"
        "v_cndmask_b32 v202, v202, 0, vcc\n"
        "v_cndmask_b32 v203, v203, 0, vcc\n"
        "buffer_store_dwordx4 v[200:203], v1, s[8:11], s94 offen sc1\n"
        "s_add_u32 s94, s94, 89088\n"
        "s_waitcnt lgkmcnt(0)\n"
        "v_cndmask_b32 v204, v204, 0, vcc\n"
        "v_cndmask_b32 v205, v205, 0, vcc\n"
        "v_cndmask_b32 v206, v206, 0, vcc\n"
        "v_cndmask_b32 v207, v207, 0, vcc\n"
        "buffer_store_dwordx4 v[204:207], v1, s[8:11], s94 offen sc1\n"
        "s_add_u32 s94, s94, 89088\n"
        "s_branch .Lwq_tr_done\n"
        ".Lwq_pub:\n"
        "s_waitcnt vmcnt(7)\n"
        "v_mov_b32 v192, s96\n"
        "v_mov_b32 v193, s97\n"
        "v_mov_b32 v194, s96\n"
        "v_mov_b32 v195, s97\n"
        "s_mov_b64 exec, 0xff\n"
        "buffer_store_dwordx4 v[192:195], v1, s[8:11], s95 offen sc1\n"
        "s_mov_b64 exec, -1\n"
        "s_branch .Lwq_pub_done\n"
        ".Lh2poll:\n"
        "s_mov_b32 s44, 0x20000\n"
        ".Lpoll18:\n"
        "buffer_load_dwordx2 v[16:17], v6, s[8:11], s30 offen sc1\n"
        "s_waitcnt vmcnt(0)\n"
        "v_cmp_ne_u64 vcc, s[96:97], v[16:17]\n"
        "s_cbranch_vccz .Lpolled19\n"
        "s_sub_u32 s44, s44, 1\n"
        "s_cbranch_scc0 .Lpoll18\n"
        ".Lpolled19:\n"
        "s_branch .Lh2ok\n"
        : "+{v0}"(tid)
        : [ka] "s"(__builtin_amdgcn_kernarg_segment_ptr()), [bid] "s"(blockIdx.x)
        : "v1", "v2", "v3", "v4", "v5", "v6", "v7", "v8", "v9", "v10", "v11", "v12", "v13", "v14", "v15", "v16", "v17", "v18", "v19", "v20", "v21", "v22", "v23", "v24", "v25", "v26", "v27", "v28", "v29", "v30", "v31", "v32", "v33", "v34", "v35", "v36", "v37", "v38", "v39", "v40", "v41", "v42", "v43", "v44", "v45", "v46", "v47", "v48", "v49", "v50", "v51", "v52", "v53", "v54", "v55", "v56", "v57", "v58", "v59", "v60", "v61", "v62", "v63", "v64", "v65", "v66", "v67", "v68", "v69", "v70", "v71", "v72", "v73", "v74", "v75", "v76", "v77", "v78", "v79", "v80", "v81", "v82", "v83", "v84", "v85", "v86", "v87", "v88", "v89", "v90", "v91", "v92", "v93", "v94", "v95", "v96", "v97", "v98", "v99", "v100", "v101", "v102", "v103", "v104", "v105", "v106", "v107", "v108", "v109", "v110", "v111", "v112", "v113", "v114", "v115", "v116", "v117", "v118", "v119", "v120", "v121", "v122", "v123", "v124", "v125", "v126", "v127", "v128", "v129", "v130", "v131", "v132", "v133", "v134", "v135", "v136", "v137", "v138", "v139", "v140", "v141", "v142", "v143", "v144", "v145", "v146", "v147", "v148", "v149", "v150", "v151", "v152", "v153", "v154", "v155", "v156", "v157", "v158", "v159", "v160", "v161", "v162", "v163", "v164", "v165", "v166", "v167", "v168", "v169", "v170", "v171", "v172", "v173", "v174", "v175", "v176", "v177", "v178", "v179", "v180", "v181", "v182", "v183", "v184", "v185", "v186", "v187", "v188", "v189", "v190", "v191", "v192", "v193", "v194", "v195", "v196", "v197", "v198", "v199", "v200", "v201", "v202", "v203", "v204", "v205", "v206", "v207", "v208", "v209", "v210", "v211", "v212", "v213", "v214", "v215", "v216", "v217", "v218", "v219", "v220", "v221", "v222", "v223", "v224", "v225", "v226", "v227", "v228", "v229", "v230", "v231", "v232", "v233", "v234", "v235", "v236", "v237", "v238", "v239", "v240", "v241", "v242", "v243", "v244", "v245", "v246", "v247", "v248", "v249", "v250", "v251", "v252", "v253", "v254", "v255", "a0", "a1", "a2", "a3", "a4", "a5", "a6", "a7", "a8", "a9", "a10", "a11", "a12", "a13", "a14", "a15", "a16", "a17", "a18", "a19", "a20", "a21", "a22", "a23", "a24", "a25", "a26", "a27", "a28", "a29", "a30", "a31", "a32", "a33", "a34", "a35", "a36", "a37", "a38", "a39", "a40", "a41", "a42", "a43", "a44", "a45", "a46", "a47", "a48", "a49", "a50", "a51", "a52", "a53", "a54", "a55", "a56", "a57", "a58", "a59", "a60", "a61", "a62", "a63", "a64", "a65", "a66", "a67", "a68", "a69", "a70", "a71", "a72", "a73", "a74", "a75", "a76", "a77", "a78", "a79", "a80", "a81", "a82", "a83", "a84", "a85", "a86", "a87", "a88", "a89", "a90", "a91", "a92", "a93", "a94", "a95", "a96", "a97", "a98", "a99", "a100", "a101", "a102", "a103", "a104", "a105", "a106", "a107", "a108", "a109", "a110", "a111", "a112", "a113", "a114", "a115", "a116", "a117", "a118", "a119", "a120", "a121", "a122", "a123", "a124", "a125", "a126", "a127", "a128", "a129", "a130", "a131", "a132", "a133", "a134", "a135", "a136", "a137", "a138", "a139", "a140", "a141", "a142", "a143", "a144", "a145", "a146", "a147", "a148", "a149", "a150", "a151", "a152", "a153", "a154", "a155", "a156", "a157", "a158", "a159", "a160", "a161", "a162", "a163", "a164", "a165", "a166", "a167", "a168", "a169", "a170", "a171", "a172", "a173", "a174", "a175", "a176", "a177", "a178", "a179", "a180", "a181", "a182", "a183", "a184", "a185", "a186", "a187", "a188", "a189", "a190", "a191", "a192", "a193", "a194", "a195", "a196", "a197", "a198", "a199", "a200", "a201", "a202", "a203", "a204", "a205", "a206", "a207", "a208", "a209", "a210", "a211", "a212", "a213", "a214", "a215", "a216", "a217", "a218", "a219", "a220", "a221", "a222", "a223", "a224", "a225", "a226", "a227", "a228", "a229", "a230", "a231", "a232", "a233", "a234", "a235", "a236", "a237", "a238", "a239", "a240", "a241", "a242", "a243", "a244", "a245", "a246", "a247", "a248", "a249", "a250", "a251", "a252", "a253", "a254", "a255", "s8", "s9", "s10", "s11", "s12", "s13", "s14", "s15", "s16", "s17", "s18", "s19", "s20", "s21", "s22", "s23", "s24", "s25", "s26", "s27", "s28", "s29", "s30", "s31", "s32", "s33", "s34", "s35", "s36", "s37", "s38", "s39", "s40", "s41", "s42", "s43", "s44", "s45", "s46", "s47", "s48", "s49", "s50", "s51", "s52", "s53", "s54", "s55", "s56", "s57", "s58", "s59", "s60", "s61", "s62", "s63", "s64", "s65", "s66", "s67", "s68", "s69", "s70", "s71", "s72", "s73", "s74", "s75", "s76", "s77", "s78", "s79", "s80", "s81", "s82", "s83", "s84", "s85", "s86", "s87", "s88", "s89", "s90", "s91", "s92", "s93", "s94", "s95", "s96", "s97", "s98", "s99", "s100", "s101", "vcc", "scc", "memory");
}

extern "C" void kernel_launch(void* const* d_in, const int* in_sizes, int n_in, void* d_out, int out_size, void* d_ws, size_t ws_size, hipStream_t stream) {
    (void)hipFuncSetAttribute((const void*)main_kernel, hipFuncAttributeMaxDynamicSharedMemorySize, LDS_BYTES);
    main_kernel<<<256, 256, LDS_BYTES, stream>>>((const u16*)d_in[0], (const u16*)d_in[1], (const u16*)d_in[2], (const u16*)d_in[4], (const u16*)d_in[5],
                                                 (const u16*)d_in[6], (const u16*)d_in[7], (u16*)d_out, (unsigned char*)d_ws);
}
```
